```python
import torch

def _softmax(x, axis=-1):
    return torch.softmax(x, dim=axis)
def _gelu(x):
    return torch.nn.functional.gelu(x, approximate="tanh")
def _silu(x):
    return torch.nn.functional.silu(x)
def _sigmoid(x):
    return torch.sigmoid(x)
def _layernorm(x, axis=-1, eps=1e-5):
    m = x.mean(dim=axis, keepdim=True)
    v = x.var(dim=axis, keepdim=True, unbiased=False)
    return (x - m) / torch.sqrt(v + eps)
def _scatter_add(tgt, idx, src, axis=0):
    return tgt.clone().index_add_(axis, idx, src)
def _segment_sum(src, seg, n):
    out = torch.zeros((n,) + src.shape[1:], dtype=src.dtype)
    return out.index_add_(0, seg, src)
def _pad(x, pw):
    flat = tuple(v for pair in reversed(pw) for v in pair)
    return torch.nn.functional.pad(x, flat)

kernel_name = 'gated_mix__sliding_local_dilated__grouped_kv_mqa__prompt'

def setup_inputs():

    _w = (lambda a: (lambda t: t.to(torch.bfloat16) if t.is_floating_point() else t)(torch.as_tensor(a)))
    def _A_setup():
        B, S, D = 1, 2048, 512
        return {
            "x": _w((lambda *s: torch.randn(*s, dtype=torch.bfloat16) * 0.05)(B, S, D)),
            "wqkv": _w((lambda *s: torch.randn(*s, dtype=torch.bfloat16) * 0.8)(D, 3 * D)),
            "wo": _w((lambda *s: torch.randn(*s, dtype=torch.bfloat16) * 0.05)(D, D)),
        }
    _A_in = _A_setup()

    def _B_setup():
        B, S, D, H = 1, 2048, 512, 32
        Dh = D // H
        return {
            "x": _w((lambda *s: torch.randn(*s, dtype=torch.bfloat16) * 0.05)(B, S, D)),
            "wq": _w((lambda *s: torch.randn(*s, dtype=torch.bfloat16) * 1.6)(D, H * Dh)),
            "wk": _w((lambda *s: torch.randn(*s, dtype=torch.bfloat16) * 1.6)(D, Dh)),
            "wv": _w((lambda *s: torch.randn(*s, dtype=torch.bfloat16) * 0.2)(D, Dh)),
            "wo": _w((lambda *s: torch.randn(*s, dtype=torch.bfloat16) * 0.2)(H * Dh, D)),
        }
    _B_in = _B_setup()

    return {
        "A_x": _A_in["x"],
        "A_wqkv": _A_in["wqkv"],
        "A_wo": _A_in["wo"],
        "B_x": _B_in["x"],
        "B_wq": _B_in["wq"],
        "B_wk": _B_in["wk"],
        "B_wv": _B_in["wv"],
        "B_wo": _B_in["wo"],
        "gate_w": _w((lambda *s: torch.randn(*s, dtype=torch.bfloat16) * 0.05)(512) * 16.0),
    }

def reference(A_x, A_wqkv, A_wo, B_x, B_wq, B_wk, B_wv, B_wo, gate_w):
    def _A_ref(x, wqkv, wo):
        B, S, D = x.shape
        H = 32; Dh = D // H
        W, dil = 8, 2
        qkv = (lambda x, *d: x.permute(*d))((x @ wqkv).reshape(B, S, 3, H, Dh), 2, 0, 3, 1, 4)
        q, k, v = qkv[0], qkv[1], qkv[2]
        scale = Dh ** -0.5
        pos = torch.arange(S)
        off = torch.arange(W) * dil
        idx = pos[:, None] - off[None, :]
        mask = idx >= 0
        idx_c = torch.where(mask, idx, 0).reshape(-1)
        kg = k[:, :, idx_c, :].reshape(B, H, S, W, Dh)
        vg = v[:, :, idx_c, :].reshape(B, H, S, W, Dh)
        logits = torch.einsum("bhsd,bhswd->bhsw", q, kg) * scale
        logits = torch.where(mask[None, None, :, :], logits, -1e9)
        attn = _softmax(logits, axis=-1)
        o = torch.einsum("bhsw,bhswd->bhsd", attn, vg)
        return (lambda x, *d: x.permute(*d))(o, 0, 2, 1, 3).reshape(B, S, D) @ wo

    def _B_ref(x, wq, wk, wv, wo):
        B, S, D = x.shape
        H = 32
        Dh = D // H
        q = (lambda x, *d: x.permute(*d))((x @ wq).reshape(B, S, H, Dh), 0, 2, 1, 3)
        k = x @ wk
        v = x @ wv
        scores = torch.einsum("bhqd,bkd->bhqk", q, k) * (Dh ** -0.5)
        p = _softmax(scores, axis=-1)
        o = torch.einsum("bhqk,bkd->bhqd", p, v)
        return (lambda x, *d: x.permute(*d))(o, 0, 2, 1, 3).reshape(B, S, H * Dh) @ wo

    x = A_x
    g = _sigmoid((x * gate_w).sum(-1, keepdims=True))
    a = _A_ref(x, A_wqkv, A_wo)
    b = _B_ref(x, B_wq, B_wk, B_wv, B_wo)
    return g * a + (1 - g) * b
```

```cpp
#include <hip/hip_runtime.h>
#include <hip/hip_bf16.h>

typedef __bf16 bf16x2 __attribute__((ext_vector_type(2)));
typedef __bf16 bf16x4 __attribute__((ext_vector_type(4)));
typedef __bf16 bf16x8 __attribute__((ext_vector_type(8)));
typedef __bf16 bf16x16 __attribute__((ext_vector_type(16)));
typedef short s16x4 __attribute__((ext_vector_type(4)));
typedef float f32x4 __attribute__((ext_vector_type(4)));
typedef float f32x8 __attribute__((ext_vector_type(8)));
typedef float f32x16 __attribute__((ext_vector_type(16)));
typedef unsigned u32x2 __attribute__((ext_vector_type(2)));
typedef unsigned u32x4 __attribute__((ext_vector_type(4)));
typedef unsigned char u8;

#define LOG2E 1.4426950408889634f

#define WS_Q   (0u)
#define WS_OA  (2u << 20)
#define WS_OB  (4u << 20)
#define WS_KV  (6u << 20)
#define WS_WOA ((6u << 20) + (256u << 10))
#define WS_WOB (WS_WOA + (512u << 10))
#define WS_G   (WS_WOB + (512u << 10))

__device__ __forceinline__ unsigned pk_bf16(float lo, float hi) {
  bf16x2 v; v[0] = (__bf16)lo; v[1] = (__bf16)hi;
  return __builtin_bit_cast(unsigned, v);
}
__device__ __forceinline__ float dot2bf(unsigned a, unsigned b, float c) {
  return __builtin_amdgcn_fdot2_f32_bf16(__builtin_bit_cast(bf16x2, a), __builtin_bit_cast(bf16x2, b), c, false);
}
__device__ __forceinline__ u32x4 mkrsrc(const void* p, unsigned bytes) {
  unsigned long long a = (unsigned long long)p;
  u32x4 r;
  r[0] = __builtin_amdgcn_readfirstlane((unsigned)a);
  r[1] = __builtin_amdgcn_readfirstlane((unsigned)(a >> 32) & 0xffffu);
  r[2] = bytes; r[3] = 0x00020000u;
  return r;
}
__device__ __forceinline__ void dma16(u32x4 rsrc, unsigned voff, unsigned soff, unsigned ldsdst) {
  unsigned keep;
  asm volatile("s_mov_b32 %0, m0\n\ts_mov_b32 m0, %4\n\ts_nop 2\n\t"
               "buffer_load_dwordx4 %1, %2, %3 offen lds\n\ts_mov_b32 m0, %0"
               : "=&s"(keep) : "v"(voff), "s"(rsrc), "s"(soff), "s"(ldsdst) : "memory");
}
__device__ __forceinline__ bf16x4 tr_read(const u8* p) {
  return __builtin_bit_cast(bf16x4, __builtin_amdgcn_ds_read_tr16_b64_v4i16(
      (__attribute__((address_space(3))) s16x4*)p));
}
__device__ __forceinline__ bf16x8 cat8(bf16x4 a, bf16x4 b) {
  return __builtin_shufflevector(a, b, 0, 1, 2, 3, 4, 5, 6, 7);
}
#define RAW_BARRIER() do { asm volatile("s_waitcnt lgkmcnt(0)\n\ts_barrier" ::: "memory"); } while (0)

#define K1_STAGE 36864
#define K1_NS 4
#define K1_X 0
#define K1_W 16384
#define K1_HX 32768
#define K1_WKV 34816

__global__ void __launch_bounds__(512, 2)
k1_proj(const __bf16* __restrict__ x, const __bf16* __restrict__ wqkv, const __bf16* __restrict__ woA,
        const __bf16* __restrict__ wq, const __bf16* __restrict__ wk, const __bf16* __restrict__ wv,
        const __bf16* __restrict__ woB, const __bf16* __restrict__ gw, u8* __restrict__ ws) {
  __shared__ __attribute__((aligned(1024))) u8 lds[K1_STAGE * K1_NS];
  const int tid = threadIdx.x, lane = tid & 63;
  const int wid = __builtin_amdgcn_readfirstlane(tid >> 6);
  const int bid = blockIdx.x, xcd = bid & 7, bi = bid >> 3;
  const int tb = (xcd & 1) * 8 + (bi & 7);
  const int hp = (xcd >> 1) * 4 + (bi >> 3);
  const int t0 = tb * 128;
  const unsigned ldsbase = (unsigned)(unsigned long long)(__attribute__((address_space(3))) u8*)lds;
  const int r32 = lane & 31, h = lane >> 5, r16 = lane & 15, g4 = lane >> 4;

  f32x16 acc[2][2];
  f32x4 hacc = {0.f, 0.f, 0.f, 0.f}, kvacc = {0.f, 0.f, 0.f, 0.f};
#pragma unroll
  for (int a = 0; a < 2; ++a)
#pragma unroll
    for (int b = 0; b < 2; ++b)
#pragma unroll
      for (int i = 0; i < 16; ++i) acc[a][b][i] = 0.f;

  if (wid >= 4) {
    const int lw = wid - 4;
    const u32x4 rx = mkrsrc(x, 2048u * 1024u);
    const __bf16* wsrc = (lw == 3) ? (wq + hp * 32) : (wqkv + lw * 512 + hp * 32);
    const unsigned wstride = (lw == 3) ? 1024u : 3072u;
    const u32x4 rw = mkrsrc(wsrc, 512u * wstride - (unsigned)((lw == 3) ? hp * 64 : (lw * 1024 + hp * 64)));
    const unsigned voffW = (unsigned)(lane >> 2) * wstride + (unsigned)(lane & 3) * 16u;
    const int xr = lane >> 3;
    unsigned voffX[4];
#pragma unroll
    for (int i = 0; i < 4; ++i) {
      int row = lw * 32 + i * 8 + xr;
      voffX[i] = (unsigned)(t0 + row) * 1024u + (unsigned)(((lane & 7) ^ ((row >> 1) & 7)) * 16);
    }
    u32x4 re; unsigned voffE, estep, edst;
    if (lw < 2) {
      int row = lw * 8 + xr;
      int tok = t0 - 16 + row; tok = tok < 0 ? 0 : tok;
      re = rx; voffE = (unsigned)tok * 1024u + (unsigned)(((lane & 7) ^ ((row >> 1) & 7)) * 16);
      estep = 128u; edst = K1_HX + lw * 1024;
    } else {
      re = mkrsrc((hp & 1) ? wv : wk, 512u * 32u);
      voffE = (unsigned)((lw - 2) * 1024 + lane * 16);
      estep = 2048u; edst = K1_WKV + (lw - 2) * 1024;
    }
    auto issue = [&](int s) {
      unsigned base = ldsbase + (unsigned)(s & (K1_NS - 1)) * K1_STAGE;
#pragma unroll
      for (int i = 0; i < 4; ++i) {
        dma16(rx, voffX[i], (unsigned)s * 128u, base + K1_X + lw * 4096 + i * 1024);
        dma16(rw, voffW, (unsigned)(s * 64 + i * 16) * wstride, base + K1_W + lw * 4096 + i * 1024);
      }
      dma16(re, voffE, (unsigned)s * estep, base + edst);
    };
    issue(0); issue(1); issue(2);
    const int kvrow = (hp >> 1) * 16 + r16;
    for (int s = 0; s < 8; ++s) {
      if (s <= 5) asm volatile("s_waitcnt vmcnt(18)" ::: "memory");
      else if (s == 6) asm volatile("s_waitcnt vmcnt(9)" ::: "memory");
      else asm volatile("s_waitcnt vmcnt(0)" ::: "memory");
      RAW_BARRIER();
      if (s + 3 < 8) issue(s + 3);
      const u8* st = lds + (s & (K1_NS - 1)) * K1_STAGE;
#pragma unroll
      for (int k2 = 0; k2 < 2; ++k2) {
        const u8* wp = st + K1_W + (1 + (lw >> 1)) * 4096 + (k2 * 32 + g4 * 8 + ((lane & 15) >> 2)) * 64 + (lw & 1) * 32 + (lane & 3) * 8;
        bf16x8 wf = cat8(tr_read(wp), tr_read(wp + 256));
        bf16x8 xf = *(const bf16x8*)(st + K1_HX + r16 * 128 + (((k2 * 4 + g4) ^ ((r16 >> 1) & 7)) * 16));
        hacc = __builtin_amdgcn_mfma_f32_16x16x32_bf16(wf, xf, hacc, 0, 0, 0);
        if (lw == 3) {
          const u8* kp = st + K1_WKV + (k2 * 32 + g4 * 8 + ((lane & 15) >> 2)) * 32 + (lane & 3) * 8;
          bf16x8 kf = cat8(tr_read(kp), tr_read(kp + 128));
          bf16x8 xg = *(const bf16x8*)(st + K1_X + kvrow * 128 + (((k2 * 4 + g4) ^ ((kvrow >> 1) & 7)) * 16));
          if (hp & 1) kvacc = __builtin_amdgcn_mfma_f32_16x16x32_bf16(xg, kf, kvacc, 0, 0, 0);
          else        kvacc = __builtin_amdgcn_mfma_f32_16x16x32_bf16(kf, xg, kvacc, 0, 0, 0);
        }
      }
    }
  } else {
    {
      u32x4 gv = *(const u32x4*)(gw + lane * 8);
#pragma unroll
      for (int i = 0; i < 2; ++i) {
        int tok = t0 + hp * 8 + wid * 2 + i;
        u32x4 xv = *(const u32x4*)(x + (size_t)tok * 512 + lane * 8);
        float z = 0.f;
#pragma unroll
        for (int j = 0; j < 4; ++j) z = dot2bf(xv[j], gv[j], z);
#pragma unroll
        for (int m = 32; m >= 1; m >>= 1) z += __shfl_xor(z, m, 64);
        if (lane == 0) ((float*)(ws + WS_G))[tok] = 1.f / (1.f + __expf(-z));
      }
    }
    {
      int f = bid * 4 + wid;
      const __bf16* wo = (f & 512) ? woB : woA;
      int fs = (f & 511) >> 4, nt = f & 15;
      int n = nt * 32 + 16 * ((r32 >> 2) & 1) + (r32 & 3) + 4 * (r32 >> 3);
      const unsigned short* src = (const unsigned short*)wo + (size_t)(fs * 16 + 4 * h) * 512 + n;
      u32x4 o;
#pragma unroll
      for (int j = 0; j < 4; ++j) {
        int k0 = (2 * j & 3) + 8 * (j >> 1);
        o[j] = (unsigned)src[k0 * 512] | ((unsigned)src[(k0 + 1) * 512] << 16);
      }
      *(u32x4*)(ws + WS_WOA + (size_t)f * 1024 + lane * 16) = o;
    }
    const int np = wid & 1, th = wid >> 1;
    for (int s = 0; s < 8; ++s) {
      RAW_BARRIER();
      const u8* st = lds + (s & (K1_NS - 1)) * K1_STAGE;
#pragma unroll
      for (int ks = 0; ks < 4; ++ks) {
        bf16x8 wf[2], xf[2];
#pragma unroll
        for (int a = 0; a < 2; ++a) {
          const u8* wp = st + K1_W + (np * 2 + a) * 4096 + (ks * 16 + h * 8 + ((lane & 15) >> 2)) * 64 + ((lane >> 4) & 1) * 32 + (lane & 3) * 8;
          wf[a] = cat8(tr_read(wp), tr_read(wp + 256));
          int row = th * 64 + a * 32 + r32;
          xf[a] = *(const bf16x8*)(st + K1_X + row * 128 + (((ks * 2 + h) ^ ((row >> 1) & 7)) * 16));
        }
#pragma unroll
        for (int a = 0; a < 2; ++a)
#pragma unroll
          for (int b = 0; b < 2; ++b)
            acc[a][b] = __builtin_amdgcn_mfma_f32_32x32x16_bf16(wf[a], xf[b], acc[a][b], 0, 0, 0);
      }
    }
  }
  RAW_BARRIER();
#define EP(which, hd, hh) (((which) * 4 + (hd) * 2 + (hh)) * 2304)
  if (wid < 4) {
    const int np = wid & 1, th = wid >> 1;
#pragma unroll
    for (int a = 0; a < 2; ++a)
#pragma unroll
      for (int b = 0; b < 2; ++b)
#pragma unroll
        for (int hd = 0; hd < 2; ++hd) {
          const int seg = np * 2 + a;
          const int row = th * 64 + b * 32 + r32;
          u32x4 o;
          if (a == 1 && np == 1) {
            const float sc = 0.25f * LOG2E;
#pragma unroll
            for (int j = 0; j < 4; ++j) o[j] = pk_bf16(acc[a][b][hd * 8 + 2 * j] * sc, acc[a][b][hd * 8 + 2 * j + 1] * sc);
            int tt = tb * 4 + th * 2 + b;
            *(u32x4*)(ws + WS_Q + ((size_t)(tt * 32 + hp * 2 + hd) * 64 + lane) * 16) = o;
          } else {
#pragma unroll
            for (int j = 0; j < 4; ++j) o[j] = pk_bf16(acc[a][b][hd * 8 + 2 * j], acc[a][b][hd * 8 + 2 * j + 1]);
            *(u32x4*)(lds + EP(seg, hd, h) + (row + 16) * 16) = o;
          }
        }
  } else {
    const int lw = wid - 4;
    u32x2 o; o[0] = pk_bf16(hacc[0], hacc[1]); o[1] = pk_bf16(hacc[2], hacc[3]);
    *(u32x2*)(lds + EP(1 + (lw >> 1), lw & 1, g4 & 1) + r16 * 16 + (g4 >> 1) * 8) = o;
    if (lw == 3) {
      u32x2 kv; kv[0] = pk_bf16(kvacc[0], kvacc[1]); kv[1] = pk_bf16(kvacc[2], kvacc[3]);
      int blk = tb * 8 + (hp >> 1);
      int kt = blk >> 1, sp = blk & 1;
      u8* dst = ws + WS_KV + (size_t)kt * 2048;
      if (hp & 1) dst += 1024 + sp * 512 + ((g4 >> 1) * 16 + r16) * 16 + (g4 & 1) * 8;
      else        dst += ((g4 & 1) * 32 + sp * 16 + r16) * 16 + (g4 >> 1) * 8;
      *(u32x2*)dst = kv;
    }
  }
  RAW_BARRIER();
  {
    const int tt = wid & 3, hd = wid >> 2;
    const int row = tt * 32 + r32 + 16;
    const int tok = t0 + tt * 32 + r32;
    u32x4 q = *(const u32x4*)(lds + EP(0, hd, h) + row * 16);
    float s[8];
#pragma unroll
    for (int w = 0; w < 8; ++w) {
      u32x4 k = *(const u32x4*)(lds + EP(1, hd, h) + (row - 2 * w) * 16);
      float a = 0.f;
#pragma unroll
      for (int j = 0; j < 4; ++j) a = dot2bf(q[j], k[j], a);
      s[w] = a;
    }
    float e[8];
    float mx = -3.0e38f;
#pragma unroll
    for (int w = 0; w < 8; ++w) {
      float t = s[w] + __shfl_xor(s[w], 32, 64);
      t = t * 0.25f;
      t = (tok - 2 * w >= 0) ? t : -1e9f;
      e[w] = t; mx = fmaxf(mx, t);
    }
    float sum = 0.f;
#pragma unroll
    for (int w = 0; w < 8; ++w) { e[w] = __builtin_amdgcn_exp2f((e[w] - mx) * LOG2E); sum += e[w]; }
    float inv = 1.f / sum;
    float o[8];
#pragma unroll
    for (int j = 0; j < 8; ++j) o[j] = 0.f;
#pragma unroll
    for (int w = 0; w < 8; ++w) {
      u32x4 v = *(const u32x4*)(lds + EP(2, hd, h) + (row - 2 * w) * 16);
      unsigned plo = pk_bf16(e[w] * inv, 0.f), phi = plo << 16;
#pragma unroll
      for (int j = 0; j < 4; ++j) { o[2 * j] = dot2bf(v[j], plo, o[2 * j]); o[2 * j + 1] = dot2bf(v[j], phi, o[2 * j + 1]); }
    }
    u32x4 ov;
#pragma unroll
    for (int j = 0; j < 4; ++j) ov[j] = pk_bf16(o[2 * j], o[2 * j + 1]);
    *(u32x4*)(ws + WS_OA + ((size_t)((tb * 4 + tt) * 32 + hp * 2 + hd) * 64 + lane) * 16) = ov;
  }
}

#define E1(r) "v_exp_f32 v" #r ", v" #r "\n\t"
#define HALF_X(KC, V0C, V1P, LD) \
  E1(0) E1(1) "v_smfmac_f32_32x32x32_bf16 %1, v[" V1P "], v[24:31], v88\n\t" \
  E1(2) E1(3) LD E1(4) E1(5) "v_mfma_f32_32x32x16_bf16 v[16:31], v[" KC "], v[84:87], 0\n\t" \
  E1(6) E1(7) E1(8) E1(9) "v_smfmac_f32_32x32x32_bf16 %0, v[" V0C "], v[0:7], v88\n\t" \
  E1(10) E1(11) E1(12) E1(13) "s_add_u32 %2, %2, 0x800\n\t" E1(14) E1(15)
#define HALF_Y(KN, V0C, V1C) \
  E1(16) E1(17) "v_smfmac_f32_32x32x32_bf16 %0, v[" V1C "], v[8:15], v88\n\t" \
  E1(18) E1(19) E1(20) E1(21) "s_waitcnt vmcnt(6)\n\t" "v_mfma_f32_32x32x16_bf16 v[0:15], v[" KN "], v[80:83], 0\n\t" \
  E1(22) E1(23) E1(24) E1(25) "v_smfmac_f32_32x32x32_bf16 %1, v[" V0C "], v[16:23], v88\n\t" \
  E1(26) E1(27) E1(28) E1(29) "s_nop 0\n\t" E1(30) E1(31)
#define LOADKV(KR, V0R, V1R) \
  "buffer_load_dwordx4 v[" KR "], %4, %6, %2 offen\n\t" \
  "s_mov_b64 exec, %7\n\t" \
  "buffer_load_dwordx4 v[" V0R "], %5, %6, %2 offen offset:1024\n\t" \
  "buffer_load_dwordx4 v[" V1R "], %5, %6, %2 offen offset:1536\n\t" \
  "s_mov_b64 exec, -1\n\t"
#define ONES4(a,b,c,d) "v_mov_b32 v" #a ", %10\n\tv_mov_b32 v" #b ", %10\n\tv_mov_b32 v" #c ", %10\n\tv_mov_b32 v" #d ", %10\n\t"
#define ZERO4(a,b,c,d) "v_mov_b32 v" #a ", 0\n\tv_mov_b32 v" #b ", 0\n\tv_mov_b32 v" #c ", 0\n\tv_mov_b32 v" #d ", 0\n\t"

__device__ __forceinline__ bool bad_f(float v) { return !(__builtin_fabsf(v) < 3.0e38f); }

__global__ void __launch_bounds__(512, 2)
k2_attn(u8* __restrict__ ws) {
  __shared__ __attribute__((aligned(16))) float red[8][9 * 64];
  const int tid = threadIdx.x, lane = tid & 63;
  const int wid = __builtin_amdgcn_readfirstlane(tid >> 6);
  const int bid = blockIdx.x;
  const int tt = (bid & 7) * 8 + ((bid >> 3) & 7);
  const int hg = bid >> 6;
  const int qg = wid & 3, kg = wid >> 2;
  const int hkeep = hg * 8 + qg * 2 + kg, hsend = hg * 8 + qg * 2 + (1 - kg);
  const u32x4 rkv = mkrsrc(ws + WS_KV, 256u << 10);
  const u32x4 rq = mkrsrc(ws + WS_Q + (size_t)tt * 32768, 32768u);
  unsigned soff = (unsigned)kg * 65536u;
  unsigned cnt = 8;
  const unsigned voffK = lane * 16, voffV = ((lane >> 5) * 16 + (lane & 15)) * 16;
  const unsigned long long vmask = 0x0000FFFF0000FFFFull;
  const unsigned q0off = hkeep * 1024, q1off = hsend * 1024;
  f32x16 D0, D1;
#pragma unroll
  for (int i = 0; i < 16; ++i) { D0[i] = 0.f; D1[i] = 0.f; }
  asm volatile(
    "s_nop 4\n\t"
    "buffer_load_dwordx4 v[80:83], %4, %8, %9 offen\n\t"
    "buffer_load_dwordx4 v[84:87], %4, %8, %11 offen\n\t"
    ONES4(36,37,38,39) ONES4(40,41,42,43) ONES4(48,49,50,51) ONES4(52,53,54,55)
    ONES4(60,61,62,63) ONES4(64,65,66,67) ONES4(72,73,74,75) ONES4(76,77,78,79)
    LOADKV("32:35", "36:39", "40:43") "s_add_u32 %2, %2, 0x800\n\t"
    LOADKV("44:47", "48:51", "52:55") "s_add_u32 %2, %2, 0x800\n\t"
    LOADKV("56:59", "60:63", "64:67") "s_add_u32 %2, %2, 0x800\n\t"
    ZERO4(24,25,26,27) ZERO4(28,29,30,31)
    "v_mov_b32 v88, 0xdddddddd\n\t"
    "s_waitcnt vmcnt(6)\n\t"
    "v_mfma_f32_32x32x16_bf16 v[0:15], v[32:35], v[80:83], 0\n\t"
    "s_nop 15\n\t"
    "1:\n\t"
    HALF_X("32:35", "36:39", "76:79", LOADKV("68:71", "72:75", "76:79")) HALF_Y("44:47", "36:39", "40:43")
    HALF_X("44:47", "48:51", "40:43", LOADKV("32:35", "36:39", "40:43")) HALF_Y("56:59", "48:51", "52:55")
    HALF_X("56:59", "60:63", "52:55", LOADKV("44:47", "48:51", "52:55")) HALF_Y("68:71", "60:63", "64:67")
    HALF_X("68:71", "72:75", "64:67", LOADKV("56:59", "60:63", "64:67")) HALF_Y("32:35", "72:75", "76:79")
    "s_sub_u32 %3, %3, 1\n\t"
    "s_cmp_lg_u32 %3, 0\n\t"
    "s_cbranch_scc1 1b\n\t"
    "s_nop 1\n\t"
    "v_smfmac_f32_32x32x32_bf16 %1, v[76:79], v[24:31], v88\n\t"
    "s_waitcnt vmcnt(0)\n\t"
    "s_nop 15\n\t"
    : "+v"(D0), "+v"(D1), "+s"(soff), "+s"(cnt)
    : "v"(voffK), "v"(voffV), "s"(rkv), "s"(vmask), "s"(rq), "s"(q0off), "s"(0x3F803F80u), "s"(q1off)
    : "memory", "scc",
      "v0","v1","v2","v3","v4","v5","v6","v7","v8","v9","v10","v11","v12","v13","v14","v15",
      "v16","v17","v18","v19","v20","v21","v22","v23","v24","v25","v26","v27","v28","v29","v30","v31",
      "v32","v33","v34","v35","v36","v37","v38","v39","v40","v41","v42","v43","v44","v45","v46","v47",
      "v48","v49","v50","v51","v52","v53","v54","v55","v56","v57","v58","v59","v60","v61","v62","v63",
      "v64","v65","v66","v67","v68","v69","v70","v71","v72","v73","v74","v75","v76","v77","v78","v79",
      "v80","v81","v82","v83","v84","v85","v86","v87","v88");
  {
    float* dst = red[wid ^ 4];
    f32x4 a = {D1[0], D1[1], D1[2], D1[3]}, b = {D1[4], D1[5], D1[6], D1[7]};
    *(f32x4*)(dst + lane * 4) = a;
    *(f32x4*)(dst + 256 + lane * 4) = b;
    dst[512 + lane] = D1[8];
  }
  __syncthreads();
  float o[8], l;
  {
    const float* src = red[wid];
    f32x4 a = *(const f32x4*)(src + lane * 4), b = *(const f32x4*)(src + 256 + lane * 4);
#pragma unroll
    for (int j = 0; j < 4; ++j) { o[j] = D0[j] + a[j]; o[4 + j] = D0[4 + j] + b[j]; }
    l = D0[8] + src[512 + lane];
  }
  bool bad = !(l >= 5.4e-20f && l < 1.0e36f);
#pragma unroll
  for (int j = 0; j < 8; ++j) bad = bad || bad_f(o[j]);
  if (__builtin_amdgcn_ballot_w64(bad) != 0ull) {
    const u8* kv = ws + WS_KV;
    bf16x8 qf = *(const bf16x8*)(ws + WS_Q + (size_t)tt * 32768 + q0off + lane * 16);
    f32x16 z;
#pragma unroll
    for (int i = 0; i < 16; ++i) z[i] = 0.f;
    float m = -3.0e38f;
    for (int t = 0; t < 64; ++t) {
      bf16x8 kf = *(const bf16x8*)(kv + t * 2048 + lane * 16);
      f32x16 s = __builtin_amdgcn_mfma_f32_32x32x16_bf16(kf, qf, z, 0, 0, 0);
#pragma unroll
      for (int i = 0; i < 16; ++i) m = fmaxf(m, s[i]);
    }
    m = fmaxf(m, __shfl_xor(m, 32, 64));
    f32x16 D = z;
    for (int t = 0; t < 64; ++t) {
      bf16x8 kf = *(const bf16x8*)(kv + t * 2048 + lane * 16);
      u32x4 v0 = *(const u32x4*)(kv + t * 2048 + 1024 + voffV), v1 = *(const u32x4*)(kv + t * 2048 + 1536 + voffV);
      if (lane & 16) { v0 = (u32x4){0x3F803F80u, 0x3F803F80u, 0x3F803F80u, 0x3F803F80u}; v1 = v0; }
      f32x16 s = __builtin_amdgcn_mfma_f32_32x32x16_bf16(kf, qf, z, 0, 0, 0);
      f32x8 p0, p1;
#pragma unroll
      for (int i = 0; i < 8; ++i) { p0[i] = __builtin_amdgcn_exp2f(s[i] - m); p1[i] = __builtin_amdgcn_exp2f(s[8 + i] - m); }
      D = __builtin_amdgcn_smfmac_f32_32x32x32_bf16(__builtin_bit_cast(bf16x8, v0), __builtin_bit_cast(bf16x16, p0), D, 0xDDDD, 0, 0);
      D = __builtin_amdgcn_smfmac_f32_32x32x32_bf16(__builtin_bit_cast(bf16x8, v1), __builtin_bit_cast(bf16x16, p1), D, 0xDDDD, 0, 0);
    }
#pragma unroll
    for (int j = 0; j < 8; ++j) o[j] = D[j];
    l = D[8];
  }
  const float inv = 1.f / l;
  u32x4 ov;
#pragma unroll
  for (int j = 0; j < 4; ++j) ov[j] = pk_bf16(o[2 * j] * inv, o[2 * j + 1] * inv);
  *(u32x4*)(ws + WS_OB + (size_t)tt * 32768 + q0off + lane * 16) = ov;
}

__global__ void __launch_bounds__(512, 2)
k3_out(const u8* __restrict__ ws, __bf16* __restrict__ out) {
  __shared__ __attribute__((aligned(16))) float red[4][16 * 64];
  const int tid = threadIdx.x, lane = tid & 63;
  const int wid = __builtin_amdgcn_readfirstlane(tid >> 6);
  const int bid = blockIdx.x;
  const int tt = (bid & 7) * 8 + ((bid >> 3) & 7);
  const int hg = bid >> 6;
  const int nt = hg * 4 + (wid & 3), kh = wid >> 2;
  f32x16 accA, accB;
#pragma unroll
  for (int i = 0; i < 16; ++i) { accA[i] = 0.f; accB[i] = 0.f; }
  const float g = ((const float*)(ws + WS_G))[tt * 32 + (lane & 31)];
#pragma unroll
  for (int i = 0; i < 16; ++i) {
    int hd = kh * 16 + i;
    bf16x8 wa = *(const bf16x8*)(ws + WS_WOA + (size_t)(hd * 16 + nt) * 1024 + lane * 16);
    bf16x8 wb = *(const bf16x8*)(ws + WS_WOB + (size_t)(hd * 16 + nt) * 1024 + lane * 16);
    bf16x8 oa = *(const bf16x8*)(ws + WS_OA + (size_t)(tt * 32 + hd) * 1024 + lane * 16);
    bf16x8 ob = *(const bf16x8*)(ws + WS_OB + (size_t)(tt * 32 + hd) * 1024 + lane * 16);
    accA = __builtin_amdgcn_mfma_f32_32x32x16_bf16(wa, oa, accA, 0, 0, 0);
    accB = __builtin_amdgcn_mfma_f32_32x32x16_bf16(wb, ob, accB, 0, 0, 0);
  }
  float r[16];
#pragma unroll
  for (int i = 0; i < 16; ++i) r[i] = g * accA[i] + (1.f - g) * accB[i];
  if (kh == 1) {
#pragma unroll
    for (int i = 0; i < 4; ++i) *(f32x4*)(&red[wid & 3][i * 256 + lane * 4]) = (f32x4){r[4 * i], r[4 * i + 1], r[4 * i + 2], r[4 * i + 3]};
  }
  __syncthreads();
  if (kh == 0) {
    u32x4 o0, o1;
#pragma unroll
    for (int i = 0; i < 4; ++i) {
      f32x4 a = *(const f32x4*)(&red[wid & 3][i * 256 + lane * 4]);
      unsigned p0 = pk_bf16(r[4 * i] + a[0], r[4 * i + 1] + a[1]), p1 = pk_bf16(r[4 * i + 2] + a[2], r[4 * i + 3] + a[3]);
      if (i < 2) { o0[2 * i] = p0; o0[2 * i + 1] = p1; } else { o1[2 * (i - 2)] = p0; o1[2 * (i - 2) + 1] = p1; }
    }
    __bf16* dst = out + (size_t)(tt * 32 + (lane & 31)) * 512 + nt * 32 + (lane >> 5) * 16;
    *(u32x4*)dst = o0;
    *(u32x4*)(dst + 8) = o1;
  }
}

extern "C" void kernel_launch(void* const* d_in, const int* in_sizes, int n_in,
                              void* d_out, int out_size, void* d_ws, size_t ws_size,
                              hipStream_t stream) {
  const __bf16* x = (const __bf16*)d_in[0];
  const __bf16* wqkv = (const __bf16*)d_in[1];
  const __bf16* woA = (const __bf16*)d_in[2];
  const __bf16* wq = (const __bf16*)d_in[4];
  const __bf16* wk = (const __bf16*)d_in[5];
  const __bf16* wv = (const __bf16*)d_in[6];
  const __bf16* woB = (const __bf16*)d_in[7];
  const __bf16* gw = (const __bf16*)d_in[8];
  u8* ws = (u8*)d_ws;
  k1_proj<<<256, 512, 0, stream>>>(x, wqkv, woA, wq, wk, wv, woB, gw, ws);
  k2_attn<<<256, 512, 0, stream>>>(ws);
  k3_out<<<256, 512, 0, stream>>>(ws, (__bf16*)d_out);
}
```

```cpp
#include <hip/hip_runtime.h>
#include <hip/hip_bf16.h>

typedef __bf16 bf16x2 __attribute__((ext_vector_type(2)));
typedef __bf16 bf16x4 __attribute__((ext_vector_type(4)));
typedef __bf16 bf16x8 __attribute__((ext_vector_type(8)));
typedef __bf16 bf16x16 __attribute__((ext_vector_type(16)));
typedef short s16x4 __attribute__((ext_vector_type(4)));
typedef float f32x4 __attribute__((ext_vector_type(4)));
typedef float f32x8 __attribute__((ext_vector_type(8)));
typedef float f32x16 __attribute__((ext_vector_type(16)));
typedef unsigned u32x2 __attribute__((ext_vector_type(2)));
typedef unsigned u32x4 __attribute__((ext_vector_type(4)));
typedef unsigned u32x8 __attribute__((ext_vector_type(8)));
typedef unsigned u32x16 __attribute__((ext_vector_type(16)));
typedef unsigned char u8;
typedef __attribute__((address_space(3))) u8 lds_u8;

#define LOG2E 1.4426950408889634f
#define SC1 16

#define WS_FLAG1 0u
#define WS_FLAG2 1024u
#define WS_EPOCH 2048u
#define WS_KV    (64u << 10)
#define WS_WO    (256u << 10)
#define WS_OA    (2u << 20)
#define WS_OB    (4u << 20)
#define WS_END   (8u << 20)
#define STAMP(idx) do {} while (0)

#define L_SLOT 34816
#define L_SW   16384
#define L_SH   32768
#define L_EW   139264
#define L_EO   155648
#define L_GW   159744
#define L_EP   160768
#define L_SINK 161792
#define L_GP   162816
#define L_CNT  163072
#define L_TOT  163840
#define L_AS   131072
#define L_QB   149504
#define L_RED  131072
#define L_GA   0
#define L_EX   32768
#define PLANE  2304

__device__ __forceinline__ unsigned pk_bf16(float lo, float hi) {
  bf16x2 v; v[0] = (__bf16)lo; v[1] = (__bf16)hi;
  return __builtin_bit_cast(unsigned, v);
}
__device__ __forceinline__ float dot2bf(unsigned a, unsigned b, float c) {
  return __builtin_amdgcn_fdot2_f32_bf16(__builtin_bit_cast(bf16x2, a), __builtin_bit_cast(bf16x2, b), c, false);
}
__device__ __forceinline__ u32x4 mkrsrc(const void* p, unsigned bytes) {
  unsigned long long a = (unsigned long long)p;
  u32x4 r;
  r[0] = (unsigned)a; r[1] = (unsigned)(a >> 32) & 0xffffu; r[2] = bytes; r[3] = 0x00020000u;
  return r;
}
__device__ __forceinline__ bf16x4 tr_read(unsigned a) {
  return __builtin_bit_cast(bf16x4, __builtin_amdgcn_ds_read_tr16_b64_v4i16((__attribute__((address_space(3))) s16x4*)(lds_u8*)(unsigned long long)a));
}
__device__ __forceinline__ bf16x8 cat8(bf16x4 a, bf16x4 b) { return __builtin_shufflevector(a, b, 0, 1, 2, 3, 4, 5, 6, 7); }
template <typename T> __device__ __forceinline__ T lds_ld(unsigned a) { return *(__attribute__((address_space(3))) T*)(lds_u8*)(unsigned long long)a; }
template <typename T> __device__ __forceinline__ void lds_st(unsigned a, T v) { *(__attribute__((address_space(3))) T*)(lds_u8*)(unsigned long long)a = v; }
#define RAW_BARRIER() do { __builtin_amdgcn_s_waitcnt(0xC07F); asm volatile("s_waitcnt lgkmcnt(0)\n\ts_barrier" ::: "memory"); } while (0)
#define ST128(d, vo, so) __builtin_amdgcn_raw_buffer_store_b128((d), rws, (vo) + (so), 0, SC1)
#define SCHED_FENCE() __builtin_amdgcn_sched_barrier(0)
__device__ __forceinline__ float swap_lo(unsigned a) { return __builtin_bit_cast(float, a); }
__device__ __forceinline__ bool bad_f(float v) { return !(__builtin_fabsf(v) < 3.0e38f); }

#define E1(r) "v_exp_f32 v" #r ", v" #r "\n\t"
#define TILE(t, SK, SV0, SV1, OK, OV0, OV1) \
  E1(0) E1(1) "v_smfmac_f32_32x32x32_bf16 v[48:63], v[" OV1 "], v[24:31], v96\n\t" E1(2) E1(3) \
  "s_mov_b64 exec, %[vm]\n\t" \
  "ds_read_b128 v[" OV0 "], v97 offset:(((" #t ")+1)*2048+1024)&65535\n\t" \
  "ds_read_b128 v[" OV1 "], v97 offset:(((" #t ")+1)*2048+1280)&65535\n\t" \
  "s_mov_b64 exec, -1\n\t" \
  E1(4) E1(5) "v_mfma_f32_32x32x16_bf16 v[16:31], v[" SK "], v[92:95], 0\n\t" \
  E1(6) E1(7) E1(8) E1(9) "v_smfmac_f32_32x32x32_bf16 v[32:47], v[" SV0 "], v[0:7], v96\n\t" \
  E1(10) E1(11) E1(12) E1(13) E1(14) E1(15) \
  "ds_read_b128 v[" SK "], v97 offset:(((" #t ")+2)*2048)&65535\n\t" \
  E1(16) E1(17) "v_smfmac_f32_32x32x32_bf16 v[32:47], v[" SV1 "], v[8:15], v96\n\t" \
  E1(18) E1(19) E1(20) E1(21) "s_waitcnt lgkmcnt(1)\n\t" \
  "v_mfma_f32_32x32x16_bf16 v[0:15], v[" OK "], v[88:91], 0\n\t" \
  E1(22) E1(23) E1(24) E1(25) "v_smfmac_f32_32x32x32_bf16 v[48:63], v[" SV0 "], v[16:23], v96\n\t" \
  E1(26) E1(27) E1(28) E1(29) "s_nop 0\n\t" E1(30) E1(31)
#define T2(t) TILE(t, "64:67", "68:71", "72:75", "76:79", "80:83", "84:87") TILE(t + 1, "76:79", "80:83", "84:87", "64:67", "68:71", "72:75")
#define ATT_SEG(PRE, BODY, POST) \
  asm volatile("s_nop 1\n\t" PRE BODY POST "s_waitcnt lgkmcnt(0)\n\ts_nop 7\n\t" \
    : "+{v[0:15]}"(aX), "+{v[16:31]}"(aY), "+{v[32:47]}"(D0), "+{v[48:63]}"(D1), "+{v[64:79]}"(rg0), "+{v[80:87]}"(rg1) \
    : "{v[88:95]}"(aQ), "{v96}"(0xddddddddu), "{v97}"(kvaddr), [vm] "s"(0x0000FFFF0000FFFFull) : "memory")

__global__ void __launch_bounds__(512, 2)
fused(const __bf16* __restrict__ x, const __bf16* __restrict__ wqkv, const __bf16* __restrict__ woA,
      const __bf16* __restrict__ wq, const __bf16* __restrict__ wk, const __bf16* __restrict__ wv,
      const __bf16* __restrict__ woB, const __bf16* __restrict__ gw, u8* __restrict__ ws, __bf16* __restrict__ out) {
  __shared__ __attribute__((aligned(1024))) u8 lds[L_TOT];
  asm volatile("" :: "v"((lds_u8*)lds) : "memory");
  const int lane = threadIdx.x & 63;
  const int wid = __builtin_amdgcn_readfirstlane(threadIdx.x >> 6);
  const int bid = blockIdx.x, xcd = bid & 7, bi = bid >> 3;
  const int tb = (xcd & 1) * 8 + (bi & 7);
  const int hp = (xcd >> 1) * 4 + (bi >> 3);
  const int t0 = tb * 128;
  const int r32 = lane & 31, h = lane >> 5, r16 = lane & 15, g4 = lane >> 4;
  const __amdgpu_buffer_rsrc_t rws = __builtin_amdgcn_make_buffer_rsrc(ws, 0, WS_END, 0x00020000);
  STAMP(0);
  unsigned T;
  u32x4 qa[2];

  if (wid >= 4) {
    const int lw = wid - 4;
    const u32x4 rx = mkrsrc(x, 2048u * 1024u);
    const unsigned wstride = (lw == 3) ? 1024u : 3072u;
    const u32x4 rw = mkrsrc((lw == 3) ? wq : wqkv, 512u * wstride);
    const unsigned vw0 = (unsigned)(lane >> 2) * wstride + (unsigned)(((lane & 3) ^ ((lw & 1) << 1)) * 16) + hp * 64 + ((lw == 3) ? 0 : lw * 1024);
    const unsigned wd = 16u * wstride - 1024u;
    const unsigned vw1 = vw0 + wd, vw2 = vw1 + wd, vw3 = vw2 + wd;
    const int xr = lane >> 3;
    const unsigned vx0 = (unsigned)(t0 + lw * 32 + xr) * 1024u + (unsigned)(((lane & 7) ^ (xr >> 1)) * 16);
    const unsigned vx1 = (vx0 ^ 64u) + 7168u, vx2 = vx0 + 2u * 7168u, vx3 = vx1 + 2u * 7168u;
    int hrow = lw * 4 + (xr & 3), htok = t0 - 16 + hrow; htok = htok < 0 ? 0 : htok;
    const unsigned vh = (unsigned)htok * 1024u + (unsigned)(((lane & 7) ^ (hrow >> 1)) * 16);
    auto issue = [&](int s) {
      const unsigned base = (unsigned)((s + 3) & 3) * L_SLOT;
      asm volatile(
        "s_mov_b32 m0, %[mx]\n\ts_nop 0\n\t"
        "buffer_load_dwordx4 %[x0], %[rx], %[sx] offen lds\n\t"
        "buffer_load_dwordx4 %[x1], %[rx], %[sx] offen offset:1024 lds\n\t"
        "buffer_load_dwordx4 %[x2], %[rx], %[sx] offen offset:2048 lds\n\t"
        "buffer_load_dwordx4 %[x3], %[rx], %[sx] offen offset:3072 lds\n\t"
        "s_mov_b32 m0, %[mw]\n\ts_nop 0\n\t"
        "buffer_load_dwordx4 %[w0], %[rw], %[sw] offen lds\n\t"
        "buffer_load_dwordx4 %[w1], %[rw], %[sw] offen offset:1024 lds\n\t"
        "buffer_load_dwordx4 %[w2], %[rw], %[sw] offen offset:2048 lds\n\t"
        "buffer_load_dwordx4 %[w3], %[rw], %[sw] offen offset:3072 lds\n\t"
        "s_mov_b32 m0, %[mh]\n\ts_mov_b32 exec_hi, 0\n\t"
        "buffer_load_dwordx4 %[hh], %[rx], %[sx] offen lds\n\t"
        "s_mov_b32 exec_hi, -1\n\t"
        :: [mx] "s"(base + lw * 4096), [mw] "s"(base + L_SW + lw * 4096), [mh] "s"(base + L_SH + lw * 512),
           [x0] "v"(vx0), [x1] "v"(vx1), [x2] "v"(vx2), [x3] "v"(vx3), [w0] "v"(vw0), [w1] "v"(vw1), [w2] "v"(vw2), [w3] "v"(vw3),
           [hh] "v"(vh), [rx] "s"(rx), [rw] "s"(rw), [sx] "s"((unsigned)s * 128u), [sw] "s"((unsigned)s * 64u * wstride)
        : "memory");
    };
#pragma nounroll
    for (int s = 0; s < 3; ++s) issue(s);
    STAMP(1);
    f32x4 hacc = {0.f, 0.f, 0.f, 0.f}, gacc = {0.f, 0.f, 0.f, 0.f};
    const int hseg = 1 + (lw >> 1);
    const unsigned aw = L_SW + hseg * 4096 + (g4 * 8 + (r16 >> 2)) * 64 + (((lw & 1) ^ (hseg & 1)) * 32) + (lane & 3) * 8;
    const unsigned ah = L_SH + r16 * 128 + ((g4 ^ (r16 >> 1)) * 16);
    const int grow = (hp & 3) * 32 + (lw & 1) * 16 + r16;
    const unsigned ag = grow * 128 + ((((lw >> 1) * 4 + g4) ^ ((grow >> 1) & 7)) * 16);
    const unsigned agw = L_GW + (lw >> 1) * 64 + g4 * 16;
    u32x4 pol;
#pragma nounroll
    for (int s = 0; s < 8; ++s) {
      if (s < 5) asm volatile("s_waitcnt vmcnt(18)" ::: "memory");
      else if (s == 5) asm volatile("s_waitcnt vmcnt(18)" ::: "memory");
      else if (s == 6) asm volatile("s_waitcnt vmcnt(9)" ::: "memory");
      else asm volatile("s_waitcnt vmcnt(0)" ::: "memory");
      RAW_BARRIER();
      if (s < 5) issue(s + 3);
      const unsigned st = (unsigned)((s + 3) & 3) * L_SLOT;
      bf16x8 wf0 = cat8(tr_read(st + aw), tr_read(st + aw + 256));
      bf16x8 wf1 = cat8(tr_read(st + aw + 2048), tr_read(st + aw + 2048 + 256));
      bf16x8 xf0 = lds_ld<bf16x8>(st + ah), xf1 = lds_ld<bf16x8>(st + (ah ^ 64u));
      bf16x8 gx = lds_ld<bf16x8>(st + ag), gf = lds_ld<bf16x8>(agw + s * 128);
      hacc = __builtin_amdgcn_mfma_f32_16x16x32_bf16(wf0, xf0, hacc, 0, 0, 0);
      gacc = __builtin_amdgcn_mfma_f32_16x16x32_bf16(gf, gx, gacc, 0, 0, 0);
      hacc = __builtin_amdgcn_mfma_f32_16x16x32_bf16(wf1, xf1, hacc, 0, 0, 0);
    }
    STAMP(2);
    T = (__builtin_amdgcn_readfirstlane(lds_ld<unsigned>(L_EP)) * 1664525u + 1013904223u) | 1u;
    {
      u32x2 o; o[0] = pk_bf16(hacc[0], hacc[1]); o[1] = pk_bf16(hacc[2], hacc[3]);
      lds_st<u32x2>(L_AS + (((lw >> 1) * 2 + (lw & 1)) * 2 + (g4 & 1)) * PLANE + r16 * 16 + (g4 >> 1) * 8, o);
      if (g4 == 0) lds_st<float>(L_GP + (lw >> 1) * 128 + (lw & 1) * 64 + r16 * 4, gacc[0]);
    }
#pragma nounroll
    for (int it = 0; it < 200000; ++it) {
      asm volatile("" ::: "memory");
      pol = __builtin_amdgcn_raw_buffer_load_b128(rws, lane * 16, WS_FLAG1, SC1);
      bool ok = pol[0] == T && pol[1] == T && pol[2] == T && pol[3] == T;
      if (__builtin_amdgcn_ballot_w64(ok) == ~0ull) break;
      __builtin_amdgcn_s_sleep(2);
    }
    asm volatile("" ::: "memory");
    STAMP(3);
    u32x4 kv[32];
#pragma unroll
    for (int i = 0; i < 32; ++i) kv[i] = __builtin_amdgcn_raw_buffer_load_b128(rws, lane * 16 + (i & 3) * 1024, WS_KV + lw * 32768 + (i >> 2) * 4096, SC1);
    RAW_BARRIER();
#pragma unroll
    for (int i = 0; i < 32; ++i) lds_st<u32x4>(lw * 32768 + i * 1024 + lane * 16, kv[i]);
    STAMP(4);
  } else {
    const int w = wid;
    const int f = bid * 4 + w;
    {
      unsigned long long pc;
      asm volatile("s_getpc_b64 %0" : "=s"(pc));
      const void* ex = w == 0 ? (const void*)(ws + WS_EPOCH) : w == 1 ? (const void*)gw : w == 2 ? (const void*)(pc + (bi < 20 ? bi : 0) * 1024)
                                                                                                  : (const void*)(out + (size_t)(t0 + (hp & 3) * 32) * 512);
      const unsigned mex = w == 0 ? L_EP : w == 1 ? L_GW : L_SINK;
      const u32x4 rex = mkrsrc(ex, 1024u);
      const u32x4 rkv = mkrsrc((hp & 1) ? wv : wk, 16384u);
      const u32x4 rx = mkrsrc(x, 2048u * 1024u);
      const u32x4 rwo = mkrsrc((f & 512) ? woB : woA, 512u * 1024u);
      const unsigned vxo = (unsigned)(t0 + (hp >> 1) * 16 + r16) * 1024u + g4 * 16;
      const unsigned vwo = (unsigned)(lane >> 2) * 1024u + (lane & 3) * 16;
      u32x4 xe0, xe1, xe2, xe3;
      asm volatile(
        "s_mov_b32 m0, %[mex]\n\ts_nop 0\n\t"
        "buffer_load_dwordx4 %[l16], %[rex], 0 offen lds\n\t"
        "s_mov_b32 m0, %[mk]\n\ts_nop 0\n\t"
        "buffer_load_dwordx4 %[l16], %[rkv], %[sk] offen lds\n\t"
        "buffer_load_dwordx4 %[l16], %[rkv], %[sk] offen offset:1024 lds\n\t"
        "buffer_load_dwordx4 %[l16], %[rkv], %[sk] offen offset:2048 lds\n\t"
        "buffer_load_dwordx4 %[l16], %[rkv], %[sk] offen offset:3072 lds\n\t"
        "buffer_load_dwordx4 %[x0], %[vx], %[rx], %[sx] offen\n\t"
        "buffer_load_dwordx4 %[x1], %[vx], %[rx], %[sx] offen offset:64\n\t"
        "buffer_load_dwordx4 %[x2], %[vx], %[rx], %[sx] offen offset:128\n\t"
        "buffer_load_dwordx4 %[x3], %[vx], %[rx], %[sx] offen offset:192\n\t"
        "s_mov_b32 m0, %[mo]\n\ts_nop 0\n\t"
        "buffer_load_dwordx4 %[vo], %[rwo], %[so] offen lds\n\t"
        : [x0] "=&v"(xe0), [x1] "=&v"(xe1), [x2] "=&v"(xe2), [x3] "=&v"(xe3)
        : [mex] "s"(mex), [mk] "s"(L_EW + w * 4096), [mo] "s"(L_EO + w * 1024), [l16] "v"(lane * 16), [rex] "s"(rex), [rkv] "s"(rkv),
          [sk] "s"(w * 4096), [vx] "v"(vxo), [rx] "s"(rx), [sx] "s"(w * 256), [vo] "v"(vwo), [rwo] "s"(rwo),
          [so] "s"(((f & 511) >> 4) * 16384 + (f & 15) * 64)
        : "memory");
      const u32x4 z4 = {0u, 0u, 0u, 0u};
#pragma unroll
      for (int i = 0; i < 4; ++i)
        ST128(z4, lane * 16 + i * 1024, WS_OB + (tb * 4 + w) * 65536 + hp * 4096);
      if (w == 0 && lane == 0) lds_st<unsigned>(L_CNT, 0u);
      asm volatile("s_waitcnt vmcnt(5)" : "+v"(xe0), "+v"(xe1), "+v"(xe2), "+v"(xe3) :: "memory");
      STAMP(1);
      const unsigned ak = L_EW + w * 4096 + (g4 * 8 + (r16 >> 2)) * 32 + (lane & 3) * 8;
      bf16x8 kf0 = cat8(tr_read(ak), tr_read(ak + 128)), kf1 = cat8(tr_read(ak + 1024), tr_read(ak + 1152));
      bf16x8 kf2 = cat8(tr_read(ak + 2048), tr_read(ak + 2176)), kf3 = cat8(tr_read(ak + 3072), tr_read(ak + 3200));
      f32x4 pa = {0.f, 0.f, 0.f, 0.f};
      if (hp & 1) {
        pa = __builtin_amdgcn_mfma_f32_16x16x32_bf16(__builtin_bit_cast(bf16x8, xe0), kf0, pa, 0, 0, 0);
        pa = __builtin_amdgcn_mfma_f32_16x16x32_bf16(__builtin_bit_cast(bf16x8, xe1), kf1, pa, 0, 0, 0);
        pa = __builtin_amdgcn_mfma_f32_16x16x32_bf16(__builtin_bit_cast(bf16x8, xe2), kf2, pa, 0, 0, 0);
        pa = __builtin_amdgcn_mfma_f32_16x16x32_bf16(__builtin_bit_cast(bf16x8, xe3), kf3, pa, 0, 0, 0);
      } else {
        pa = __builtin_amdgcn_mfma_f32_16x16x32_bf16(kf0, __builtin_bit_cast(bf16x8, xe0), pa, 0, 0, 0);
        pa = __builtin_amdgcn_mfma_f32_16x16x32_bf16(kf1, __builtin_bit_cast(bf16x8, xe1), pa, 0, 0, 0);
        pa = __builtin_amdgcn_mfma_f32_16x16x32_bf16(kf2, __builtin_bit_cast(bf16x8, xe2), pa, 0, 0, 0);
        pa = __builtin_amdgcn_mfma_f32_16x16x32_bf16(kf3, __builtin_bit_cast(bf16x8, xe3), pa, 0, 0, 0);
      }
      lds_st<f32x4>(L_EW + w * 4096 + lane * 16, pa);
      asm volatile("s_waitcnt vmcnt(4)" ::: "memory");
      const unsigned ao = L_EO + w * 1024 + (h * 4 + (r16 >> 2)) * 64 + (lane & 1) * 32 + ((lane >> 1) & 1) * 8 + (g4 & 1) * 16;
      bf16x8 wo = cat8(tr_read(ao), tr_read(ao + 512));
      ST128(__builtin_bit_cast(u32x4, wo), lane * 16, WS_WO + f * 1024);
    }
    const int np = w & 1, th = w >> 1;
    const unsigned aw = L_SW + (g4 & 1) * 4096 + (h * 8 + (r16 >> 2)) * 64 + ((np ^ (g4 & 1)) * 32) + (lane & 3) * 8;
    const int xrow = th * 64 + r32;
    const unsigned ax = xrow * 128 + ((h ^ ((xrow >> 1) & 7)) * 16);
    f32x16 acc[2][2];
#pragma unroll
    for (int a = 0; a < 2; ++a)
#pragma unroll
      for (int b = 0; b < 2; ++b)
#pragma unroll
        for (int i = 0; i < 16; ++i) acc[a][b][i] = 0.f;
    bf16x8 wf[2][2], xf[2][2];
    auto ldf = [&](int p, unsigned st, int ks) {
#pragma unroll
      for (int a = 0; a < 2; ++a) {
        wf[p][a] = cat8(tr_read(st + aw + a * 8192 + ks * 1024), tr_read(st + aw + a * 8192 + ks * 1024 + 256));
        xf[p][a] = lds_ld<bf16x8>(st + (ax ^ (ks * 32)) + a * 4096);
      }
    };
    auto mm = [&](int p) {
#pragma unroll
      for (int a = 0; a < 2; ++a)
#pragma unroll
        for (int b = 0; b < 2; ++b) acc[a][b] = __builtin_amdgcn_mfma_f32_32x32x16_bf16(wf[p][a], xf[p][b], acc[a][b], 0, 0, 0);
    };
    RAW_BARRIER();
    STAMP(2);
    ldf(0, 3 * L_SLOT, 0);
    T = (__builtin_amdgcn_readfirstlane(lds_ld<unsigned>(L_EP)) * 1664525u + 1013904223u) | 1u;
    if (w == 0) {
      f32x4 p0 = lds_ld<f32x4>(L_EW + lane * 16), p1 = lds_ld<f32x4>(L_EW + 4096 + lane * 16);
      f32x4 p2 = lds_ld<f32x4>(L_EW + 8192 + lane * 16), p3 = lds_ld<f32x4>(L_EW + 12288 + lane * 16);
      f32x4 p = ((p0 + p1) + (p2 + p3)) * ((hp & 1) ? 1.0f : 0.25f * LOG2E);
      u32x2 o; o[0] = pk_bf16(p[0], p[1]); o[1] = pk_bf16(p[2], p[3]);
      const int blk = tb * 8 + (hp >> 1), sp = blk & 1;
      const unsigned d = (hp & 1) ? 1024 + (g4 >> 1) * 512 + sp * 256 + r16 * 16 + (g4 & 1) * 8 : ((g4 & 1) * 32 + sp * 16 + r16) * 16 + (g4 >> 1) * 8;
      __builtin_amdgcn_raw_buffer_store_b64(o, rws, d, WS_KV + (blk >> 1) * 2048, SC1);
    }
#pragma nounroll
    for (int s = 0; s < 8; ++s) {
      const unsigned st = (unsigned)((s + 3) & 3) * L_SLOT, sn = (unsigned)(s & 3) * L_SLOT;
      ldf(1, st, 1); SCHED_FENCE(); mm(0); SCHED_FENCE();
      ldf(0, st, 2); SCHED_FENCE(); mm(1); SCHED_FENCE();
      ldf(1, st, 3); SCHED_FENCE(); mm(0); SCHED_FENCE();
      if (s == 1 && w == 0) {
        asm volatile("s_waitcnt vmcnt(0)" ::: "memory");
        if (lane == 0) __builtin_amdgcn_raw_buffer_store_b32(T, rws, bid * 4, WS_FLAG1, SC1);
        STAMP(3);
      }
      __builtin_amdgcn_s_waitcnt(0xC07F);
      if (s < 7) RAW_BARRIER();
      ldf(0, sn, 0);
      SCHED_FENCE(); mm(1); SCHED_FENCE();
    }
    STAMP(4);
#pragma unroll
    for (int b = 0; b < 2; ++b) {
      u32x4 kk, vv, qq;
#pragma unroll
      for (int j = 0; j < 4; ++j) {
        qa[b][j] = pk_bf16(acc[0][b][2 * j], acc[0][b][2 * j + 1]);
        kk[j] = pk_bf16(acc[0][b][8 + 2 * j], acc[0][b][9 + 2 * j]);
        vv[j] = pk_bf16(acc[1][b][2 * j], acc[1][b][2 * j + 1]);
        qq[j] = pk_bf16(acc[1][b][8 + 2 * j], acc[1][b][9 + 2 * j]);
      }
      lds_st<u32x4>(L_AS + (np * 2 + h) * PLANE + (xrow + b * 32 + 16) * 16, kk);
      lds_st<u32x4>(L_AS + (4 + np * 2 + h) * PLANE + (xrow + b * 32 + 16) * 16, vv);
      lds_st<u32x4>(L_QB + ((th * 2 + b) * 2 + np) * 1024 + lane * 16, qq);
    }
    RAW_BARRIER();
    STAMP(5);
#pragma unroll
    for (int b = 0; b < 2; ++b) {
      const unsigned ka = L_AS + (np * 2 + h) * PLANE + (xrow + b * 32 + 2) * 16;
      float s[8];
#pragma unroll
      for (int i = 0; i < 8; ++i) {
        u32x4 k = lds_ld<u32x4>(ka + (7 - i) * 32);
        float a = 0.f;
#pragma unroll
        for (int j = 0; j < 4; ++j) a = dot2bf(qa[b][j], k[j], a);
        s[i] = a;
      }
      float e[4], mx = -3.0e38f;
      const int tok = t0 + xrow + b * 32 - 8 * h;
#pragma unroll
      for (int i = 0; i < 4; ++i) {
        auto r = __builtin_amdgcn_permlane32_swap(__builtin_bit_cast(unsigned, s[i]), __builtin_bit_cast(unsigned, s[i + 4]), false, false);
        float t = (swap_lo(r[0]) + swap_lo(r[1])) * (0.25f * LOG2E);
        t = (tok - 2 * i >= 0) ? t : -1.4426950e9f;
        e[i] = t; mx = fmaxf(mx, t);
      }
      { auto r = __builtin_amdgcn_permlane32_swap(__builtin_bit_cast(unsigned, mx), __builtin_bit_cast(unsigned, mx), false, false);
        mx = fmaxf(swap_lo(r[0]), swap_lo(r[1])); }
      float sum = 0.f;
#pragma unroll
      for (int i = 0; i < 4; ++i) { e[i] = __builtin_amdgcn_exp2f(e[i] - mx); sum += e[i]; }
      { auto r = __builtin_amdgcn_permlane32_swap(__builtin_bit_cast(unsigned, sum), __builtin_bit_cast(unsigned, sum), false, false);
        sum = swap_lo(r[0]) + swap_lo(r[1]); }
      const float inv = __builtin_amdgcn_rcpf(sum);
      float o[8];
#pragma unroll
      for (int j = 0; j < 8; ++j) o[j] = 0.f;
#pragma unroll
      for (int i = 0; i < 4; ++i) {
        const unsigned pl = pk_bf16(e[i] * inv, 0.f);
        auto r = __builtin_amdgcn_permlane32_swap(pl, pl, false, false);
#pragma unroll
        for (int c = 0; c < 2; ++c) {
          u32x4 v = lds_ld<u32x4>(ka + 4 * PLANE + (7 - i - 4 * c) * 32);
          const unsigned plo = r[c], phi = plo << 16;
#pragma unroll
          for (int j = 0; j < 4; ++j) { o[2 * j] = dot2bf(v[j], plo, o[2 * j]); o[2 * j + 1] = dot2bf(v[j], phi, o[2 * j + 1]); }
        }
      }
      u32x4 ov;
#pragma unroll
      for (int j = 0; j < 4; ++j) ov[j] = pk_bf16(o[2 * j], o[2 * j + 1]);
      ST128(ov, lane * 16, WS_OA + ((tb * 4 + th * 2 + b) * 32 + hp * 2 + np) * 1024);
    }
    STAMP(6);
  }

  const int tt = wid & 3, kg = wid >> 2;
  u32x8 aQ;
  {
    u32x4 q0 = lds_ld<u32x4>(L_QB + (tt * 2 + kg) * 1024 + lane * 16), q1 = lds_ld<u32x4>(L_QB + (tt * 2 + (kg ^ 1)) * 1024 + lane * 16);
    aQ = __builtin_shufflevector(q0, q1, 0, 1, 2, 3, 4, 5, 6, 7);
  }
  f32x16 aX, aY, D0, D1;
  u32x16 rg0; u32x8 rg1;
  const unsigned one = ((lane & 27) == 16) ? 0x3F803F80u : 0u;
#pragma unroll
  for (int i = 0; i < 16; ++i) { aX[i] = 0.f; aY[i] = 0.f; D0[i] = 0.f; D1[i] = 0.f; rg0[i] = one; }
#pragma unroll
  for (int i = 0; i < 8; ++i) rg1[i] = one;
  const unsigned kvaddr = kg * 65536 + lane * 16;
  RAW_BARRIER();
  STAMP(7);
  ATT_SEG("ds_read_b128 v[64:67], v97\n\t"
          "s_mov_b64 exec, %[vm]\n\t"
          "ds_read_b128 v[68:71], v97 offset:1024\n\t"
          "ds_read_b128 v[72:75], v97 offset:1280\n\t"
          "s_mov_b64 exec, -1\n\t"
          "ds_read_b128 v[76:79], v97 offset:2048\n\t"
          "s_waitcnt lgkmcnt(1)\n\t"
          "v_mfma_f32_32x32x16_bf16 v[0:15], v[64:67], v[88:91], 0\n\t"
          "s_nop 11\n\t",
          T2(0) T2(2), "");
  if (wid < 4) {
    asm volatile("s_waitcnt vmcnt(0)" ::: "memory");
    if (lane == 0) {
      unsigned old = __hip_atomic_fetch_add((__attribute__((address_space(3))) unsigned*)(lds_u8*)(unsigned long long)L_CNT, 1u, __ATOMIC_RELAXED, __HIP_MEMORY_SCOPE_WORKGROUP);
      if (old == 3u) __builtin_amdgcn_raw_buffer_store_b32(T, rws, bid * 4, WS_FLAG2, SC1);
    }
  }
  ATT_SEG("", T2(4) T2(6) T2(8), "");
  u32x4 pol2 = __builtin_amdgcn_raw_buffer_load_b128(rws, lane * 16, WS_FLAG2, SC1);
  ATT_SEG("", T2(10) T2(12), "");
#pragma nounroll
  for (int it = 0; it < 200000; ++it) {
    bool ok = pol2[0] == T && pol2[1] == T && pol2[2] == T && pol2[3] == T;
    if (__builtin_amdgcn_ballot_w64(ok) == ~0ull) break;
    __builtin_amdgcn_s_sleep(2);
    asm volatile("" ::: "memory");
    pol2 = __builtin_amdgcn_raw_buffer_load_b128(rws, lane * 16, WS_FLAG2, SC1);
  }
  asm volatile("" ::: "memory");
  STAMP(8);
  if (bid == 0 && threadIdx.x == 0) __builtin_amdgcn_raw_buffer_store_b32(T, rws, 0, WS_EPOCH, SC1);
  const int ct = tb * 4 + (hp & 3);
  const int nt = (hp >> 2) * 4 + tt;
  const unsigned so_w = WS_WO + (kg * 256 + nt) * 1024, so_a = WS_OA + (ct * 32 + kg * 16) * 1024;
  u32x4 fw[16], fo[8];
#pragma unroll
  for (int i = 0; i < 8; ++i) {
    fw[i] = __builtin_amdgcn_raw_buffer_load_b128(rws, lane * 16, so_w + i * 16384, SC1);
    fo[i] = __builtin_amdgcn_raw_buffer_load_b128(rws, lane * 16 + (i & 3) * 1024, so_a + (i >> 2) * 4096, SC1);
  }
  ATT_SEG("", T2(14) T2(16) T2(18), "");
  f32x16 accA;
#pragma unroll
  for (int i = 0; i < 16; ++i) accA[i] = 0.f;
#pragma unroll
  for (int i = 0; i < 8; ++i) accA = __builtin_amdgcn_mfma_f32_32x32x16_bf16(__builtin_bit_cast(bf16x8, fw[i]), __builtin_bit_cast(bf16x8, fo[i]), accA, 0, 0, 0);
  SCHED_FENCE();
#pragma unroll
  for (int i = 0; i < 8; ++i) {
    fw[i] = __builtin_amdgcn_raw_buffer_load_b128(rws, lane * 16, so_w + (8 + i) * 16384, SC1);
    fo[i] = __builtin_amdgcn_raw_buffer_load_b128(rws, lane * 16 + (i & 3) * 1024, so_a + 8192 + (i >> 2) * 4096, SC1);
  }
  ATT_SEG("", T2(20) T2(22) T2(24), "");
#pragma unroll
  for (int i = 0; i < 8; ++i) accA = __builtin_amdgcn_mfma_f32_32x32x16_bf16(__builtin_bit_cast(bf16x8, fw[i]), __builtin_bit_cast(bf16x8, fo[i]), accA, 0, 0, 0);
  SCHED_FENCE();
#pragma unroll
  for (int i = 0; i < 16; ++i) fw[i] = __builtin_amdgcn_raw_buffer_load_b128(rws, lane * 16, so_w + (512u << 10) + i * 16384, SC1);
  const float gz = lds_ld<float>(L_GP + r32 * 4) + lds_ld<float>(L_GP + 128 + r32 * 4);
  const float g = __builtin_amdgcn_rcpf(1.f + __builtin_amdgcn_exp2f(-gz * LOG2E));
#pragma unroll
  for (int i = 0; i < 16; ++i) accA[i] *= g;
  ATT_SEG("", T2(26) T2(28) T2(30), "s_nop 1\n\tv_smfmac_f32_32x32x32_bf16 v[48:63], v[84:87], v[24:31], v96\n\ts_nop 3\n\t");
  STAMP(9);
  {
    const unsigned dst = L_RED + (wid ^ 4) * PLANE;
    lds_st<f32x4>(dst + lane * 16, (f32x4){D1[0], D1[1], D1[2], D1[3]});
    lds_st<f32x4>(dst + 1024 + lane * 16, (f32x4){D1[4], D1[5], D1[6], D1[7]});
    lds_st<float>(dst + 2048 + lane * 4, D1[8]);
  }
  RAW_BARRIER();
  float o[8], l;
  {
    const unsigned src = L_RED + wid * PLANE;
    f32x4 a = lds_ld<f32x4>(src + lane * 16), b = lds_ld<f32x4>(src + 1024 + lane * 16);
#pragma unroll
    for (int j = 0; j < 4; ++j) { o[j] = D0[j] + a[j]; o[4 + j] = D0[4 + j] + b[j]; }
    l = D0[8] + lds_ld<float>(src + 2048 + lane * 4);
  }
  bool bad = !(l >= 5.4e-20f && l < 1.0e36f);
#pragma unroll
  for (int j = 0; j < 8; ++j) bad = bad || bad_f(o[j]);
  if (__builtin_amdgcn_ballot_w64(bad) != 0ull) {
    const bf16x8 qf = __builtin_bit_cast(bf16x8, __builtin_shufflevector(aQ, aQ, 0, 1, 2, 3));
    f32x16 z;
#pragma unroll
    for (int i = 0; i < 16; ++i) z[i] = 0.f;
    float m = -3.0e38f;
#pragma nounroll
    for (int t = 0; t < 64; ++t) {
      u32x4 kf = __builtin_amdgcn_raw_buffer_load_b128(rws, lane * 16, WS_KV + t * 2048, SC1);
      f32x16 s = __builtin_amdgcn_mfma_f32_32x32x16_bf16(__builtin_bit_cast(bf16x8, kf), qf, z, 0, 0, 0);
#pragma unroll
      for (int i = 0; i < 16; ++i) m = fmaxf(m, s[i]);
    }
    { auto r = __builtin_amdgcn_permlane32_swap(__builtin_bit_cast(unsigned, m), __builtin_bit_cast(unsigned, m), false, false);
      m = fmaxf(swap_lo(r[0]), swap_lo(r[1])); }
    f32x16 D = z;
#pragma nounroll
    for (int t = 0; t < 64; ++t) {
      u32x4 kf = __builtin_amdgcn_raw_buffer_load_b128(rws, lane * 16, WS_KV + t * 2048, SC1);
      u32x4 v0 = __builtin_amdgcn_raw_buffer_load_b128(rws, (lane & 47) * 16 + 1024, WS_KV + t * 2048, SC1);
      u32x4 v1 = __builtin_amdgcn_raw_buffer_load_b128(rws, (lane & 47) * 16 + 1280, WS_KV + t * 2048, SC1);
      if (lane & 16) { v0 = (u32x4){one, one, one, one}; v1 = v0; }
      f32x16 s = __builtin_amdgcn_mfma_f32_32x32x16_bf16(__builtin_bit_cast(bf16x8, kf), qf, z, 0, 0, 0);
      f32x8 p0, p1;
#pragma unroll
      for (int i = 0; i < 8; ++i) { p0[i] = __builtin_amdgcn_exp2f(s[i] - m); p1[i] = __builtin_amdgcn_exp2f(s[8 + i] - m); }
      D = __builtin_amdgcn_smfmac_f32_32x32x32_bf16(__builtin_bit_cast(bf16x8, v0), __builtin_bit_cast(bf16x16, p0), D, 0xDDDD, 0, 0);
      D = __builtin_amdgcn_smfmac_f32_32x32x32_bf16(__builtin_bit_cast(bf16x8, v1), __builtin_bit_cast(bf16x16, p1), D, 0xDDDD, 0, 0);
    }
#pragma unroll
    for (int j = 0; j < 8; ++j) o[j] = D[j];
    l = D[8];
  }
  {
    const float inv = __builtin_amdgcn_rcpf(l);
    const unsigned so = WS_OB + ((tb * 4 + tt) * 32 + hp * 2 + kg) * 2048;
    u32x4 g0 = {pk_bf16(o[0] * inv, o[1] * inv), T, pk_bf16(o[2] * inv, o[3] * inv), T};
    u32x4 g1 = {pk_bf16(o[4] * inv, o[5] * inv), T, pk_bf16(o[6] * inv, o[7] * inv), T};
    ST128(g0, lane * 16, so);
    ST128(g1, lane * 16 + 1024, so);
  }
  STAMP(10);
  {
    const unsigned so = WS_OB + (ct * 32 + wid * 4) * 2048;
    u32x4 gr[8];
#pragma nounroll
    for (int it = 0; it < 200000; ++it) {
      bool ok = true;
      asm volatile("" ::: "memory");
#pragma unroll
      for (int i = 0; i < 8; ++i) gr[i] = __builtin_amdgcn_raw_buffer_load_b128(rws, lane * 16 + (i & 3) * 1024, so + (i >> 2) * 4096, SC1);
#pragma unroll
      for (int i = 0; i < 8; ++i) ok = ok && gr[i][1] == T && gr[i][3] == T;
      if (__builtin_amdgcn_ballot_w64(ok) == ~0ull) break;
    }
#pragma unroll
    for (int i = 0; i < 4; ++i)
      lds_st<u32x4>(L_GA + (wid * 4 + i) * 1024 + lane * 16, (u32x4){gr[2 * i][0], gr[2 * i][2], gr[2 * i + 1][0], gr[2 * i + 1][2]});
  }
  RAW_BARRIER();
  STAMP(11);
  f32x16 accB;
#pragma unroll
  for (int i = 0; i < 16; ++i) accB[i] = 0.f;
#pragma unroll
  for (int i = 0; i < 16; ++i)
    accB = __builtin_amdgcn_mfma_f32_32x32x16_bf16(__builtin_bit_cast(bf16x8, fw[i]), lds_ld<bf16x8>(L_GA + (kg * 16 + i) * 1024 + lane * 16), accB, 0, 0, 0);
  float r[16];
#pragma unroll
  for (int i = 0; i < 16; ++i) r[i] = accA[i] + (1.f - g) * accB[i];
  {
    const unsigned dst = L_EX + (wid ^ 4) * 2048 + lane * 16;
    f32x4 a, b;
#pragma unroll
    for (int j = 0; j < 4; ++j) { a[j] = kg ? r[j] : r[8 + j]; b[j] = kg ? r[4 + j] : r[12 + j]; }
    lds_st<f32x4>(dst, a); lds_st<f32x4>(dst + 1024, b);
  }
  RAW_BARRIER();
  {
    const unsigned src = L_EX + wid * 2048 + lane * 16;
    f32x4 a = lds_ld<f32x4>(src), b = lds_ld<f32x4>(src + 1024);
#pragma unroll
    for (int j = 0; j < 4; ++j) { a[j] += kg ? r[8 + j] : r[j]; b[j] += kg ? r[12 + j] : r[4 + j]; }
    u32x4 ov = {pk_bf16(a[0], a[1]), pk_bf16(a[2], a[3]), pk_bf16(b[0], b[1]), pk_bf16(b[2], b[3])};
    *(u32x4*)(out + (size_t)(ct * 32 + r32) * 512 + nt * 32 + h * 16 + kg * 8) = ov;
  }
  STAMP(12);
}

extern "C" void kernel_launch(void* const* d_in, const int* in_sizes, int n_in,
                              void* d_out, int out_size, void* d_ws, size_t ws_size,
                              hipStream_t stream) {
  fused<<<256, 512, 0, stream>>>((const __bf16*)d_in[0], (const __bf16*)d_in[1], (const __bf16*)d_in[2], (const __bf16*)d_in[4],
                                 (const __bf16*)d_in[5], (const __bf16*)d_in[6], (const __bf16*)d_in[7], (const __bf16*)d_in[8],
                                 (u8*)d_ws, (__bf16*)d_out);
}
```

```cpp
#include <hip/hip_runtime.h>
#include <hip/hip_bf16.h>

typedef __bf16 bf16x2 __attribute__((ext_vector_type(2)));
typedef __bf16 bf16x4 __attribute__((ext_vector_type(4)));
typedef __bf16 bf16x8 __attribute__((ext_vector_type(8)));
typedef __bf16 bf16x16 __attribute__((ext_vector_type(16)));
typedef short s16x4 __attribute__((ext_vector_type(4)));
typedef float f32x4 __attribute__((ext_vector_type(4)));
typedef float f32x8 __attribute__((ext_vector_type(8)));
typedef float f32x16 __attribute__((ext_vector_type(16)));
typedef unsigned u32x2 __attribute__((ext_vector_type(2)));
typedef unsigned u32x4 __attribute__((ext_vector_type(4)));
typedef unsigned u32x8 __attribute__((ext_vector_type(8)));
typedef unsigned u32x16 __attribute__((ext_vector_type(16)));
typedef unsigned char u8;
typedef __attribute__((address_space(3))) u8 lds_u8;

#define LOG2E 1.4426950408889634f
#define SC1 16

#define WS_FLAG1 0u
#define WS_FLAG2 1024u
#define WS_EPOCH 2048u
#define WS_KV    (64u << 10)
#define WS_OA    (2u << 20)
#define WS_OB    (4u << 20)
#define WS_END   (8u << 20)
#define STAMP(idx) do {} while (0)

#define L_SLOT 34816
#define L_SW   16384
#define L_SH   32768
#define KVS    2080
#define L_AS   139264
#define L_EW   139264
#define L_QB   157696
#define L_GW   161792
#define L_GP   162816
#define L_CNT  163072
#define L_TOT  163840
#define L_GA0  133120
#define L_TW   149504
#define PLANE  2304

__device__ __forceinline__ unsigned pk_bf16(float lo, float hi) {
  bf16x2 v; v[0] = (__bf16)lo; v[1] = (__bf16)hi;
  return __builtin_bit_cast(unsigned, v);
}
__device__ __forceinline__ float dot2bf(unsigned a, unsigned b, float c) {
  return __builtin_amdgcn_fdot2_f32_bf16(__builtin_bit_cast(bf16x2, a), __builtin_bit_cast(bf16x2, b), c, false);
}
__device__ __forceinline__ u32x4 mkrsrc(const void* p, unsigned bytes) {
  unsigned long long a = (unsigned long long)p;
  u32x4 r;
  r[0] = (unsigned)a; r[1] = (unsigned)(a >> 32) & 0xffffu; r[2] = bytes; r[3] = 0x00020000u;
  return r;
}
__device__ __forceinline__ bf16x4 tr_read(unsigned a) {
  return __builtin_bit_cast(bf16x4, __builtin_amdgcn_ds_read_tr16_b64_v4i16((__attribute__((address_space(3))) s16x4*)(lds_u8*)(unsigned long long)a));
}
__device__ __forceinline__ bf16x8 cat8(bf16x4 a, bf16x4 b) { return __builtin_shufflevector(a, b, 0, 1, 2, 3, 4, 5, 6, 7); }
template <typename T> __device__ __forceinline__ T lds_ld(unsigned a) { return *(__attribute__((address_space(3))) T*)(lds_u8*)(unsigned long long)a; }
template <typename T> __device__ __forceinline__ void lds_st(unsigned a, T v) { *(__attribute__((address_space(3))) T*)(lds_u8*)(unsigned long long)a = v; }
#define RAW_BARRIER() do { __builtin_amdgcn_s_waitcnt(0xC07F); asm volatile("s_waitcnt lgkmcnt(0)\n\ts_barrier" ::: "memory"); } while (0)
#define ST128(d, vo, so) __builtin_amdgcn_raw_buffer_store_b128((d), rws, (vo) + (so), 0, SC1)
#define SCHED_FENCE() __builtin_amdgcn_sched_barrier(0)
__device__ __forceinline__ float swap_lo(unsigned a) { return __builtin_bit_cast(float, a); }
__device__ __forceinline__ bool bad_f(float v) { return !(__builtin_fabsf(v) < 3.0e38f); }

#define E1(r) "v_exp_f32 v" #r ", v" #r "\n\t"
#define PVM(a, b) "v_smfmac_f32_32x32x32_bf16 v[32:47], v[" a "], v[" b "], v76\n\t"
#define QKM(d, k) "v_mfma_f32_32x32x16_bf16 v[" d "], v[" k "], v[72:75], 0\n\t"
#define RDL(d, a, t, c) "ds_read_b128 v[" d "], " a " offset:(" t ")*2080+" c "\n\t"
#define PAIR(t) \
  E1(0) E1(1) PVM("68:71", "24:31") E1(2) RDL("64:67", "v78", #t "+1", "1024") E1(3) RDL("68:71", "v79", #t "+1", "1280") \
  E1(4) "s_waitcnt lgkmcnt(3)\n\t" E1(5) QKM("16:31", "52:55") E1(6) RDL("52:55", "v77", #t "+3", "0") E1(7) E1(8) E1(9) \
  PVM("56:59", "0:7") E1(10) E1(11) E1(12) E1(13) E1(14) E1(15) \
  E1(16) E1(17) PVM("60:63", "8:15") E1(18) RDL("56:59", "v78", #t "+2", "1024") E1(19) RDL("60:63", "v79", #t "+2", "1280") \
  E1(20) "s_waitcnt lgkmcnt(3)\n\t" E1(21) QKM("0:15", "48:51") E1(22) RDL("48:51", "v77", #t "+4", "0") E1(23) E1(24) E1(25) \
  PVM("64:67", "16:23") E1(26) E1(27) E1(28) E1(29) E1(30) E1(31)

__global__ void __launch_bounds__(512, 2)
fused(const __bf16* __restrict__ x, const __bf16* __restrict__ wqkv, const __bf16* __restrict__ woA,
      const __bf16* __restrict__ wq, const __bf16* __restrict__ wk, const __bf16* __restrict__ wv,
      const __bf16* __restrict__ woB, const __bf16* __restrict__ gw, u8* __restrict__ ws, __bf16* __restrict__ out) {
  __shared__ __attribute__((aligned(1024))) u8 lds[L_TOT];
  asm volatile("" :: "v"((lds_u8*)lds), "s"(x), "s"(wqkv), "s"(wq), "s"(wk), "s"(wv), "s"(gw), "s"(ws), "s"(woA), "s"(woB), "s"(out) : "memory");
  const int lane = threadIdx.x & 63;
  const int wid = __builtin_amdgcn_readfirstlane(threadIdx.x >> 6);
  const int bid = blockIdx.x, xcd = bid & 7, bi = bid >> 3;
  const int tb = (xcd & 1) * 8 + (bi & 7);
  const int hp = (xcd >> 1) * 4 + (bi >> 3);
  const int rot = bi + 2 * (bi >> 3);
  const int t0 = tb * 128;
  const int r32 = lane & 31, h = lane >> 5, r16 = lane & 15, g4 = lane >> 4;
  const __amdgpu_buffer_rsrc_t rws = __builtin_amdgcn_make_buffer_rsrc(ws, 0, WS_END, 0x00020000);
  STAMP(0);
  unsigned T;
  asm volatile("s_load_dword %0, %1, 0x800" : "=s"(T) : "s"(ws) : "memory");
  u32x4 aQ;
  const int w = wid & 3, np = w & 1, th = w >> 1;
  const unsigned ones = 0x3F803F80u;

  if (wid >= 4) {
    const int lw = w;
    const u32x4 rx = mkrsrc(x, 2048u * 1024u);
    const unsigned wstride = (lw == 3) ? 1024u : 3072u;
    const u32x4 rw = mkrsrc((lw == 3) ? wq : wqkv, 512u * wstride);
    const unsigned vw0 = (unsigned)(lane >> 2) * wstride + (unsigned)(((lane & 3) ^ ((lw & 1) << 1)) * 16) + hp * 64 + ((lw == 3) ? 0 : lw * 1024);
    const unsigned wd = 16u * wstride - 1024u;
    const unsigned vw1 = vw0 + wd, vw2 = vw1 + wd, vw3 = vw2 + wd;
    const int xr = lane >> 3;
    const unsigned vx0 = (unsigned)(t0 + lw * 32 + xr) * 1024u + (unsigned)(((lane & 7) ^ (xr >> 1)) * 16);
    const unsigned vx1 = (vx0 ^ 64u) + 7168u, vx2 = vx0 + 2u * 7168u, vx3 = vx1 + 2u * 7168u;
    int hrow = lw * 4 + (xr & 3), htok = t0 - 16 + hrow; htok = htok < 0 ? 0 : htok;
    const unsigned vh = (unsigned)htok * 1024u + (unsigned)(((lane & 7) ^ (hrow >> 1)) * 16);
    auto issue = [&](int s) {
      const unsigned base = (unsigned)(s & 3) * L_SLOT, sr = (unsigned)(s + rot) & 7u;
      asm volatile(
        "s_mov_b32 m0, %[mx]\n\ts_nop 0\n\t"
        "buffer_load_dwordx4 %[x0], %[rx], %[sx] offen lds\n\t"
        "buffer_load_dwordx4 %[x1], %[rx], %[sx] offen offset:1024 lds\n\t"
        "buffer_load_dwordx4 %[x2], %[rx], %[sx] offen offset:2048 lds\n\t"
        "buffer_load_dwordx4 %[x3], %[rx], %[sx] offen offset:3072 lds\n\t"
        "s_mov_b32 m0, %[mw]\n\ts_nop 0\n\t"
        "buffer_load_dwordx4 %[w0], %[rw], %[sw] offen lds\n\t"
        "buffer_load_dwordx4 %[w1], %[rw], %[sw] offen offset:1024 lds\n\t"
        "buffer_load_dwordx4 %[w2], %[rw], %[sw] offen offset:2048 lds\n\t"
        "buffer_load_dwordx4 %[w3], %[rw], %[sw] offen offset:3072 lds\n\t"
        "s_mov_b32 m0, %[mh]\n\ts_mov_b32 exec_hi, 0\n\t"
        "buffer_load_dwordx4 %[hh], %[rx], %[sx] offen lds\n\t"
        "s_mov_b32 exec_hi, -1\n\t"
        :: [mx] "s"(base + lw * 4096), [mw] "s"(base + L_SW + lw * 4096), [mh] "s"(base + L_SH + lw * 512),
           [x0] "v"(vx0), [x1] "v"(vx1), [x2] "v"(vx2), [x3] "v"(vx3), [w0] "v"(vw0), [w1] "v"(vw1), [w2] "v"(vw2), [w3] "v"(vw3),
           [hh] "v"(vh), [rx] "s"(rx), [rw] "s"(rw), [sx] "s"(sr * 128u), [sw] "s"(sr * 64u * wstride)
        : "memory");
    };
#pragma nounroll
    for (int s = 0; s < 3; ++s) issue(s);
    STAMP(1);
    f32x4 hacc = {0.f, 0.f, 0.f, 0.f}, gacc = {0.f, 0.f, 0.f, 0.f};
    const int hseg = 1 + (lw >> 1);
    const unsigned aw = L_SW + hseg * 4096 + (g4 * 8 + (r16 >> 2)) * 64 + (((lw & 1) ^ (hseg & 1)) * 32) + (lane & 3) * 8;
    const unsigned ah = L_SH + r16 * 128 + ((g4 ^ (r16 >> 1)) * 16);
    const int grow = (hp & 3) * 32 + (lw & 1) * 16 + r16;
    const unsigned ag = grow * 128 + ((((lw >> 1) * 4 + g4) ^ ((grow >> 1) & 7)) * 16);
    const unsigned agw = L_GW + (lw >> 1) * 64 + g4 * 16;
    auto work = [&](int s) {
      const unsigned st = (unsigned)(s & 3) * L_SLOT, sr = (unsigned)(s + rot) & 7u;
      bf16x8 wf0 = cat8(tr_read(st + aw), tr_read(st + aw + 256));
      bf16x8 wf1 = cat8(tr_read(st + aw + 2048), tr_read(st + aw + 2048 + 256));
      bf16x8 xf0 = lds_ld<bf16x8>(st + ah), xf1 = lds_ld<bf16x8>(st + (ah ^ 64u));
      bf16x8 gx = lds_ld<bf16x8>(st + ag), gf = lds_ld<bf16x8>(agw + sr * 128);
      hacc = __builtin_amdgcn_mfma_f32_16x16x32_bf16(wf0, xf0, hacc, 0, 0, 0);
      gacc = __builtin_amdgcn_mfma_f32_16x16x32_bf16(gf, gx, gacc, 0, 0, 0);
      hacc = __builtin_amdgcn_mfma_f32_16x16x32_bf16(wf1, xf1, hacc, 0, 0, 0);
    };
    const u32x4 rw4 = mkrsrc(ws, WS_END);
    u32x4 pol = {0u, 0u, 0u, 0u};
#pragma nounroll
    for (int s = 0; s < 6; ++s) {
      if (s < 5) asm volatile("s_waitcnt vmcnt(18)" ::: "memory");
      else asm volatile("s_waitcnt vmcnt(19)" ::: "memory");
      RAW_BARRIER();
      if (s == 4) asm volatile("buffer_load_dwordx4 %0, %1, %2, 0 offen sc1" : "+{v[100:103]}"(pol) : "v"(lane * 16), "s"(rw4) : "memory");
      if (s < 5) issue(s + 3);
      work(s);
    }
    asm volatile("" : "+s"(T));
    T = (T * 1664525u + 1013904223u) | 1u;
    asm volatile("s_waitcnt vmcnt(9)" : "+{v[100:103]}"(pol) :: "memory");
    RAW_BARRIER();
    STAMP(2);
    bool ok = __builtin_amdgcn_ballot_w64(pol[0] == T && pol[1] == T && pol[2] == T && pol[3] == T) == ~0ull;
    u32x4 kv[10];
    if (ok) {
#pragma unroll
      for (int i = 0; i < 10; ++i) kv[i] = __builtin_amdgcn_raw_buffer_load_b128(rws, lane * 16 + (i & 3) * 1024, WS_KV + lw * 10240 + (i >> 2) * 4096, SC1);
    }
    asm volatile("" ::: "memory");
    work(6);
    if (ok) asm volatile("s_waitcnt vmcnt(10)" ::: "memory");
    else asm volatile("s_waitcnt vmcnt(0)" ::: "memory");
    RAW_BARRIER();
    work(7);
    {
      u32x2 o; o[0] = pk_bf16(hacc[0], hacc[1]); o[1] = pk_bf16(hacc[2], hacc[3]);
      lds_st<u32x2>(L_AS + (((lw >> 1) * 2 + (lw & 1)) * 2 + (g4 & 1)) * PLANE + r16 * 16 + (g4 >> 1) * 8, o);
      if (g4 == 0) lds_st<float>(L_GP + (lw >> 1) * 128 + (lw & 1) * 64 + r16 * 4, gacc[0]);
      const unsigned c = (lane & 1) ? 0u : ones;
      if (lane < 10) lds_st<u32x4>((lw * 5 + (lane >> 1)) * KVS + 2048 + (lane & 1) * 16, (u32x4){c, c, c, c});
    }
    if (!ok) {
#pragma nounroll
      for (int it = 0; it < 200000; ++it) {
        asm volatile("" ::: "memory");
        u32x4 p = __builtin_amdgcn_raw_buffer_load_b128(rws, lane * 16, WS_FLAG1, SC1);
        if (__builtin_amdgcn_ballot_w64(p[0] == T && p[1] == T && p[2] == T && p[3] == T) == ~0ull) break;
        __builtin_amdgcn_s_sleep(1);
      }
      asm volatile("" ::: "memory");
      STAMP(3);
#pragma unroll
      for (int i = 0; i < 10; ++i) kv[i] = __builtin_amdgcn_raw_buffer_load_b128(rws, lane * 16 + (i & 3) * 1024, WS_KV + lw * 10240 + (i >> 2) * 4096, SC1);
    }
    if (bid == 0 && threadIdx.x == 256) __builtin_amdgcn_raw_buffer_store_b32(T, rws, 0, WS_EPOCH, SC1);
#pragma unroll
    for (int i = 0; i < 10; ++i) lds_st<u32x4>(lw * (5 * KVS) + (i >> 1) * KVS + (i & 1) * 1024 + lane * 16, kv[i]);
    STAMP(4);
    RAW_BARRIER();
    __builtin_amdgcn_s_setprio(1);
    aQ = lds_ld<u32x4>(L_QB + lw * 1024 + lane * 16);
  } else {
    {
      const u32x4 rex = mkrsrc(gw, 1024u);
      const u32x4 rkv = mkrsrc((hp & 1) ? wv : wk, 16384u);
      const u32x4 rx = mkrsrc(x, 2048u * 1024u);
      const unsigned vxo = (unsigned)(t0 + (hp >> 1) * 16 + r16) * 1024u + g4 * 16;
      u32x4 xe0, xe1, xe2, xe3;
      asm volatile(
        "s_mov_b32 m0, %[mk]\n\ts_nop 0\n\t"
        "buffer_load_dwordx4 %[l16], %[rkv], %[sk] offen lds\n\t"
        "buffer_load_dwordx4 %[l16], %[rkv], %[sk] offen offset:1024 lds\n\t"
        "buffer_load_dwordx4 %[l16], %[rkv], %[sk] offen offset:2048 lds\n\t"
        "buffer_load_dwordx4 %[l16], %[rkv], %[sk] offen offset:3072 lds\n\t"
        "buffer_load_dwordx4 %[x0], %[vx], %[rx], %[sx] offen\n\t"
        "buffer_load_dwordx4 %[x1], %[vx], %[rx], %[sx] offen offset:64\n\t"
        "buffer_load_dwordx4 %[x2], %[vx], %[rx], %[sx] offen offset:128\n\t"
        "buffer_load_dwordx4 %[x3], %[vx], %[rx], %[sx] offen offset:192\n\t"
        "s_mov_b32 m0, %[mex]\n\ts_nop 0\n\t"
        "buffer_load_dword %[l4], %[rex], %[sx] offen lds\n\t"
        : [x0] "=&v"(xe0), [x1] "=&v"(xe1), [x2] "=&v"(xe2), [x3] "=&v"(xe3)
        : [mex] "s"(L_GW + w * 256), [mk] "s"(L_EW + w * 4096), [l16] "v"(lane * 16), [l4] "v"(lane * 4), [rex] "s"(rex), [rkv] "s"(rkv),
          [sk] "s"(w * 4096), [vx] "v"(vxo), [rx] "s"(rx), [sx] "s"(w * 256)
        : "memory");
      if (w == 0 && lane < 2) lds_st<unsigned>(L_CNT + lane * 4, 0u);
      asm volatile("s_waitcnt vmcnt(0)" : "+v"(xe0), "+v"(xe1), "+v"(xe2), "+v"(xe3) :: "memory");
      STAMP(1);
      const unsigned ak = L_EW + w * 4096 + (g4 * 8 + (r16 >> 2)) * 32 + (lane & 3) * 8;
      bf16x8 kf0 = cat8(tr_read(ak), tr_read(ak + 128)), kf1 = cat8(tr_read(ak + 1024), tr_read(ak + 1152));
      bf16x8 kf2 = cat8(tr_read(ak + 2048), tr_read(ak + 2176)), kf3 = cat8(tr_read(ak + 3072), tr_read(ak + 3200));
      f32x4 pa = {0.f, 0.f, 0.f, 0.f};
      if (hp & 1) {
        pa = __builtin_amdgcn_mfma_f32_16x16x32_bf16(__builtin_bit_cast(bf16x8, xe0), kf0, pa, 0, 0, 0);
        pa = __builtin_amdgcn_mfma_f32_16x16x32_bf16(__builtin_bit_cast(bf16x8, xe1), kf1, pa, 0, 0, 0);
        pa = __builtin_amdgcn_mfma_f32_16x16x32_bf16(__builtin_bit_cast(bf16x8, xe2), kf2, pa, 0, 0, 0);
        pa = __builtin_amdgcn_mfma_f32_16x16x32_bf16(__builtin_bit_cast(bf16x8, xe3), kf3, pa, 0, 0, 0);
      } else {
        pa = __builtin_amdgcn_mfma_f32_16x16x32_bf16(kf0, __builtin_bit_cast(bf16x8, xe0), pa, 0, 0, 0);
        pa = __builtin_amdgcn_mfma_f32_16x16x32_bf16(kf1, __builtin_bit_cast(bf16x8, xe1), pa, 0, 0, 0);
        pa = __builtin_amdgcn_mfma_f32_16x16x32_bf16(kf2, __builtin_bit_cast(bf16x8, xe2), pa, 0, 0, 0);
        pa = __builtin_amdgcn_mfma_f32_16x16x32_bf16(kf3, __builtin_bit_cast(bf16x8, xe3), pa, 0, 0, 0);
      }
      lds_st<f32x4>(L_EW + w * 4096 + lane * 16, pa);
    }
    const unsigned aw = L_SW + (g4 & 1) * 4096 + (h * 8 + (r16 >> 2)) * 64 + ((np ^ (g4 & 1)) * 32) + (lane & 3) * 8;
    const int xrow = th * 64 + r32;
    const unsigned ax = xrow * 128 + ((h ^ ((xrow >> 1) & 7)) * 16);
    f32x16 acc[2][2];
#pragma unroll
    for (int a = 0; a < 2; ++a)
#pragma unroll
      for (int b = 0; b < 2; ++b)
#pragma unroll
        for (int i = 0; i < 16; ++i) acc[a][b][i] = 0.f;
    bf16x8 wf[2][2], xf[2][2];
    auto ldf = [&](int p, unsigned st, int ks) {
#pragma unroll
      for (int a = 0; a < 2; ++a) {
        wf[p][a] = cat8(tr_read(st + aw + a * 8192 + ks * 1024), tr_read(st + aw + a * 8192 + ks * 1024 + 256));
        xf[p][a] = lds_ld<bf16x8>(st + (ax ^ (ks * 32)) + a * 4096);
      }
    };
    auto mm = [&](int p) {
#pragma unroll
      for (int a = 0; a < 2; ++a)
#pragma unroll
        for (int b = 0; b < 2; ++b) acc[a][b] = __builtin_amdgcn_mfma_f32_32x32x16_bf16(wf[p][a], xf[p][b], acc[a][b], 0, 0, 0);
    };
    RAW_BARRIER();
    STAMP(2);
    asm volatile("" : "+s"(T));
    T = (T * 1664525u + 1013904223u) | 1u;
    if (w == 0) {
      f32x4 p0 = lds_ld<f32x4>(L_EW + lane * 16), p1 = lds_ld<f32x4>(L_EW + 4096 + lane * 16);
      f32x4 p2 = lds_ld<f32x4>(L_EW + 8192 + lane * 16), p3 = lds_ld<f32x4>(L_EW + 12288 + lane * 16);
      f32x4 p = ((p0 + p1) + (p2 + p3)) * ((hp & 1) ? 1.0f : 0.25f * LOG2E);
      u32x2 o; o[0] = pk_bf16(p[0], p[1]); o[1] = pk_bf16(p[2], p[3]);
      const int blk = tb * 8 + (hp >> 1), sp = blk & 1;
      const unsigned d = (hp & 1) ? 1024 + (g4 >> 1) * 512 + sp * 256 + r16 * 16 + (g4 & 1) * 8 : ((g4 & 1) * 32 + sp * 16 + r16) * 16 + (g4 >> 1) * 8;
      __builtin_amdgcn_raw_buffer_store_b64(o, rws, d, WS_KV + (blk >> 1) * 2048, SC1);
    }
    ldf(0, 0, 0);
#pragma nounroll
    for (int s = 0; s < 8; ++s) {
      const unsigned st = (unsigned)(s & 3) * L_SLOT, sn = (unsigned)((s + 1) & 3) * L_SLOT;
      ldf(1, st, 1); SCHED_FENCE(); mm(0); SCHED_FENCE();
      ldf(0, st, 2); SCHED_FENCE(); mm(1); SCHED_FENCE();
      ldf(1, st, 3); SCHED_FENCE(); mm(0); SCHED_FENCE();
      if (s == 1 && w == 0) {
        asm volatile("s_waitcnt vmcnt(0)" ::: "memory");
        if (lane == 0) __builtin_amdgcn_raw_buffer_store_b32(T, rws, bid * 4, WS_FLAG1, SC1);
        STAMP(3);
      }
      __builtin_amdgcn_s_waitcnt(0xC07F);
      if (s < 7) RAW_BARRIER();
      ldf(0, sn, 0);
      SCHED_FENCE(); mm(1); SCHED_FENCE();
    }
    STAMP(4);
    u32x4 qa[2];
#pragma unroll
    for (int b = 0; b < 2; ++b) {
      u32x4 kk, vv;
#pragma unroll
      for (int j = 0; j < 4; ++j) {
        qa[b][j] = pk_bf16(acc[0][b][2 * j], acc[0][b][2 * j + 1]);
        kk[j] = pk_bf16(acc[0][b][8 + 2 * j], acc[0][b][9 + 2 * j]);
        vv[j] = pk_bf16(acc[1][b][2 * j], acc[1][b][2 * j + 1]);
      }
      lds_st<u32x4>(L_AS + (np * 2 + h) * PLANE + (xrow + b * 32 + 16) * 16, kk);
      lds_st<u32x4>(L_AS + (4 + np * 2 + h) * PLANE + (xrow + b * 32 + 16) * 16, vv);
    }
    {
      u32x4 qo;
#pragma unroll
      for (int j = 0; j < 4; ++j) {
        const unsigned q0 = pk_bf16(acc[1][0][8 + 2 * j], acc[1][0][9 + 2 * j]), q1 = pk_bf16(acc[1][1][8 + 2 * j], acc[1][1][9 + 2 * j]);
        aQ[j] = np ? q1 : q0; qo[j] = np ? q0 : q1;
      }
      lds_st<u32x4>(L_QB + w * 1024 + lane * 16, qo);
    }
    RAW_BARRIER();
    __builtin_amdgcn_s_setprio(2);
    STAMP(5);
    u32x4 kv[22];
#pragma unroll
    for (int i = 0; i < 22; ++i) kv[i] = __builtin_amdgcn_raw_buffer_load_b128(rws, lane * 16 + (i & 3) * 1024, WS_KV + 40960 + w * 22528 + (i >> 2) * 4096, SC1);
    asm volatile("" ::: "memory");
#pragma unroll
    for (int b = 0; b < 2; ++b) {
      const unsigned ka = L_AS + (np * 2 + h) * PLANE + (xrow + b * 32 + 2) * 16;
      float s[8];
#pragma unroll
      for (int i = 0; i < 8; ++i) {
        u32x4 k = lds_ld<u32x4>(ka + (7 - i) * 32);
        float a = 0.f;
#pragma unroll
        for (int j = 0; j < 4; ++j) a = dot2bf(qa[b][j], k[j], a);
        s[i] = a;
      }
      float e[4], mx = -3.0e38f;
      const int tok = t0 + xrow + b * 32 - 8 * h;
#pragma unroll
      for (int i = 0; i < 4; ++i) {
        auto r = __builtin_amdgcn_permlane32_swap(__builtin_bit_cast(unsigned, s[i]), __builtin_bit_cast(unsigned, s[i + 4]), false, false);
        float t = (swap_lo(r[0]) + swap_lo(r[1])) * (0.25f * LOG2E);
        t = (tok - 2 * i >= 0) ? t : -1.4426950e9f;
        e[i] = t; mx = fmaxf(mx, t);
      }
      { auto r = __builtin_amdgcn_permlane32_swap(__builtin_bit_cast(unsigned, mx), __builtin_bit_cast(unsigned, mx), false, false);
        mx = fmaxf(swap_lo(r[0]), swap_lo(r[1])); }
      float sum = 0.f;
#pragma unroll
      for (int i = 0; i < 4; ++i) { e[i] = __builtin_amdgcn_exp2f(e[i] - mx); sum += e[i]; }
      { auto r = __builtin_amdgcn_permlane32_swap(__builtin_bit_cast(unsigned, sum), __builtin_bit_cast(unsigned, sum), false, false);
        sum = swap_lo(r[0]) + swap_lo(r[1]); }
      const float inv = __builtin_amdgcn_rcpf(sum);
      float o[8];
#pragma unroll
      for (int j = 0; j < 8; ++j) o[j] = 0.f;
#pragma unroll
      for (int i = 0; i < 4; ++i) {
        const unsigned pl = pk_bf16(e[i] * inv, 0.f);
        auto r = __builtin_amdgcn_permlane32_swap(pl, pl, false, false);
#pragma unroll
        for (int c = 0; c < 2; ++c) {
          u32x4 v = lds_ld<u32x4>(ka + 4 * PLANE + (7 - i - 4 * c) * 32);
          const unsigned plo = r[c], phi = plo << 16;
#pragma unroll
          for (int j = 0; j < 4; ++j) { o[2 * j] = dot2bf(v[j], plo, o[2 * j]); o[2 * j + 1] = dot2bf(v[j], phi, o[2 * j + 1]); }
        }
      }
      u32x4 ov;
#pragma unroll
      for (int j = 0; j < 4; ++j) ov[j] = pk_bf16(o[2 * j], o[2 * j + 1]);
      ST128(ov, lane * 16, WS_OA + ((tb * 4 + th * 2 + b) * 32 + hp * 2 + np) * 1024);
    }
    {
      const u32x4 z4 = {0u, 0u, 0u, 0u};
#pragma unroll
      for (int i = 0; i < 4; ++i) ST128(z4, lane * 16 + i * 1024, WS_OB + (tb * 4 + w) * 65536 + hp * 4096);
    }
    STAMP(6);
#pragma unroll
    for (int i = 0; i < 22; ++i) lds_st<u32x4>((20 + w * 11) * KVS + (i >> 1) * KVS + (i & 1) * 1024 + lane * 16, kv[i]);
    {
      const unsigned c = (lane & 1) ? 0u : ones;
      if (lane < 22) lds_st<u32x4>((20 + w * 11 + (lane >> 1)) * KVS + 2048 + (lane & 1) * 16, (u32x4){c, c, c, c});
    }
    __builtin_amdgcn_s_setprio(0);
    RAW_BARRIER();
    asm volatile("s_waitcnt vmcnt(0)" ::: "memory");
    if (lane == 0) {
      unsigned old = __hip_atomic_fetch_add((__attribute__((address_space(3))) unsigned*)(lds_u8*)(unsigned long long)L_CNT, 1u, __ATOMIC_RELAXED, __HIP_MEMORY_SCOPE_WORKGROUP);
      if (old == 3u) __builtin_amdgcn_raw_buffer_store_b32(T, rws, (tb * 16 + hp) * 4, WS_FLAG2, SC1);
    }
  }

  const int tt = th * 2 + ((wid >> 2) ^ np);
  f32x16 D;
  const bool hi = (lane & 16) != 0, on = (lane & 27) == 16;
  const unsigned aK = lane * 16, aV0 = hi ? (on ? 1024u : 1040u) : aK, aV1 = hi ? aV0 - 256u : aK;
  STAMP(7);
  {
    f32x16 aX, aY;
    u32x16 rg0; u32x8 rg1; u32x4 ad = {0xddddddddu, aK, aV0, aV1};
#pragma unroll
    for (int i = 0; i < 16; ++i) { aY[i] = 0.f; D[i] = 0.f; }
#pragma unroll
    for (int i = 0; i < 8; ++i) rg1[i] = 0u;
    unsigned cnt = 4u;
    asm volatile(
      RDL("48:51", "v77", "0", "0") RDL("52:55", "v77", "1", "0") RDL("56:59", "v78", "0", "1024") RDL("60:63", "v79", "0", "1280")
      "s_waitcnt lgkmcnt(3)\n\t"
      QKM("0:15", "48:51")
      "s_nop 1\n\t"
      RDL("48:51", "v77", "2", "0")
      "s_nop 9\n\t"
      "1:\n\t"
      PAIR(0) PAIR(2) PAIR(4) PAIR(6) PAIR(8) PAIR(10) PAIR(12) PAIR(14)
      "v_add_u32 v77, 0x8200, v77\n\t"
      "v_add_u32 v78, 0x8200, v78\n\t"
      "v_add_u32 v79, 0x8200, v79\n\t"
      "s_cmp_lg_u32 %[cnt], %[bar]\n\t"
      "s_cbranch_scc1 2f\n\t"
      "s_barrier\n\t"
      "2:\n\t"
      "s_sub_u32 %[cnt], %[cnt], 1\n\t"
      "s_cmp_lg_u32 %[cnt], 0\n\t"
      "s_cbranch_scc1 1b\n\t"
      PVM("68:71", "24:31")
      "s_waitcnt lgkmcnt(0)\n\ts_nop 11\n\t"
      : "=&{v[0:15]}"(aX), "+{v[16:31]}"(aY), "+{v[32:47]}"(D), "=&{v[48:63]}"(rg0), "+{v[64:71]}"(rg1), "+{v[76:79]}"(ad), [cnt] "+s"(cnt)
      : "{v[72:75]}"(aQ), [bar] "s"(wid & 4) : "memory", "scc");
  }
  STAMP(9);
  float o[8], l = D[8];
#pragma unroll
  for (int j = 0; j < 8; ++j) o[j] = D[j];
  bool bad = !(l >= 5.4e-20f && l < 1.0e36f);
#pragma unroll
  for (int j = 0; j < 8; ++j) bad = bad || bad_f(o[j]);
  if (__builtin_amdgcn_ballot_w64(bad) != 0ull) {
    const bf16x8 qf = __builtin_bit_cast(bf16x8, aQ);
    f32x16 z;
#pragma unroll
    for (int i = 0; i < 16; ++i) z[i] = 0.f;
    float m = -3.0e38f;
#pragma nounroll
    for (int t = 0; t < 64; ++t) {
      f32x16 s = __builtin_amdgcn_mfma_f32_32x32x16_bf16(lds_ld<bf16x8>(aK + t * KVS), qf, z, 0, 0, 0);
#pragma unroll
      for (int i = 0; i < 16; ++i) m = fmaxf(m, s[i]);
    }
    { auto r = __builtin_amdgcn_permlane32_swap(__builtin_bit_cast(unsigned, m), __builtin_bit_cast(unsigned, m), false, false);
      m = fmaxf(swap_lo(r[0]), swap_lo(r[1])); }
    f32x16 F = z;
#pragma nounroll
    for (int t = 0; t < 64; ++t) {
      f32x16 s = __builtin_amdgcn_mfma_f32_32x32x16_bf16(lds_ld<bf16x8>(aK + t * KVS), qf, z, 0, 0, 0);
      f32x8 p0, p1;
#pragma unroll
      for (int i = 0; i < 8; ++i) { p0[i] = __builtin_amdgcn_exp2f(s[i] - m); p1[i] = __builtin_amdgcn_exp2f(s[8 + i] - m); }
      F = __builtin_amdgcn_smfmac_f32_32x32x32_bf16(lds_ld<bf16x8>(aV0 + 1024 + t * KVS), __builtin_bit_cast(bf16x16, p0), F, 0xDDDD, 0, 0);
      F = __builtin_amdgcn_smfmac_f32_32x32x32_bf16(lds_ld<bf16x8>(aV1 + 1280 + t * KVS), __builtin_bit_cast(bf16x16, p1), F, 0xDDDD, 0, 0);
    }
#pragma unroll
    for (int j = 0; j < 8; ++j) o[j] = F[j];
    l = F[8];
  }
  {
#pragma nounroll
    for (int it = 0; it < 200000; ++it) {
      asm volatile("" ::: "memory");
      if (__builtin_amdgcn_readfirstlane(lds_ld<unsigned>(L_CNT)) == 4u) break;
      __builtin_amdgcn_s_sleep(1);
    }
    const float inv = __builtin_amdgcn_rcpf(l);
    const unsigned so = WS_OB + ((tb * 4 + tt) * 32 + hp * 2 + np) * 2048;
    u32x4 g0 = {pk_bf16(o[0] * inv, o[1] * inv), T, pk_bf16(o[2] * inv, o[3] * inv), T};
    u32x4 g1 = {pk_bf16(o[4] * inv, o[5] * inv), T, pk_bf16(o[6] * inv, o[7] * inv), T};
    ST128(g0, lane * 16, so);
    ST128(g1, lane * 16 + 1024, so);
  }
  STAMP(10);
  if (wid < 4) return;

  const int ct = tb * 4 + (hp & 3);
  const int nt = (hp >> 2) * 4 + w;
  const unsigned vwo = (unsigned)(lane >> 2) * 1024u + (lane & 3) * 16 + nt * 64;
  const unsigned tw = L_TW + w * 3072;
  const unsigned ao = tw + (h * 4 + (r16 >> 2)) * 64 + (lane & 1) * 32 + ((lane >> 1) & 1) * 8 + (g4 & 1) * 16;
  f32x16 accA, accB;
#pragma unroll
  for (int i = 0; i < 16; ++i) { accA[i] = 0.f; accB[i] = 0.f; }
#pragma nounroll
  for (int it = 0; it < 200000; ++it) {
    asm volatile("" ::: "memory");
    unsigned p = __builtin_amdgcn_raw_buffer_load_b32(rws, r16 * 4, WS_FLAG2 + tb * 64, SC1);
    if (__builtin_amdgcn_ballot_w64(p == T) == ~0ull) break;
    __builtin_amdgcn_s_sleep(8);
  }
  asm volatile("" ::: "memory");
  STAMP(8);
  {
    const __amdgpu_buffer_rsrc_t ra = __builtin_amdgcn_make_buffer_rsrc((void*)woA, 0, 512u * 1024u, 0x00020000);
#pragma nounroll
    for (int p = 0; p < 2; ++p) {
      u32x4 rw[16], fo[16];
#pragma unroll
      for (int i = 0; i < 16; ++i) {
        rw[i] = __builtin_amdgcn_raw_buffer_load_b128(ra, vwo, (p * 16 + i) * 16384, 0);
        fo[i] = __builtin_amdgcn_raw_buffer_load_b128(rws, lane * 16 + (i & 3) * 1024, WS_OA + (ct * 32 + p * 16) * 1024 + (i >> 2) * 4096, SC1);
      }
      SCHED_FENCE();
#pragma unroll
      for (int i = 0; i < 16; ++i) {
        lds_st<u32x4>(tw + (i % 3) * 1024 + lane * 16, rw[i]);
        bf16x8 f = cat8(tr_read(ao + (i % 3) * 1024), tr_read(ao + (i % 3) * 1024 + 512));
        accA = __builtin_amdgcn_mfma_f32_32x32x16_bf16(f, __builtin_bit_cast(bf16x8, fo[i]), accA, 0, 0, 0);
      }
    }
  }
  const float gz = lds_ld<float>(L_GP + r32 * 4) + lds_ld<float>(L_GP + 128 + r32 * 4);
  const float g = __builtin_amdgcn_rcpf(1.f + __builtin_amdgcn_exp2f(-gz * LOG2E));
#pragma unroll
  for (int i = 0; i < 16; ++i) accA[i] *= g;
  STAMP(11);
  {
    const __amdgpu_buffer_rsrc_t rb = __builtin_amdgcn_make_buffer_rsrc((void*)woB, 0, 512u * 1024u, 0x00020000);
#pragma nounroll
    for (int p = 0; p < 2; ++p) {
      const int par = (ct ^ p ^ 1) & 1;
      bf16x8 fw[16];
      {
        u32x4 rw[16];
#pragma unroll
        for (int i = 0; i < 16; ++i) rw[i] = __builtin_amdgcn_raw_buffer_load_b128(rb, vwo, (2 * i + par) * 16384, 0);
        SCHED_FENCE();
#pragma unroll
        for (int i = 0; i < 16; ++i) {
          lds_st<u32x4>(tw + (i % 3) * 1024 + lane * 16, rw[i]);
          fw[i] = cat8(tr_read(ao + (i % 3) * 1024), tr_read(ao + (i % 3) * 1024 + 512));
        }
      }
      if (p) RAW_BARRIER();
      const unsigned ga = p ? 0u : L_GA0;
      const unsigned so = WS_OB + (ct * 32 + w * 8 + par) * 2048;
      u32x4 gr[8];
#pragma nounroll
      for (int it = 0; it < 200000; ++it) {
        asm volatile("" ::: "memory");
#pragma unroll
        for (int i = 0; i < 8; ++i) gr[i] = __builtin_amdgcn_raw_buffer_load_b128(rws, lane * 16 + (i & 1) * 1024, so + (i >> 1) * 4096, SC1);
        unsigned x1 = 0u;
#pragma unroll
        for (int i = 0; i < 8; ++i) x1 |= (gr[i][1] ^ T) | (gr[i][3] ^ T);
        if (__builtin_amdgcn_ballot_w64(x1 != 0u) == 0ull) break;
        __builtin_amdgcn_s_sleep(2);
      }
      if (p) STAMP(12);
#pragma unroll
      for (int i = 0; i < 4; ++i)
        lds_st<u32x4>(ga + (w * 4 + i) * 1024 + lane * 16, (u32x4){gr[2 * i][0], gr[2 * i][2], gr[2 * i + 1][0], gr[2 * i + 1][2]});
      if (p) RAW_BARRIER();
      else {
        __builtin_amdgcn_s_waitcnt(0xC07F);
        if (lane == 0) __hip_atomic_fetch_add((__attribute__((address_space(3))) unsigned*)(lds_u8*)(unsigned long long)(L_CNT + 4), 1u, __ATOMIC_RELAXED, __HIP_MEMORY_SCOPE_WORKGROUP);
#pragma nounroll
        for (int it = 0; it < 2000000; ++it) {
          asm volatile("" ::: "memory");
          if (__builtin_amdgcn_readfirstlane(lds_ld<unsigned>(L_CNT + 4)) == 4u) break;
          __builtin_amdgcn_s_sleep(2);
        }
        asm volatile("" ::: "memory");
      }
#pragma unroll
      for (int i = 0; i < 16; ++i)
        accB = __builtin_amdgcn_mfma_f32_32x32x16_bf16(fw[i], lds_ld<bf16x8>(ga + i * 1024 + lane * 16), accB, 0, 0, 0);
      if (!p) STAMP(13);
    }
  }
  {
    u32x4 o0, o1;
#pragma unroll
    for (int j = 0; j < 4; ++j) {
      o0[j] = pk_bf16(accA[2 * j] + (1.f - g) * accB[2 * j], accA[2 * j + 1] + (1.f - g) * accB[2 * j + 1]);
      o1[j] = pk_bf16(accA[8 + 2 * j] + (1.f - g) * accB[8 + 2 * j], accA[9 + 2 * j] + (1.f - g) * accB[9 + 2 * j]);
    }
    __bf16* dst = out + (size_t)(ct * 32 + r32) * 512 + nt * 32 + h * 16;
    *(u32x4*)dst = o0;
    *(u32x4*)(dst + 8) = o1;
  }
  STAMP(14);
}

extern "C" void kernel_launch(void* const* d_in, const int* in_sizes, int n_in,
                              void* d_out, int out_size, void* d_ws, size_t ws_size,
                              hipStream_t stream) {
  fused<<<256, 512, 0, stream>>>((const __bf16*)d_in[0], (const __bf16*)d_in[1], (const __bf16*)d_in[2], (const __bf16*)d_in[4],
                                 (const __bf16*)d_in[5], (const __bf16*)d_in[6], (const __bf16*)d_in[7], (const __bf16*)d_in[8],
                                 (u8*)d_ws, (__bf16*)d_out);
}
```

```cpp
#include <hip/hip_runtime.h>
#include <hip/hip_bf16.h>

typedef __bf16 bf16x2 __attribute__((ext_vector_type(2)));
typedef __bf16 bf16x4 __attribute__((ext_vector_type(4)));
typedef __bf16 bf16x8 __attribute__((ext_vector_type(8)));
typedef __bf16 bf16x16 __attribute__((ext_vector_type(16)));
typedef short s16x4 __attribute__((ext_vector_type(4)));
typedef float f32x4 __attribute__((ext_vector_type(4)));
typedef float f32x8 __attribute__((ext_vector_type(8)));
typedef float f32x16 __attribute__((ext_vector_type(16)));
typedef unsigned u32x2 __attribute__((ext_vector_type(2)));
typedef unsigned u32x4 __attribute__((ext_vector_type(4)));
typedef unsigned u32x8 __attribute__((ext_vector_type(8)));
typedef unsigned u32x16 __attribute__((ext_vector_type(16)));
typedef unsigned char u8;
typedef __attribute__((address_space(3))) u8 lds_u8;

#define STR2(x) #x
#define STR(x) STR2(x)
#define SLP 2
#define LOG2E 1.4426950408889634f
#define SC1 16
#define PRIO_C 2
#define PRIO_L 1

#define WS_FLAG1 0u
#define WS_EPOCH 2048u
#define WS_KV    (64u << 10)
#define WS_KT    (256u << 10)
#define WS_WF    (1u << 20)
#define WS_HL    (2u << 20)
#define WS_O     (4u << 20)
#define WS_END   (12u << 20)
#define TS(i) do {} while (0)
#define TSG(o) ""
#define TSDUMP() do {} while (0)
#define TS0() do {} while (0)
#define TSI() unsigned tsv = 0;

#define C_KV   0
#define C_LP   4
#define C_PL   8
#define C_G    12
#define C_PG   16
#define L_Z    96
#define L_GP   128
#define L_GW   1024
#define L_KV   2048
#define KVS    2080
#define L_EW   2048
#define L_TW   18432
#define L_X(j) (49152u + ((j) << 13))
#define L_WQ(j) (32768u + ((81920u + ((j) << 13)) & 131071u))
#define L_RING 114688
#define L_PL   135168
#define L_GA   2048
#define L_TOT  163840

__device__ __forceinline__ unsigned pk_bf16(float lo, float hi) {
  bf16x2 v; v[0] = (__bf16)lo; v[1] = (__bf16)hi;
  return __builtin_bit_cast(unsigned, v);
}
__device__ __forceinline__ float dot2bf(unsigned a, unsigned b, float c) {
  return __builtin_amdgcn_fdot2_f32_bf16(__builtin_bit_cast(bf16x2, a), __builtin_bit_cast(bf16x2, b), c, false);
}
__device__ __forceinline__ u32x4 mkrsrc(const void* p, unsigned bytes) {
  unsigned long long a = (unsigned long long)p;
  u32x4 r;
  r[0] = (unsigned)a; r[1] = (unsigned)(a >> 32) & 0xffffu; r[2] = bytes; r[3] = 0x00020000u;
  return r;
}
__device__ __forceinline__ bf16x4 tr_read(unsigned a) {
  return __builtin_bit_cast(bf16x4, __builtin_amdgcn_ds_read_tr16_b64_v4i16((__attribute__((address_space(3))) s16x4*)(lds_u8*)(unsigned long long)a));
}
__device__ __forceinline__ bf16x8 cat8(bf16x4 a, bf16x4 b) { return __builtin_shufflevector(a, b, 0, 1, 2, 3, 4, 5, 6, 7); }
template <typename T> __device__ __forceinline__ T lds_ld(unsigned a) { return *(__attribute__((address_space(3))) T*)(lds_u8*)(unsigned long long)a; }
template <typename T> __device__ __forceinline__ void lds_st(unsigned a, T v) { *(__attribute__((address_space(3))) T*)(lds_u8*)(unsigned long long)a = v; }
#define RAW_BARRIER() do { __builtin_amdgcn_s_waitcnt(0xC07F); asm volatile("s_waitcnt lgkmcnt(0)\n\ts_barrier" ::: "memory"); } while (0)
#define VMW(n) asm volatile("s_waitcnt vmcnt(" #n ")" ::: "memory")
#define ST128(d, vo, so) __builtin_amdgcn_raw_buffer_store_b128((d), rws, (vo) + (so), 0, SC1)
#define SCHED_FENCE() __builtin_amdgcn_sched_barrier(0)
__device__ __forceinline__ float swap_lo(unsigned a) { return __builtin_bit_cast(float, a); }
__device__ __forceinline__ bool bad_f(float v) { return !(__builtin_fabsf(v) < 3.0e38f); }
__device__ __forceinline__ void post(unsigned a) {
  __builtin_amdgcn_s_waitcnt(0xC07F);
  asm volatile("s_waitcnt lgkmcnt(0)\n\ts_mov_b64 exec, 1\n\tds_add_u32 %0, %1\n\ts_mov_b64 exec, -1" :: "v"(a), "v"(1u) : "memory");
}
template <int SLEEP> __device__ __forceinline__ void wait_ge(unsigned a, unsigned target) {
  unsigned t, v, spin = 4000000u;
  asm volatile("1:\n\tds_read_b32 %1, %3\n\ts_waitcnt lgkmcnt(0)\n\tv_readfirstlane_b32 %0, %1\n\ts_cmp_ge_u32 %0, %4\n\ts_cbranch_scc1 2f\n\t"
               ".if %5\n\ts_sleep %5\n\t.endif\n\ts_sub_u32 %2, %2, 1\n\ts_cbranch_scc0 1b\n\t2:"
               : "=&s"(t), "=&v"(v), "+s"(spin) : "v"(a), "s"(target), "n"(SLEEP) : "memory", "scc");
}
#define DMA2(m, v0, v1, rs, so) asm volatile("s_mov_b32 m0, %0\n\ts_nop 0\n\t" \
    "buffer_load_dwordx4 %1, %3, %4 offen lds\n\tbuffer_load_dwordx4 %2, %3, %4 offen offset:1024 lds" \
    :: "s"(m), "v"(v0), "v"(v1), "s"(rs), "s"(so) : "memory")
__device__ __forceinline__ void wait_pg(unsigned target) {
  unsigned t, spin = 4000000u; u32x4 v;
  asm volatile("1:\n\tds_read_b128 v[124:127], %3\n\ts_waitcnt lgkmcnt(0)\n\tv_min3_u32 v124, v124, v125, v126\n\ts_nop 0\n\tv_readfirstlane_b32 %0, v124\n\t"
               "s_cmp_ge_u32 %0, %4\n\ts_cbranch_scc1 2f\n\ts_sleep " STR(SLP) "\n\ts_sub_u32 %2, %2, 1\n\ts_cbranch_scc0 1b\n\t2:"
               : "=&s"(t), "=&{v[124:127]}"(v), "+s"(spin) : "v"(C_PG), "s"(target) : "memory", "scc");
}
#define REP(n, body) ".set i_, 0\n\t.rept " #n "\n\t" body ".set i_, i_+1\n\t.endr\n\t"
#define GLDT(r, n, va, rs, so) REP(n, \
    "buffer_load_dwordx4 v[" #r "+4*i_:" #r "+4*i_+3], " va ", " rs ", " so " offen offset:(i_&3)*1024 sc1\n\t" \
    ".if (i_&3)==3\n\ts_add_u32 " so ", " so ", 0x1000\n\t.endif\n\t")
#define GLDN(r, n, so) do { unsigned so_ = (so); asm volatile(GLDT(r, n, "%1", "%2", "%0") : "+s"(so_) : "v"(l16), "s"(rw4) : "memory", "scc"); } while (0)
#define TAGT(r, n, d, t) "v_mov_b32 " d ", 0\n\t" REP(n, \
    "v_bitop3_b32 v[" #r "+8*i_+1], v[" #r "+8*i_+1], v[" #r "+8*i_+3], " t " bitop3:0x7e\n\t" \
    "v_bitop3_b32 v[" #r "+8*i_+5], v[" #r "+8*i_+5], v[" #r "+8*i_+7], " t " bitop3:0x7e\n\t" \
    "v_or3_b32 " d ", " d ", v[" #r "+8*i_+1], v[" #r "+8*i_+5]\n\t" \
    "v_mov_b32 v[" #r "+8*i_+1], v[" #r "+8*i_+2]\n\t" \
    "v_pk_mov_b32 v[" #r "+8*i_+2:" #r "+8*i_+3], v[" #r "+8*i_+4:" #r "+8*i_+5], v[" #r "+8*i_+6:" #r "+8*i_+7] op_sel:[0,0]\n\t")
#define TAGCHK(r, n, d) asm volatile(TAGT(r, n, "%0", "%1") : "=&v"(d) : "s"(T) : "memory")
#define KVPUT(r, st, n, a0, a1) asm volatile(REP(n, \
    ".if (i_&1)==0\n\tds_write_b128 %0, v[" #r "+" #st "*i_:" #r "+" #st "*i_+3] offset:(i_>>1)*2080\n\t" \
    ".else\n\tds_write_b128 %1, v[" #r "+" #st "*i_:" #r "+" #st "*i_+3] offset:(i_>>1)*2080\n\t.endif\n\t") :: "v"(a0), "v"(a1) : "memory")
#define GETT() asm volatile("s_waitcnt lgkmcnt(0)\n\ts_mul_i32 %0, s100, 1664525\n\ts_add_u32 %0, %0, 1013904223\n\ts_or_b32 %0, %0, 1" : "=s"(T) :: "memory", "scc")

#define E1(r) "v_exp_f32 v" #r ", v" #r "\n\t"
#define E4(a, b, c, d) E1(a) E1(b) E1(c) E1(d)
#define PVM(d, a, b) "v_smfmac_f32_32x32x32_bf16 v[" d "], v[" a "], v[" b "], v76\n\t"
#define QKM(d, k, q) "v_mfma_f32_32x32x16_bf16 v[" d "], v[" k "], v[" q "], 0\n\t"
#define RDL(d, a, t, c) "ds_read_b128 v[" d "], " a " offset:(" t ")*2080+" c "\n\t"
#define DUAL(t, Ka, V0a, V1a, Kb, V1b) \
  E1(0) E1(1) "s_waitcnt lgkmcnt(1)\n\t" PVM("80:95", V1b, "24:31") RDL(V1b, "v79", #t "+1", "1280") E4(2, 3, 4, 5) \
  QKM("16:31", Ka, "96:99") RDL(Ka, "v77", #t "+2", "0") E4(6, 7, 8, 9) \
  PVM("32:47", V0a, "0:7") E4(10, 11, 12, 13) E1(14) E1(15) \
  E1(16) E1(17) PVM("32:47", V1a, "8:15") E4(18, 19, 20, 21) \
  QKM("0:15", Kb, "72:75") E4(22, 23, 24, 25) \
  PVM("80:95", V0a, "16:23") RDL(V0a, "v78", #t "+2", "1024") E4(26, 27, 28, 29) E1(30) E1(31)
#define DUALX(t) DUAL(t, "48:51", "56:59", "60:63", "52:55", "68:71")
#define DUALY(t) DUAL(t, "52:55", "64:67", "68:71", "48:51", "60:63")
__device__ __forceinline__ int lane_id() {
  int l;
  asm volatile("v_mbcnt_lo_u32_b32 %0, -1, 0\n\tv_mbcnt_hi_u32_b32 %0, -1, %0" : "=v"(l));
  return l;
}
#define LANE_VARS \
  TSI() \
  const int lane = lane_id() & 63; \
  const int r32 = lane & 31, h = lane >> 5, r16 = lane & 15, g4 = lane >> 4, gs = g4 & 1; \
  const unsigned l16 = lane * 16; \
  const unsigned aK = L_KV + l16; \
  const unsigned ax = r32 * 128 + ((h ^ ((r32 >> 1) & 7)) * 16); \
  const unsigned awf = (h * 8 + (r16 >> 2)) * 128 + (r16 >> 3) * 64 + gs * 32 + (lane & 3) * 8; \
  const unsigned vsw = ((lane & 7) ^ (gs << 2)) * 16 + hg * 128;
#define GAP 7
#define GAPX() do { SCHED_FENCE(); asm volatile("s_nop " STR(GAP)); SCHED_FENCE(); } while (0)

__global__ void __launch_bounds__(512, 2) __attribute__((amdgpu_num_vgpr(64), amdgpu_num_sgpr(96)))
fused(const __bf16* __restrict__ x, const __bf16* __restrict__ wqkv, const __bf16* __restrict__ woA,
      const __bf16* __restrict__ wq, const __bf16* __restrict__ wk, const __bf16* __restrict__ wv,
      const __bf16* __restrict__ woB, const __bf16* __restrict__ gw, u8* __restrict__ ws, __bf16* __restrict__ out) {
  asm volatile("" :: "s"(x), "s"(wqkv), "s"(wq), "s"(wk), "s"(wv), "s"(gw), "s"(ws), "s"(woA), "s"(woB), "s"(out) : "memory", "v255");
  const int wid = __builtin_amdgcn_readfirstlane(threadIdx.x >> 6);
  const int bid = blockIdx.x, xcd = bid & 7, bi = bid >> 3;
  const int tb = (xcd >> 1) * 8 + (bi >> 2);
  const int hg = (xcd & 1) * 4 + (bi & 3);
  const unsigned rot = (bi >> 2) + 2 * (bi & 3);
  const int t0 = tb * 64;
  const __amdgpu_buffer_rsrc_t rws = __builtin_amdgcn_make_buffer_rsrc(ws, 0, WS_END, 0x00020000);
  const u32x4 rw4 = mkrsrc(ws, WS_END);
  TS0();
  unsigned T;
  asm volatile("s_load_dword s100, %0, 0x800" :: "s"(ws) : "memory", "s100");
  const int w = wid & 3;
  const unsigned ones = 0x3F803F80u;
  f32x16 zero16;
#pragma unroll
  for (int i = 0; i < 16; ++i) zero16[i] = 0.f;

  if (wid >= 4) {
    LANE_VARS
    const u32x4 rw = mkrsrc(wq, 512u * 1024u);
    const unsigned vw0 = (unsigned)(w * 16 + (lane >> 3)) * 1024u + vsw, vw1 = vw0 + 7168u;
    const u32x4 rwo = mkrsrc(bid < 128 ? woA : woB, 512u * 1024u);
    const unsigned fn = (bid & 3) * 4 + w, fk = (bid >> 2) & 31;
    asm volatile("s_barrier" ::: "memory");
    asm volatile("s_mov_b32 m0, %0\n\ts_nop 0\n\tbuffer_load_dwordx4 %1, %2, %3 offen lds"
                 :: "s"(L_TW + w * 1024), "v"((lane >> 2) * 1024 + (lane & 3) * 16), "s"(rwo), "s"(fk * 16384 + fn * 64) : "memory");
#pragma nounroll
    for (unsigned j = 0; j < 8; ++j) DMA2(L_WQ(j) + w * 2048, vw0, vw1, rw, ((j + rot) & 7u) << 16);
    TS(1);
    VMW(16);
    {
      const unsigned ao = L_TW + w * 1024 + (h * 4 + (r16 >> 2)) * 64 + (lane & 1) * 32 + ((lane >> 1) & 1) * 8 + gs * 16;
      ST128(__builtin_bit_cast(u32x4, cat8(tr_read(ao), tr_read(ao + 512))), l16, WS_WF + (((bid >> 7) * 16 + fn) * 32 + fk) * 1024);
    }
    const unsigned aw = awf ^ ((w & 1) * 64);
    const unsigned axq = ax + (w >> 1) * 4096;
    f32x16 q = zero16;
    bf16x8 wa[4], xa[4], wb[4], xb[4];
    auto ldq = [&](bf16x8* wf, bf16x8* xf, unsigned j) {
      const unsigned bw = L_WQ(j) + aw, bx = L_X(j);
#pragma unroll
      for (int i = 0; i < 4; ++i) {
        wf[i] = cat8(tr_read(bw + i * 2048), tr_read(bw + i * 2048 + 512));
        xf[i] = lds_ld<bf16x8>(bx + (axq ^ (i * 32)));
      }
      SCHED_FENCE();
    };
    auto mmq = [&](bf16x8* wf, bf16x8* xf) {
#pragma unroll
      for (int i = 0; i < 4; ++i) q = __builtin_amdgcn_mfma_f32_32x32x16_bf16(wf[i], xf[i], q, 0, 0, 0);
      SCHED_FENCE();
    };
    VMW(9);
    RAW_BARRIER();
    TS(2);
    ldq(wa, xa, 0);
#pragma nounroll
    for (unsigned g = 0; g < 4; ++g) {
      ldq(wb, xb, 2 * g + 1);
      mmq(wa, xa);
      if (g == 1) { asm volatile("s_waitcnt vmcnt(1)\n\ts_barrier" ::: "memory"); }
      ldq(wa, xa, (2 * g + 2) & 7);
      mmq(wb, xb);
    }
    u32x4 aQ, aQ1;
#pragma unroll
    for (int j = 0; j < 4; ++j) { aQ[j] = pk_bf16(q[2 * j], q[2 * j + 1]); aQ1[j] = pk_bf16(q[8 + 2 * j], q[9 + 2 * j]); }
    GETT();
    TS(6);
    const bool hi = (lane & 16) != 0, on = (lane & 27) == 16;
    f32x16 D0 = zero16, D1 = zero16;
    {
      const unsigned aV0 = hi ? (on ? L_KV + 1024u : L_KV + 1040u) : aK, aV1 = hi ? aV0 - 256u : aK;
      f32x16 aX, aY = zero16;
      u32x16 rg0; u32x8 rg1; u32x4 ad = {0xddddddddu, aK, aV0, aV1};
#pragma unroll
      for (int i = 0; i < 8; ++i) rg1[i] = 0u;
      unsigned cnt = 16, tgt = 0u, spin = 4000000u, tmp, tv;
      asm volatile(
        "s_waitcnt vmcnt(0) lgkmcnt(0)\n\ts_barrier\n\ts_setprio " STR(PRIO_L) "\n\t"
        TSG(7)
        RDL("48:51", "v77", "0", "0") RDL("52:55", "v77", "1", "0") RDL("56:59", "v78", "0", "1024") RDL("60:63", "v79", "0", "1280") RDL("64:67", "v78", "1", "1024")
        "s_waitcnt lgkmcnt(4)\n\t"
        QKM("0:15", "48:51", "72:75")
        "s_nop 11\n\t"
        "1:\n\t"
        DUALX(0) DUALY(1) DUALX(2) DUALY(3)
        "v_add_u32 v77, 0x2080, v77\n\t"
        "v_add_u32 v78, 0x2080, v78\n\t"
        "v_add_u32 v79, 0x2080, v79\n\t"
        "s_sub_u32 %[cnt], %[cnt], 1\n\t"
        "s_bitcmp1_b32 %[gm], %[cnt]\n\t"
        "s_cbranch_scc0 1b\n\t"
        "s_cmp_eq_u32 %[cnt], 0\n\t"
        "s_cbranch_scc1 4f\n\t"
        "s_add_u32 %[tgt], %[tgt], 4\n\t"
        TSG(6)
        "s_cmp_lg_u32 %[tgt], 8\n\t"
        "s_cbranch_scc1 3f\n\t"
        "v_mov_b32 %[tv], 1\n\t"
        "s_mov_b64 exec, 1\n\tds_add_u32 %[ca], %[tv] offset:4\n\ts_mov_b64 exec, -1\n\t"
        "3:\n\t"
        "ds_read_b32 %[tv], %[ca]\n\t"
        "s_waitcnt lgkmcnt(0)\n\t"
        "v_readfirstlane_b32 %[tmp], %[tv]\n\t"
        "s_cmp_ge_u32 %[tmp], %[tgt]\n\t"
        "s_cbranch_scc1 2f\n\t"
        "s_sleep 1\n\t"
        "s_sub_u32 %[spin], %[spin], 1\n\t"
        "s_cbranch_scc0 3b\n\t"
        "2:\n\t"
        TSG(7)
        "s_branch 1b\n\t"
        "4:\n\t"
        PVM("80:95", "68:71", "24:31")
        "s_waitcnt lgkmcnt(0)\n\ts_nop 11\n\t"
        : "=&{v[0:15]}"(aX), "+{v[16:31]}"(aY), "+{v[32:47]}"(D0), "=&{v[48:63]}"(rg0), "+{v[64:71]}"(rg1), "+{v[76:79]}"(ad), "+{v[80:95]}"(D1),
          [cnt] "+s"(cnt), [tgt] "+s"(tgt), [spin] "+s"(spin), [tmp] "=&s"(tmp), [tv] "=&v"(tv), [tsv] "+v"(tsv)
        : "{v[72:75]}"(aQ), "{v[96:99]}"(aQ1), [gm] "s"(0x8AA1u), [ca] "v"(C_KV) : "memory", "scc");
    }
    TS(19);
    bool bad = !(D0[8] >= 5.4e-20f && D0[8] < 1.0e36f) || !(D1[8] >= 5.4e-20f && D1[8] < 1.0e36f);
#pragma unroll
    for (int j = 0; j < 8; ++j) bad = bad || bad_f(D0[j]) || bad_f(D1[j]);
    if (__builtin_amdgcn_ballot_w64(bad) != 0ull) {
#pragma nounroll
      for (int qi = 0; qi < 2; ++qi) {
        const bf16x8 qf = __builtin_bit_cast(bf16x8, qi ? aQ1 : aQ);
        float m = -3.0e38f;
#pragma nounroll
        for (int t = 0; t < 64; ++t) {
          const u32x4 kf = __builtin_amdgcn_raw_buffer_load_b128(rws, l16, WS_KV + t * 2048, SC1);
          f32x16 s = __builtin_amdgcn_mfma_f32_32x32x16_bf16(__builtin_bit_cast(bf16x8, kf), qf, zero16, 0, 0, 0);
#pragma unroll
          for (int i = 0; i < 16; ++i) m = fmaxf(m, s[i]);
        }
        { auto r = __builtin_amdgcn_permlane32_swap(__builtin_bit_cast(unsigned, m), __builtin_bit_cast(unsigned, m), false, false);
          m = fmaxf(swap_lo(r[0]), swap_lo(r[1])); }
        f32x16 F = zero16;
        const unsigned pad = on ? ones : 0u;
#pragma nounroll
        for (int t = 0; t < 64; ++t) {
          const u32x4 kf = __builtin_amdgcn_raw_buffer_load_b128(rws, l16, WS_KV + t * 2048, SC1);
          u32x4 v0 = __builtin_amdgcn_raw_buffer_load_b128(rws, (l16 & ~256u) + 1024, WS_KV + t * 2048, SC1);
          u32x4 v1 = __builtin_amdgcn_raw_buffer_load_b128(rws, (l16 & ~256u) + 1280, WS_KV + t * 2048, SC1);
#pragma unroll
          for (int i = 0; i < 4; ++i) { v0[i] = hi ? pad : v0[i]; v1[i] = hi ? pad : v1[i]; }
          f32x16 s = __builtin_amdgcn_mfma_f32_32x32x16_bf16(__builtin_bit_cast(bf16x8, kf), qf, zero16, 0, 0, 0);
          f32x8 p0, p1;
#pragma unroll
          for (int i = 0; i < 8; ++i) { p0[i] = __builtin_amdgcn_exp2f(s[i] - m); p1[i] = __builtin_amdgcn_exp2f(s[8 + i] - m); }
          F = __builtin_amdgcn_smfmac_f32_32x32x32_bf16(__builtin_bit_cast(bf16x8, v0), __builtin_bit_cast(bf16x16, p0), F, 0xDDDD, 0, 0);
          F = __builtin_amdgcn_smfmac_f32_32x32x32_bf16(__builtin_bit_cast(bf16x8, v1), __builtin_bit_cast(bf16x16, p1), F, 0xDDDD, 0, 0);
        }
        if (qi) D1 = F; else D0 = F;
      }
    }
    {
      const float i0 = __builtin_amdgcn_rcpf(D0[8]), i1 = __builtin_amdgcn_rcpf(D1[8]);
      const unsigned so = WS_O + (4u << 20) + ((tb * 2 + (w >> 1)) * 32 + hg * 4 + (w & 1) * 2) * 2048;
      ST128(((u32x4){pk_bf16(D0[0] * i0, D0[1] * i0), T, pk_bf16(D0[2] * i0, D0[3] * i0), T}), l16, so);
      ST128(((u32x4){pk_bf16(D0[4] * i0, D0[5] * i0), T, pk_bf16(D0[6] * i0, D0[7] * i0), T}), l16 + 1024, so);
      ST128(((u32x4){pk_bf16(D1[0] * i1, D1[1] * i1), T, pk_bf16(D1[2] * i1, D1[3] * i1), T}), l16 + 2048, so);
      ST128(((u32x4){pk_bf16(D1[4] * i1, D1[5] * i1), T, pk_bf16(D1[6] * i1, D1[7] * i1), T}), l16 + 3072, so);
    }
    TS(20);
    TSDUMP();
    return;
  }

  LANE_VARS
  const u32x4 rx = mkrsrc(x, 2048u * 1024u);
  const int sp = (bi >> 1) & 1, isV = bi & 1;
  const int jg = (xcd & 1) * 2 + sp, xg = (jg + w) & 3;
  const unsigned vx0 = (unsigned)(t0 + xg * 16 + (lane >> 3)) * 1024u + (unsigned)(((lane & 7) ^ g4) * 16), vx1 = (vx0 ^ 64u) + 7168u;
  if (w == 0) {
    const u32x4 rkv = mkrsrc(isV ? wv : wk, 16384u);
#pragma nounroll
    for (unsigned j = 0; j < 8; ++j) {
      DMA2(L_EW + j * 2048, l16, l16, rkv, ((j + rot) & 7u) << 11);
      DMA2(L_X(j) + xg * 2048, vx0, vx1, rx, ((j + rot) & 7u) << 7);
    }
    asm volatile("s_barrier" ::: "memory");
    TS(1);
    const unsigned ak = L_EW + (g4 * 8 + (r16 >> 2)) * 32 + (lane & 3) * 8;
    const unsigned aj = (jg * 16 + r16) * 128 + ((g4 ^ (r16 >> 1)) * 16);
    f32x4 pa = {0.f, 0.f, 0.f, 0.f}, pb = pa;
#pragma nounroll
    for (unsigned b = 0; b < 2; ++b) {
      if (b == 0) VMW(16); else VMW(0);
      bf16x8 kf[8], xf[8];
      const unsigned bk = ak + b * 8192, bx = L_X(4 * b);
#pragma unroll
      for (int i = 0; i < 8; ++i) {
        kf[i] = cat8(tr_read(bk + i * 1024), tr_read(bk + i * 1024 + 128));
        xf[i] = lds_ld<bf16x8>(bx + (aj ^ ((i & 1) * 64)) + (i >> 1) * 8192);
      }
      if (isV) {
#pragma unroll
        for (int i = 0; i < 8; i += 2) {
          pa = __builtin_amdgcn_mfma_f32_16x16x32_bf16(xf[i], kf[i], pa, 0, 0, 0);
          pb = __builtin_amdgcn_mfma_f32_16x16x32_bf16(xf[i + 1], kf[i + 1], pb, 0, 0, 0);
        }
      } else {
#pragma unroll
        for (int i = 0; i < 8; i += 2) {
          pa = __builtin_amdgcn_mfma_f32_16x16x32_bf16(kf[i], xf[i], pa, 0, 0, 0);
          pb = __builtin_amdgcn_mfma_f32_16x16x32_bf16(kf[i + 1], xf[i + 1], pb, 0, 0, 0);
        }
      }
    }
    GETT();
    const f32x4 p = (pa + pb) * (isV ? 1.0f : 0.25f * LOG2E);
    u32x2 o; o[0] = pk_bf16(p[0], p[1]); o[1] = pk_bf16(p[2], p[3]);
    const int slot = (bi >> 2) * 8 + xcd;
    const int u = isV ? gs : h, fl = (isV ? h : gs) * 32 + sp * 16 + r16;
    if (slot < 22) ST128(((u32x4){o[0], T, o[1], T}), u * 1024 + fl * 16, WS_KT + slot * 4096 + isV * 2048);
    __builtin_amdgcn_raw_buffer_store_b64(o, rws, fl * 16 + u * 8, WS_KV + slot * 2048 + isV * 1024, SC1);
    TS(2);
  } else {
    if (w == 1) {
      const u32x4 rex = mkrsrc(gw, 1024u);
      asm volatile("s_mov_b32 m0, %0\n\ts_nop 0\n\tbuffer_load_dwordx4 %1, %2, 0 offen lds" :: "s"(L_GW), "v"(l16), "s"(rex) : "memory");
      if (lane < 32) lds_st<unsigned>(lane * 4, 0u);
    }
    asm volatile("s_barrier" ::: "memory");
#pragma nounroll
    for (unsigned j = 0; j < 8; ++j) DMA2(L_X(j) + xg * 2048, vx0, vx1, rx, ((j + rot) & 7u) << 7);
    TS(1);
  }
  VMW(8);
  RAW_BARRIER();
  GETT();
  const unsigned cst = (lane & 1) ? 0u : ones;
  const u32x4 cst4 = {cst, cst, cst, cst};
  const unsigned acs = L_KV + (lane >> 1) * KVS + 2048 + (lane & 1) * 16;
  if (w == 1 && lane < 12) lds_st<u32x4>(acs, cst4);
  VMW(0);
  RAW_BARRIER();
  TS(3);
  const unsigned se = WS_KT + w * 6144, sm = WS_KT + 24576 + w * 16384;
  GLDN(128, 6, se);
  GLDN(152, 16, sm);
  const u32x4 rwa = mkrsrc(wqkv, 512u * 3072u);
  const unsigned va0 = (unsigned)(lane >> 3) * 3072u + (unsigned)(w - 1) * 1024u + vsw;
  const unsigned va1 = va0 + 23552u, va2 = va1 + 23552u, va3 = va2 + 23552u;
  const unsigned rb = L_RING + (unsigned)(w - 1) * 16384u;
  auto wdma = [&](unsigned s) {
    asm volatile("s_mov_b32 m0, %0\n\ts_nop 0\n\t"
      "buffer_load_dwordx4 %1, %5, %6 offen lds\n\tbuffer_load_dwordx4 %2, %5, %6 offen offset:1024 lds\n\t"
      "buffer_load_dwordx4 %3, %5, %6 offen offset:2048 lds\n\tbuffer_load_dwordx4 %4, %5, %6 offen offset:3072 lds"
      :: "s"(rb + (s & 3u) * 4096u), "v"(va0), "v"(va1), "v"(va2), "v"(va3), "s"(rwa), "s"(((s + 2u * rot) & 15u) * 98304u) : "memory");
  };
  VMW(16);
  TS(4);
#pragma nounroll
  for (int it = 0; it < 200000; ++it) {
    unsigned d;
    TAGCHK(128, 3, d);
    if (__builtin_amdgcn_ballot_w64(d != 0u) == 0ull) break;
    GLDN(128, 6, se);
    VMW(0);
  }
  { const unsigned ae = aK + w * 3072 + ((3 * w) >> 1) * 32; KVPUT(128, 8, 3, ae, ae + 1024 + (w & 1) * 32); }
  TS(5);
  RAW_BARRIER();
  __builtin_amdgcn_s_setprio(PRIO_C);
  if (w == 0) {
    if (lane == 0) __builtin_amdgcn_raw_buffer_store_b32(T, rws, bid * 4, WS_FLAG1, SC1);
    VMW(1);
  } else {
#pragma nounroll
    for (unsigned s = 0; s < 4; ++s) wdma(s);
    VMW(16);
  }
  TS(6);
#pragma nounroll
  for (int it = 0; it < 200000; ++it) {
    unsigned d;
    TAGCHK(152, 8, d);
    if (__builtin_amdgcn_ballot_w64(d != 0u) == 0ull) break;
    GLDN(152, 16, sm);
    VMW(0);
  }
  { const unsigned am = aK + (6 + 4 * w) * KVS; KVPUT(152, 8, 8, am, am + 1024); }
  if (lane < 8) lds_st<u32x4>(acs + (6 + 4 * w) * KVS, cst4);
  post(C_KV);
  TS(7);
  {
    f32x16 gq = zero16;
    const unsigned bx = L_X(2 * w) + (hg & 1) * 4096;
    bf16x8 gf[8], xf[8];
#pragma unroll
    for (int i = 0; i < 8; ++i) {
      gf[i] = lds_ld<bf16x8>(L_GW + ((2 * w + (i >> 2) + rot) & 7u) * 128 + (i & 3) * 32 + h * 16);
      xf[i] = lds_ld<bf16x8>(bx + (ax ^ ((i & 3) * 32)) + (i >> 2) * 8192);
    }
    SCHED_FENCE();
#pragma unroll
    for (int i = 0; i < 8; ++i) gq = __builtin_amdgcn_mfma_f32_32x32x16_bf16(gf[i], xf[i], gq, 0, 0, 0);
    if (h == 0) lds_st<float>(L_GP + w * 128 + r32 * 4, gq[0]);
  }
  TS(8);
  if (w == 0) {
    __builtin_amdgcn_s_sleep(24);
#pragma nounroll
    for (int it = 0; it < 200000; ++it) {
      asm volatile("" ::: "memory");
      u32x4 p = __builtin_amdgcn_raw_buffer_load_b128(rws, l16, WS_FLAG1, SC1);
      if (__builtin_amdgcn_ballot_w64(((p[0] ^ T) | (p[1] ^ T) | (p[2] ^ T) | (p[3] ^ T)) != 0u) == 0ull) break;
      __builtin_amdgcn_s_sleep(8);
    }
    if (bid == 0 && lane == 0) __builtin_amdgcn_raw_buffer_store_b32(T, rws, 0, WS_EPOCH, SC1);
    TS(9);
#pragma nounroll
    for (unsigned b = 0; b < 3; ++b) {
      GLDN(128, 16, WS_KV + (22 + 8 * b) * 2048);
      wait_pg(2 * b + 2);
      VMW(0);
      { const unsigned af = aK + (22 + 8 * b) * KVS; KVPUT(128, 4, 16, af, af + 1024); }
      if (lane < 16) lds_st<u32x4>(acs + (22 + 8 * b) * KVS, cst4);
      __builtin_amdgcn_s_waitcnt(0xC07F);
      asm volatile("s_waitcnt lgkmcnt(0)\n\ts_mov_b64 exec, 1\n\tds_add_u32 %0, %1\n\ts_mov_b64 exec, -1" :: "v"(C_KV), "v"(4u) : "memory");
    }
    GLDN(128, 9, WS_KV + 92 * 1024);
    TS(10);
  } else {
    f32x16 acc[4] = {zero16, zero16, zero16, zero16};
    bf16x8 fa[4], fb[4];
    auto ldf = [&](bf16x8* f, unsigned s, unsigned ks) {
      const unsigned bw = rb + (s & 3u) * 4096u + awf + ks * 2048u, bx = L_X(s >> 1) + (ax ^ (((s & 1u) * 2u + ks) * 32u));
      f[0] = cat8(tr_read(bw), tr_read(bw + 512)); f[2] = lds_ld<bf16x8>(bx);
      f[1] = cat8(tr_read(bw ^ 64u), tr_read((bw ^ 64u) + 512)); f[3] = lds_ld<bf16x8>(bx + 4096);
      SCHED_FENCE();
    };
    auto mm = [&](bf16x8* f) {
      acc[0] = __builtin_amdgcn_mfma_f32_32x32x16_bf16(f[0], f[2], acc[0], 0, 0, 0);
      GAPX();
      acc[2] = __builtin_amdgcn_mfma_f32_32x32x16_bf16(f[1], f[2], acc[2], 0, 0, 0);
      GAPX();
      acc[1] = __builtin_amdgcn_mfma_f32_32x32x16_bf16(f[0], f[3], acc[1], 0, 0, 0);
      GAPX();
      acc[3] = __builtin_amdgcn_mfma_f32_32x32x16_bf16(f[1], f[3], acc[3], 0, 0, 0);
      SCHED_FENCE();
    };
    VMW(12);
    TS(9);
    ldf(fa, 0, 0);
#pragma nounroll
    for (unsigned s = 0; s < 16; ++s) {
      ldf(fb, s, 1);
      mm(fa);
      if (s < 13) VMW(8); else VMW(0);
      ldf(fa, (s + 1) & 15u, 0);
      mm(fb);
      if (s < 12) wdma(s + 4);
      lds_st<unsigned>(C_PG + (w - 1) * 4, (s + 1) >> 1);
    }
    TS(10);
    wait_ge<1>(C_KV, 8);
    GLDN(128, 9, WS_KV + (92 + 9 * w) * 1024);
    u32x4 pk[8];
#pragma unroll
    for (int i = 0; i < 8; ++i)
#pragma unroll
      for (int j = 0; j < 4; ++j) pk[i][j] = pk_bf16(acc[(i >> 2) * 2 + (i & 1)][(i & 2) * 4 + 2 * j], acc[(i >> 2) * 2 + (i & 1)][(i & 2) * 4 + 2 * j + 1]);
    if (w > 1 && r32 >= 16) {
      const unsigned vo = ((w - 2) * 2 + h) * 256 + (r32 - 16) * 16, so = WS_HL + (tb * 32 + hg * 4) * 2048;
#pragma unroll
      for (int i = 0; i < 4; ++i) {
        ST128(((u32x4){pk[2 * i + 1][0], T, pk[2 * i + 1][1], T}), vo + i * 2048, so);
        ST128(((u32x4){pk[2 * i + 1][2], T, pk[2 * i + 1][3], T}), vo + i * 2048 + 1024, so);
      }
    }
    wait_pg(8);
    const unsigned ap = L_PL + (w == 1 ? 5120 : w * 2560 - 4608) + r32 * 32 + h * 16;
#pragma unroll
    for (int i = 0; i < 8; ++i) lds_st<u32x4>(ap + (i >> 1) * 7168 + (i & 1) * 1024, pk[i]);
    post(C_PL);
  }
  wait_ge<SLP>(C_PL, 3);
  TS(11);
  const int ct = tb * 2 + (hg & 1);
  const int nt = (hg >> 1) * 4 + w;
  {
    const unsigned rc = L_PL + w * 7168;
    const unsigned sh = WS_HL + ((tb - 1) * 32 + hg * 4 + w) * 2048;
    u32x4 ha = {0u, T, 0u, T}, hb = ha;
    const bool odd = (lane & 1) != 0;
    const bf16x8 one8 = __builtin_bit_cast(bf16x8, ((u32x4){ones, ones, ones, ones}));
    f32x4 mk;
#pragma unroll
    for (int r = 0; r < 4; ++r) mk[r] = ((unsigned)(4 * g4 + r - (r16 >> 1) - 1) < 8u) ? 0.f : -3.0e38f;
#pragma unroll
    for (int gi = 3; gi >= 0; --gi) {
      if (gi == 2 && tb) { ha = __builtin_amdgcn_raw_buffer_load_b128(rws, l16, sh, SC1); hb = __builtin_amdgcn_raw_buffer_load_b128(rws, l16 + 1024, sh, SC1); }
      if (gi == 0) {
        VMW(0);
        { const unsigned ae = aK + (92 + 9 * w) * 1024 + ((92 + 9 * w) >> 1) * 32; KVPUT(128, 4, 9, ae, ae + 1024 + (w & 1) * 32); }
        if (lane < 10 - (w & 2)) lds_st<u32x4>(acs + (46 + 5 * w - (w == 3)) * KVS, cst4);
        post(C_KV);
        TS(12);
#pragma nounroll
        for (int it = 0; it < 200000; ++it) {
          if (__builtin_amdgcn_ballot_w64(((ha[1] ^ T) | (ha[3] ^ T) | (hb[1] ^ T) | (hb[3] ^ T)) != 0u) == 0ull) break;
          asm volatile("" ::: "memory");
          ha = __builtin_amdgcn_raw_buffer_load_b128(rws, l16, sh, SC1); hb = __builtin_amdgcn_raw_buffer_load_b128(rws, l16 + 1024, sh, SC1);
        }
        lds_st<u32x4>(rc + h * 2560 + r16 * 32 + gs * 16, (u32x4){ha[0], ha[2], hb[0], hb[2]});
        if (tb == 0 && h == 0) {
#pragma unroll
          for (int r = 0; r < 4; ++r) mk[r] = mk[r] == 0.f ? -4.0e9f : mk[r];
        }
        TS(13);
      }
      const bf16x8 kf = lds_ld<bf16x8>(rc + gi * 512 + r16 * 64 + g4 * 16);
      const bf16x8 qf = lds_ld<bf16x8>(odd == (h != 0) ? rc + 5120 + gi * 512 + r16 * 32 + gs * 16 : L_Z);
      const unsigned av = rc + 2560 + gi * 512 + g4 * 256 + (r16 >> 2) * 64 + (r16 & 3) * 8;
      const bf16x8 vf = cat8(tr_read(av), tr_read(av + 32));
      const f32x4 s = __builtin_amdgcn_mfma_f32_16x16x32_bf16(kf, qf, mk, 0, 0, 0);
      float mx = fmaxf(fmaxf(s[0], s[1]), fmaxf(s[2], s[3]));
      { auto r = __builtin_amdgcn_permlane32_swap(__builtin_bit_cast(unsigned, mx), __builtin_bit_cast(unsigned, mx), false, false);
        mx = fmaxf(swap_lo(r[0]), swap_lo(r[1])); }
      { auto r = __builtin_amdgcn_permlane16_swap(__builtin_bit_cast(unsigned, mx), __builtin_bit_cast(unsigned, mx), false, false);
        mx = fmaxf(swap_lo(r[0]), swap_lo(r[1])); }
      float e[4];
#pragma unroll
      for (int r = 0; r < 4; ++r) e[r] = __builtin_amdgcn_exp2f((s[r] - mx) * (0.25f * LOG2E));
      const unsigned p0 = pk_bf16(e[0], e[1]), p1 = pk_bf16(e[2], e[3]);
      const bf16x8 pf = __builtin_bit_cast(bf16x8, ((u32x4){odd ? 0u : p0, odd ? 0u : p1, odd ? p0 : 0u, odd ? p1 : 0u}));
      const f32x4 z4 = {0.f, 0.f, 0.f, 0.f};
      const f32x4 o = __builtin_amdgcn_mfma_f32_16x16x32_bf16(vf, pf, z4, 0, 0, 0);
      const f32x4 l = __builtin_amdgcn_mfma_f32_16x16x32_bf16(one8, pf, z4, 0, 0, 0);
      const float inv = __builtin_amdgcn_rcpf(l[0]);
      ST128(((u32x4){pk_bf16(o[0] * inv, o[1] * inv), T, pk_bf16(o[2] * inv, o[3] * inv), T}), gs * 1024 + (h * 32 + (gi & 1) * 16 + r16) * 16,
            WS_O + ((tb * 2 + (gi >> 1)) * 32 + hg * 4 + w) * 2048);
    }
  }
  TS(14);
  const float gz = (lds_ld<float>(L_GP + r32 * 4) + lds_ld<float>(L_GP + 128 + r32 * 4)) + (lds_ld<float>(L_GP + 256 + r32 * 4) + lds_ld<float>(L_GP + 384 + r32 * 4));
  const float g = __builtin_amdgcn_rcpf(1.f + __builtin_amdgcn_exp2f(-gz * LOG2E));
  f32x16 res = zero16;
  wait_ge<4>(C_LP, 4);
#pragma nounroll
  for (unsigned ph = 0; ph < 2; ++ph) {
    const unsigned l16 = lane_id() * 16;
    GLDN(128, 32, WS_WF + (ph * 16 + nt) * 32768);
    if (ph) { TS(17); RAW_BARRIER(); TS(18); }
    f32x16 acc;
    u32x16 c0, c1, c2, c3;
    unsigned so, d, spin = 200000u, tmp;
    asm volatile(
      "1:\n\t"
      "s_mov_b32 %[so], %[sb]\n\t"
      GLDT(64, 16, "%[va]", "%[rs]", "%[so]")
      "s_waitcnt vmcnt(0)\n\t"
      TAGT(64, 8, "%[d]", "%[T]")
      "v_cmp_ne_u32 vcc, 0, %[d]\n\t"
      "s_cbranch_vccz 2f\n\t"
      "s_sleep 2\n\t"
      "s_sub_u32 %[spin], %[spin], 1\n\t"
      "s_cbranch_scc0 1b\n\t"
      "2:\n\t"
      REP(8, "ds_write_b128 %[aw], v[64+8*i_:64+8*i_+3] offset:i_*1024\n\t")
      "v_mov_b32 %[d], 1\n\t"
      "v_mov_b32 v64, " STR(C_G) "\n\t"
      "s_mov_b64 exec, 1\n\tds_add_u32 v64, %[d]\n\ts_mov_b64 exec, -1\n\t"
      "s_mov_b32 %[spin], 4000000\n\t"
      "3:\n\t"
      "ds_read_b32 %[d], v64\n\t"
      "s_waitcnt lgkmcnt(0)\n\t"
      "v_readfirstlane_b32 %[tmp], %[d]\n\t"
      "s_cmp_ge_u32 %[tmp], %[tg]\n\t"
      "s_cbranch_scc1 4f\n\t"
      "s_sub_u32 %[spin], %[spin], 1\n\t"
      "s_cbranch_scc0 3b\n\t"
      "4:\n\t"
      REP(16, "ds_read_b128 v[64+4*i_:64+4*i_+3], %[va] offset:" STR(L_GA) "+i_*1024\n\t")
      REP(16, "s_waitcnt lgkmcnt(15)\n\t"
              ".if i_==0\n\tv_mfma_f32_32x32x16_bf16 %[acc], v[128:131], v[64:67], 0\n\t.else\n\t"
              "v_mfma_f32_32x32x16_bf16 %[acc], v[128+4*i_:128+4*i_+3], v[64+4*i_:64+4*i_+3], %[acc]\n\t.endif\n\t"
              "ds_read_b128 v[64+4*i_:64+4*i_+3], %[va] offset:" STR(L_GA) "+16384+i_*1024\n\t")
      REP(16, "s_waitcnt lgkmcnt(15-i_)\n\t"
              "v_mfma_f32_32x32x16_bf16 %[acc], v[192+4*i_:192+4*i_+3], v[64+4*i_:64+4*i_+3], %[acc]\n\t")
      "s_nop 15\n\ts_nop 1\n\t"
      : [acc] "=&v"(acc), "=&{v[64:79]}"(c0), "=&{v[80:95]}"(c1), "=&{v[96:111]}"(c2), "=&{v[112:127]}"(c3),
        [so] "=&s"(so), [d] "=&v"(d), [spin] "+s"(spin), [tmp] "=&s"(tmp)
      : [sb] "s"(WS_O + (ph << 22) + (ct * 32 + w * 8) * 2048), [va] "v"(l16), [rs] "s"(rw4), [T] "s"(T), [aw] "v"(L_GA + w * 8192 + l16), [tg] "s"(4 * ph + 4)
      : "memory", "scc", "vcc");
    const float f = ph ? 1.f - g : g;
#pragma unroll
    for (int i = 0; i < 16; ++i) res[i] += f * acc[i];
    if (ph == 0) __builtin_amdgcn_s_setprio(0);
  }
  {
    u32x4 o0, o1;
#pragma unroll
    for (int j = 0; j < 4; ++j) { o0[j] = pk_bf16(res[2 * j], res[2 * j + 1]); o1[j] = pk_bf16(res[8 + 2 * j], res[9 + 2 * j]); }
    const int ln = lane_id() & 63;
    __bf16* dst = out + (size_t)(ct * 32 + (ln & 31)) * 512 + nt * 32 + (ln >> 5) * 16;
    *(u32x4*)dst = o0;
    *(u32x4*)(dst + 8) = o1;
  }
  TS(26);
  TSDUMP();
}

extern "C" void kernel_launch(void* const* d_in, const int* in_sizes, int n_in,
                              void* d_out, int out_size, void* d_ws, size_t ws_size,
                              hipStream_t stream) {
  (void)hipFuncSetAttribute((const void*)fused, hipFuncAttributeMaxDynamicSharedMemorySize, L_TOT);
  fused<<<256, 512, L_TOT, stream>>>((const __bf16*)d_in[0], (const __bf16*)d_in[1], (const __bf16*)d_in[2], (const __bf16*)d_in[4],
                                 (const __bf16*)d_in[5], (const __bf16*)d_in[6], (const __bf16*)d_in[7], (const __bf16*)d_in[8],
                                 (u8*)d_ws, (__bf16*)d_out);
}
```

```cpp
#include <hip/hip_runtime.h>
#include <hip/hip_bf16.h>

typedef __bf16 bf16x2 __attribute__((ext_vector_type(2)));
typedef __bf16 bf16x4 __attribute__((ext_vector_type(4)));
typedef __bf16 bf16x8 __attribute__((ext_vector_type(8)));
typedef __bf16 bf16x16 __attribute__((ext_vector_type(16)));
typedef short s16x4 __attribute__((ext_vector_type(4)));
typedef float f32x4 __attribute__((ext_vector_type(4)));
typedef float f32x8 __attribute__((ext_vector_type(8)));
typedef float f32x16 __attribute__((ext_vector_type(16)));
typedef unsigned u32x2 __attribute__((ext_vector_type(2)));
typedef unsigned u32x4 __attribute__((ext_vector_type(4)));
typedef unsigned u32x8 __attribute__((ext_vector_type(8)));
typedef unsigned u32x16 __attribute__((ext_vector_type(16)));
typedef unsigned char u8;
typedef __attribute__((address_space(3))) u8 lds_u8;

#define STR2(x) #x
#define STR(x) STR2(x)
#define LOG2E 1.4426950408889634f
#define SC1 16
#define QD 2
#define B0C 1
#define EJ 6
#define MJ 7
#define MPRE 1
#define P_PK 2
#define P_LA0 2
#define P_LA 2
#define P_L0 0
#define P_F 2
#define P_PL 0
#define P_CK 2
#define BTRIG 0xd0000000
#define P_TA 0
#define P_TB 0
#define ASLP 4
#define BSLP 0

#define WS_FLAG1 0u
#define WS_EPOCH 2048u
#define WS_G     4096u
#define WS_KV    (64u << 10)
#define WS_KT    (256u << 10)
#define WS_WF    (1u << 20)
#define WS_HL    (2u << 20)
#define WS_O     (4u << 20)
#define WS_END   (8u << 20)
#define TS(i) do {} while (0)
#define TSA(i) ""
#define TSG(o, r, sh) ""
#define TSDUMP() do {} while (0)
#define TS0() do {} while (0)
#define TSI()
#define TSOP
#define TSQ(r) do {} while (0)
#define TSQC() do {} while (0)

#define C_KV   0
#define C_PL   4
#define C_RDY  8
#define C_PG   16
#define C_LP   32
#define C_Q    48
#define L_G    128
#define L_GW   1024
#define L_KV   2048
#define KVS    2080
#define L_EW   2048
#define L_TW   18432
#define L_X(j) (49152u + ((j) << 13))
#define L_WQ(j) (32768u + ((81920u + ((j) << 13)) & 131071u))
#define L_RING 114688
#define L_PL   135168
#define L_GA   2048
#define L_TOT  163840

__device__ __forceinline__ unsigned pk_bf16(float lo, float hi) {
  bf16x2 v; v[0] = (__bf16)lo; v[1] = (__bf16)hi;
  return __builtin_bit_cast(unsigned, v);
}
__device__ __forceinline__ u32x4 mkrsrc(const void* p, unsigned bytes) {
  unsigned long long a = (unsigned long long)p;
  u32x4 r;
  r[0] = (unsigned)a; r[1] = (unsigned)(a >> 32) & 0xffffu; r[2] = bytes; r[3] = 0x00020000u;
  return r;
}
__device__ __forceinline__ bf16x4 tr_read(unsigned a) {
  return __builtin_bit_cast(bf16x4, __builtin_amdgcn_ds_read_tr16_b64_v4i16((__attribute__((address_space(3))) s16x4*)(lds_u8*)(unsigned long long)a));
}
__device__ __forceinline__ bf16x8 cat8(bf16x4 a, bf16x4 b) { return __builtin_shufflevector(a, b, 0, 1, 2, 3, 4, 5, 6, 7); }
template <typename T> __device__ __forceinline__ T lds_ld(unsigned a) { return *(__attribute__((address_space(3))) T*)(lds_u8*)(unsigned long long)a; }
template <typename T> __device__ __forceinline__ void lds_st(unsigned a, T v) { *(__attribute__((address_space(3))) T*)(lds_u8*)(unsigned long long)a = v; }
#define RAW_BARRIER() do { __builtin_amdgcn_s_waitcnt(0xC07F); asm volatile("s_waitcnt lgkmcnt(0)\n\ts_barrier" ::: "memory"); } while (0)
#define VMW(n) asm volatile("s_waitcnt vmcnt(%0)" :: "n"(n) : "memory")
#define ST128(d, vo, so) __builtin_amdgcn_raw_buffer_store_b128((d), rws, (vo) + (so), 0, SC1)
#define SCHED_FENCE() __builtin_amdgcn_sched_barrier(0)
#define PRIO(n) asm volatile("s_setprio " STR(n) ::: "memory")
__device__ __forceinline__ float swap_lo(unsigned a) { return __builtin_bit_cast(float, a); }
#define POST(c, z, v) asm volatile("s_mov_b64 exec, 1\n\tds_add_u32 %0, %1 offset:" STR(c) "\n\ts_mov_b64 exec, -1" :: "v"(z), "v"(v) : "memory")
template <int C, int SLEEP> __device__ __forceinline__ void wait_ge(unsigned z, unsigned target) {
  unsigned t, v, spin = 4000000u;
  asm volatile("1:\n\tds_read_b32 %1, %3 offset:%6\n\ts_waitcnt lgkmcnt(0)\n\tv_readfirstlane_b32 %0, %1\n\ts_cmp_ge_u32 %0, %4\n\ts_cbranch_scc1 2f\n\t"
               "s_sleep %5\n\ts_sub_u32 %2, %2, 1\n\ts_cbranch_scc0 1b\n\t2:"
               : "=&s"(t), "=&v"(v), "+s"(spin) : "v"(z), "s"(target), "n"(SLEEP), "n"(C) : "memory", "scc");
}
template <int C, int N, int SLEEP> __device__ __forceinline__ void wait_min(unsigned z, unsigned target) {
  unsigned t, spin = 4000000u; u32x4 v;
  asm volatile("1:\n\tds_read_b128 v[124:127], %3 offset:%6\n\ts_waitcnt lgkmcnt(0)\n\tv_min3_u32 v124, v124, v125, v126\n\t.if %7==4\n\tv_min_u32 v124, v124, v127\n\t.endif\n\t"
               "s_nop 0\n\tv_readfirstlane_b32 %0, v124\n\t"
               "s_cmp_ge_u32 %0, %4\n\ts_cbranch_scc1 2f\n\ts_sleep %5\n\ts_sub_u32 %2, %2, 1\n\ts_cbranch_scc0 1b\n\t2:"
               : "=&s"(t), "=&{v[124:127]}"(v), "+s"(spin) : "v"(z), "s"(target), "n"(SLEEP), "n"(C), "n"(N) : "memory", "scc");
}
#define DMA2(m, v0, v1, rs, so) asm volatile("s_mov_b32 m0, %0\n\ts_nop 0\n\t" \
    "buffer_load_dwordx4 %1, %3, %4 offen lds\n\tbuffer_load_dwordx4 %2, %3, %4 offen offset:1024 lds" \
    :: "s"(m), "v"(v0), "v"(v1), "s"(rs), "s"(so) : "memory")
#define REPV(v, n, body) ".set " v ", 0\n\t.rept " #n "\n\t" body ".set " v ", " v "+1\n\t.endr\n\t"
#define REPX(v, n, body) REPV(v, n, body)
#define REP(n, body) REPV("i_", n, body)
#define GLDT(r, n, va, rs, so) REP(n, \
    "buffer_load_dwordx4 v[" r "+4*i_:" r "+4*i_+3], " va ", " rs ", " so " offen offset:(i_&3)*1024 sc1\n\t" \
    ".if (i_&3)==3\n\ts_add_u32 " so ", " so ", 0x1000\n\t.endif\n\t")
#define GLDN(r, n, so) do { unsigned so_ = (so); asm volatile(GLDT(#r, n, "%1", "%2", "%0") : "+s"(so_) : "v"(l16), "s"(rw4) : "memory", "scc"); } while (0)
#define TAGT(r, n, t) REP(n, \
    "v_bitop3_b32 v[" r "+8*i_+1], v[" r "+8*i_+1], v[" r "+8*i_+3], " t " bitop3:0x7e\n\t" \
    "v_bitop3_b32 v[" r "+8*i_+5], v[" r "+8*i_+5], v[" r "+8*i_+7], " t " bitop3:0x7e\n\t" \
    ".if i_==0\n\tv_or_b32 v[" r "+7], v[" r "+1], v[" r "+5]\n\t.else\n\t" \
    "v_or3_b32 v[" r "+7], v[" r "+7], v[" r "+8*i_+1], v[" r "+8*i_+5]\n\t.endif\n\t") \
    "v_cmp_ne_u32 vcc, 0, v[" r "+7]\n\t"
#define TAGP(r, n) REP(n, \
    "v_mov_b32 v[" r "+8*i_+1], v[" r "+8*i_+2]\n\t" \
    "v_pk_mov_b32 v[" r "+8*i_+2:" r "+8*i_+3], v[" r "+8*i_+4:" r "+8*i_+5], v[" r "+8*i_+6:" r "+8*i_+7] op_sel:[0,0]\n\t")
#define TAGGET(r, n, n2, so) do { unsigned so_, spin_ = 200000u; asm volatile("1:\n\t" TAGT(#r, n, "%[T]") TAGP(#r, n) "s_cbranch_vccz 2f\n\t" \
    "s_mov_b32 %[o], %[b]\n\t" GLDT(#r, n2, "%[va]", "%[rs]", "%[o]") "s_waitcnt vmcnt(0)\n\ts_sub_u32 %[sp], %[sp], 1\n\ts_cbranch_scc0 1b\n\t2:" \
    : [o] "=&s"(so_), [sp] "+s"(spin_) : [b] "s"(so), [va] "v"(l16), [rs] "s"(rw4), [T] "s"(T) : "memory", "scc", "vcc"); } while (0)
#define FULLT(r, t) "v_max3_u32 " t ", v[" r "], v[" r "+2], v[" r "+4]\n\tv_max3_u32 " t ", " t ", v[" r "+6], v[" r "+8]\n\t" \
    "v_max3_u32 " t ", " t ", v[" r "+10], v[" r "+12]\n\tv_max_u32 " t ", " t ", v[" r "+14]\n\tv_cmp_eq_u32 vcc, -1, " t "\n\t"
#define KVPUT(r, st, n, a, c) asm volatile(REP(n, "ds_write_b128 %0, v[" #r "+" #st "*i_:" #r "+" #st "*i_+3] offset:(i_>>1)*2080+(i_&1)*1024\n\t") \
    "s_mov_b64 exec, (1<<" #n ")-1\n\tds_write_b128 %1, %2\n\ts_mov_b64 exec, -1" :: "v"(a), "v"(c), "v"(cst4) : "memory")
#define GETT() asm volatile("s_waitcnt lgkmcnt(0)\n\ts_mul_i32 %0, s100, 1664525\n\ts_add_u32 %0, %0, 1013904223\n\ts_or_b32 %0, %0, 1" : "=s"(T) :: "memory", "scc")

#define E1(r) "v_exp_f32 v[128+" #r "], v[128+" #r "]\n\t"
#define E4(a, b, c, d) E1(a) E1(b) E1(c) E1(d)
#define PVM(d, a, b) "v_smfmac_f32_32x32x32_bf16 v[" d "], v[" a "], v[" b "], v232\n\t"
#define QKM(d, k, q) "v_mfma_f32_32x32x16_bf16 v[" d "], v[" k "], " q ", 0\n\t"
#define RDL(d, a, t, c) "ds_read_b128 v[" d "], " a " offset:(" t ")*2080+" c "\n\t"
#define DUAL(t, W, X, Ka, V0a, V1a, Kb, V1b) \
  E1(0) E1(1) W PVM("176:191", V1b, "152:159") RDL(V1b, "v235", #t "+1", "1280") E4(2, 3, 4, 5) E1(6) \
  QKM("144:159", Ka, "%[q1]") RDL(Ka, "v233", #t "+2", "0") E4(7, 8, 9, 10) E1(11) E1(12) \
  PVM("160:175", V0a, "128:135") E4(13, 14, 15, 16) E1(17) \
  PVM("160:175", V1a, "136:143") X E4(18, 19, 20, 21) E1(22) \
  QKM("128:143", Kb, "%[q0]") E4(23, 24, 25, 26) E1(27) E1(28) \
  PVM("176:191", V0a, "144:151") RDL(V0a, "v234", #t "+2", "1024") E1(29) E1(30) E1(31)
#define LW1 "s_waitcnt lgkmcnt(1)\n\t"
#define DUALX(t, W, X) DUAL(t, W, X, "192:195", "200:203", "204:207", "196:199", "212:215")
#define DUALY(t, W, X) DUAL(t, W, X, "196:199", "208:211", "212:215", "192:195", "204:207")
__device__ __forceinline__ int lane_id() {
  int l;
  asm volatile("v_mbcnt_lo_u32_b32 %0, -1, 0\n\tv_mbcnt_hi_u32_b32 %0, -1, %0" : "=v"(l));
  return l;
}
#define LANE_VARS \
  TSI() \
  const int lane = lane_id() & 63; \
  const int r32 = lane & 31, h = lane >> 5, r16 = lane & 15, g4 = lane >> 4, gs = g4 & 1; \
  const unsigned l16 = lane * 16; \
  const unsigned aK = L_KV + l16; \
  const unsigned ax = r32 * 128 + ((h ^ ((r32 >> 1) & 7)) * 16); \
  const unsigned awf = (h * 8 + (r16 >> 2)) * 128 + (r16 >> 3) * 64 + gs * 32 + (lane & 3) * 8; \
  const unsigned vsw = ((lane & 7) ^ (gs << 2)) * 16 + hg * 128;
#define PIN(x) asm volatile("" : "+v"(x))

__global__ void __launch_bounds__(512, 2) __attribute__((amdgpu_num_vgpr(64), amdgpu_num_sgpr(88)))
fused(const __bf16* __restrict__ x, const __bf16* __restrict__ wqkv, const __bf16* __restrict__ woA,
      const __bf16* __restrict__ wq, const __bf16* __restrict__ wk, const __bf16* __restrict__ wv,
      const __bf16* __restrict__ woB, const __bf16* __restrict__ gw, u8* __restrict__ ws, __bf16* __restrict__ out) {
  asm volatile("" :: "s"(x), "s"(wqkv), "s"(wq), "s"(wk), "s"(wv), "s"(gw), "s"(ws), "s"(woA), "s"(woB), "s"(out) : "memory", "v255");
  const int wid = __builtin_amdgcn_readfirstlane(threadIdx.x >> 6);
  const int bid = blockIdx.x, xcd = bid & 7, bi = bid >> 3;
  const int tb = (xcd >> 1) * 8 + (bi >> 2);
  const int hg = (xcd & 1) * 4 + (bi & 3);
  const unsigned rot = (bi >> 2) + 2 * (bi & 3);
  const int t0 = tb * 64;
  const __amdgpu_buffer_rsrc_t rws = __builtin_amdgcn_make_buffer_rsrc(ws, 0, WS_END, 0x00020000);
  const u32x4 rw4 = mkrsrc(ws, WS_END);
  TS0();
  unsigned T;
  const int w = wid & 3;
  const unsigned ones = 0x3F803F80u;
  f32x16 zero16;
#pragma unroll
  for (int i = 0; i < 16; ++i) zero16[i] = 0.f;

  if (wid >= 4) {
    LANE_VARS
    const u32x4 rwo = mkrsrc(bid < 128 ? woA : woB, 512u * 1024u);
    const unsigned fn = (bid & 3) * 4 + w, fk = (bid >> 2) & 31;
    asm volatile("s_mov_b32 m0, %0\n\ts_nop 0\n\tbuffer_load_dwordx4 %1, %2, %3 offen lds"
                 :: "s"(L_TW + w * 1024), "v"((lane >> 2) * 1024 + (lane & 3) * 16), "s"(rwo), "s"(fk * 16384 + fn * 64) : "memory");
    const bool hi = (lane & 16) != 0, on = (lane & 27) == 16;
    {
      const unsigned aV0 = hi ? (on ? L_KV + 1024u : L_KV + 1040u) : aK, aV1 = hi ? aV0 - 256u : aK;
      asm volatile(REP(40, "v_mov_b32 v[152+i_], 0\n\t") REP(4, "v_mov_b32 v[212+i_], 0\n\t")
                   "v_mov_b32 v232, 0xdddddddd\n\tv_mov_b32 v233, %0\n\tv_mov_b32 v234, %1\n\tv_mov_b32 v235, %2\n\t"
                   "v_mov_b32 v236, 0\n\tv_mov_b32 v237, %3\n\tv_mov_b32 v238, %4\n\tv_mov_b32 v239, %5\n\t"
                   "s_mov_b64 exec, 15\n\tds_write_b128 v239, v[152:155]\n\ts_mov_b64 exec, -1\n\t"
                   "s_setprio " STR(P_L0) "\n\ts_waitcnt lgkmcnt(0)\n\ts_barrier"
                   :: "v"(aK), "v"(aV0), "v"(aV1), "v"(L_G + 256 + (w >> 1) * 128 + r32 * 4), "s"(C_LP + w * 4), "v"(l16) : "memory");
    }
    TS(1);
    VMW(0);
    {
      const unsigned ao = L_TW + w * 1024 + (h * 4 + (r16 >> 2)) * 64 + (lane & 1) * 32 + ((lane >> 1) & 1) * 8 + gs * 16;
      ST128(__builtin_bit_cast(u32x4, cat8(tr_read(ao), tr_read(ao + 512))), l16, WS_WF + (((bid >> 7) * 16 + fn) * 32 + fk) * 1024);
    }
    TS(2);
    u32x4 aQ, aQ1;
    {
      u32x16 c0, c1, c2, c3, c4;
      unsigned x0 = L_X(0) + ax + (w >> 1) * 4096, x1 = x0 ^ 32u, x2 = x0 ^ 64u, x3 = x0 ^ 96u, vw, qt, bt = 2u, cn = 0u, wo = 81920u, spin = 4000000u;
#define QPOLL "s_bitcmp1_b32 %[cn], %[bt]\n\ts_cbranch_scc1 3f\n\t" \
      "2:\n\tds_read_b32 %[q0], v236 offset:" STR(C_Q) "\n\ts_waitcnt lgkmcnt(0)\n\tv_readfirstlane_b32 %[cn], %[q0]\n\t" \
      "s_bitcmp1_b32 %[cn], %[bt]\n\ts_cbranch_scc1 3f\n\ts_sub_u32 %[spin], %[spin], 1\n\ts_cbranch_scc0 2b\n\ts_mov_b32 %[spin], 0\n\t" \
      "3:\n\ts_add_u32 %[bt], %[bt], 4\n\t"
#define QRD1(f, i, o) "ds_read_b64_tr_b16 v[" f "+" #i "*4:" f "+" #i "*4+1], %[vw] offset:" o "+" #i "*2048\n\t" \
      "ds_read_b64_tr_b16 v[" f "+" #i "*4+2:" f "+" #i "*4+3], %[vw] offset:" o "+" #i "*2048+512\n\t" \
      "ds_read_b128 v[" f "+16+" #i "*4:" f "+16+" #i "*4+3], %[x" #i "] offset:" o "\n\t"
#define QRD(f, o) QRD1(f, 0, o) QRD1(f, 1, o) QRD1(f, 2, o) QRD1(f, 3, o)
#define QMM(f, n) REP(4, "s_waitcnt lgkmcnt(" n ")\n\tv_mfma_f32_32x32x16_bf16 v[48:63], v[" f "+4*i_:" f "+4*i_+3], v[" f "+16+4*i_:" f "+16+4*i_+3], v[48:63]\n\t")
      asm volatile(
        REP(16, "v_mov_b32 v[48+i_], 0\n\t")
        "1:\n\t"
        "v_add_u32 %[vw], %[wo], %[aw]\n\t"
        QPOLL QRD("64", "0")
        "s_cmp_eq_u32 %[bt], 6\n\ts_cbranch_scc1 4f\n\t"
        QMM("96", "12")
        "4:\n\t"
        QPOLL QRD("96", "8192") QMM("64", "12")
        "v_add_u32 %[x0], 0x4000, %[x0]\n\tv_add_u32 %[x1], 0x4000, %[x1]\n\tv_add_u32 %[x2], 0x4000, %[x2]\n\tv_add_u32 %[x3], 0x4000, %[x3]\n\t"
        "s_add_u32 %[wo], %[wo], 0x4000\n\ts_and_b32 %[wo], %[wo], 0x1ffff\n\t"
        "s_cmp_lg_u32 %[bt], 34\n\ts_cbranch_scc1 1b\n\t"
        QMM("96", "9-3*i_")
        "s_nop 11\n\t"
        REP(8, "v_cvt_pk_bf16_f32 v[40+i_], v[48+2*i_], v[49+2*i_]\n\t")
        : "=&{v[40:43]}"(aQ), "=&{v[44:47]}"(aQ1), "=&{v[48:63]}"(c4), "=&{v[64:79]}"(c0), "=&{v[80:95]}"(c1), "=&{v[96:111]}"(c2), "=&{v[112:127]}"(c3),
          [x0] "+v"(x0), [x1] "+v"(x1), [x2] "+v"(x2), [x3] "+v"(x3), [vw] "=&v"(vw), [q0] "=&v"(qt), [bt] "+s"(bt), [cn] "+s"(cn), [wo] "+s"(wo), [spin] "+s"(spin)
        : [aw] "v"(32768u + (awf ^ ((w & 1) * 64)))
        : "memory", "scc");
    }
    TS(6);
    const unsigned so = WS_O + (2u << 20) + ((tb * 2 + (w >> 1)) * 32 + hg * 4 + (w & 1) * 2) * 1024;
    unsigned long long bad0, bad1;
    {
#define LFIN(d, o, bad, c) \
      "v_rcp_f32 v240, v[" d "+8]\n\t" \
      "v_maximum3_f32 v241, |v[" d "]|, |v[" d "+1]|, |v[" d "+2]|\n\t" \
      "v_maximum3_f32 v242, |v[" d "+3]|, |v[" d "+4]|, |v[" d "+5]|\n\t" \
      "v_maximum3_f32 v241, v241, |v[" d "+6]|, |v[" d "+7]|\n\t" \
      "v_mul_f32 v240, v240, v243\n\t" \
      "v_maximum3_f32 v241, v241, v242, v[" d "+8]\n\t" \
      "v_cmp_nle_f32 vcc, %[lo], v[" d "+8]\n\t" \
      "v_cmp_ngt_f32 " bad ", %[hi], v241\n\t" \
      REP(8, "v_mul_f32 v[" d "+i_], v[" d "+i_], v240\n\t") \
      "s_or_b64 " bad ", " bad ", vcc\n\t" \
      REP(4, "v_cvt_pk_bf16_f32 v[" o "+i_], v[" d "+2*i_], v[" d "+2*i_+1]\n\t") \
      "s_cbranch_scc1 5f\n\t" \
      "buffer_store_dwordx4 v[" o ":" o "+3], v239, %[rs], %[so] offen offset:" c " sc1\n\t" \
      "5:\n\t"
      unsigned tgt = 0u, spin = 4000000u, tmp;
      asm volatile(
        "s_waitcnt vmcnt(0) lgkmcnt(0)\n\ts_setprio 1\n\ts_barrier\n\t"
        TSA(7)
        RDL("192:195", "v233", "0", "0") RDL("196:199", "v233", "1", "0") RDL("200:203", "v234", "0", "1024") RDL("204:207", "v235", "0", "1280") RDL("208:211", "v234", "1", "1024")
        "s_waitcnt lgkmcnt(4)\n\t"
        QKM("128:143", "192:195", "%[q0]")
        "s_nop 11\n\t"
        "s_branch 2f\n\t"
        "1:\n\t"
        DUALX(4, "", "") DUALY(5, LW1, "") DUALX(6, LW1, "") DUALY(7, LW1, "") DUALX(8, LW1, "") DUALY(9, LW1, "") DUALX(10, LW1, "") DUALY(11, LW1, "")
        "v_add_u32 v233, 0x6180, v233\n\t"
        "v_add_u32 v234, 0x6180, v234\n\t"
        "v_add_u32 v235, 0x6180, v235\n\t"
        "2:\n\t"
        DUALX(0, LW1, "ds_write_b32 v238, v233\n\t") DUALY(1, LW1, "ds_read_b32 v243, v237\n\t") DUALX(2, LW1, "s_add_u32 %[tgt], %[tgt], 4\n\t") DUALY(3, LW1, "ds_read_b32 v244, v236\n\t")
        TSG(6, "%[tgt]", 1)
        LW1
        "3:\n\t"
        "v_readfirstlane_b32 %[tmp], v244\n\t"
        "s_cmp_ge_u32 %[tmp], %[tgt]\n\t"
        "s_cbranch_scc1 1b\n\t"
        "s_cmp_eq_u32 %[tgt], 24\n\t"
        "s_cbranch_scc1 4f\n\t"
        "ds_read_b32 v244, v236\n\t"
        "s_waitcnt lgkmcnt(0)\n\t"
        TSG(7, "%[tgt]", 1)
        "s_sub_u32 %[spin], %[spin], 1\n\t"
        "s_cbranch_scc0 3b\n\t"
        "s_mov_b32 %[spin], 0\n\t"
        "s_branch 1b\n\t"
        "4:\n\t"
        PVM("176:191", "212:215", "152:159")
        LFIN("160", "216", "%[b0]", "0")
        LFIN("176", "224", "%[b1]", "1024")
        "ds_write_b32 v238, v232\n\t"
        "s_waitcnt lgkmcnt(0)\n\t"
        : [tgt] "+s"(tgt), [spin] "+s"(spin), [tmp] "=&s"(tmp), [b0] "=&s"(bad0), [b1] "=&s"(bad1) TSOP
        : [q0] "v"(aQ), [q1] "v"(aQ1), [lo] "s"(5.4e-20f), [hi] "s"(1.0e36f), [rs] "s"(rw4), [so] "s"(so) : "memory", "scc", "vcc");
    }
    TS(20);
    if ((bad0 | bad1) != 0ull) {
      const int lane = lane_id() & 63;
      const unsigned l16 = lane * 16;
      const bool hi = (lane & 16) != 0, on = (lane & 27) == 16;
#pragma nounroll
      for (int qi = 0; qi < 2; ++qi) {
        if ((qi ? bad1 : bad0) == 0ull) continue;
        const bf16x8 qf = __builtin_bit_cast(bf16x8, qi ? aQ1 : aQ);
        float m = -3.0e38f;
#pragma nounroll
        for (int t = 0; t < 64; ++t) {
          const u32x4 kf = __builtin_amdgcn_raw_buffer_load_b128(rws, l16, WS_KV + t * 2048, SC1);
          f32x16 s = __builtin_amdgcn_mfma_f32_32x32x16_bf16(__builtin_bit_cast(bf16x8, kf), qf, zero16, 0, 0, 0);
#pragma unroll
          for (int i = 0; i < 16; ++i) m = fmaxf(m, s[i]);
        }
        { auto r = __builtin_amdgcn_permlane32_swap(__builtin_bit_cast(unsigned, m), __builtin_bit_cast(unsigned, m), false, false);
          m = fmaxf(swap_lo(r[0]), swap_lo(r[1])); }
        f32x16 F = zero16;
        const unsigned pad = on ? ones : 0u;
#pragma nounroll
        for (int t = 0; t < 64; ++t) {
          const u32x4 kf = __builtin_amdgcn_raw_buffer_load_b128(rws, l16, WS_KV + t * 2048, SC1);
          u32x4 v0 = __builtin_amdgcn_raw_buffer_load_b128(rws, (l16 & ~256u) + 1024, WS_KV + t * 2048, SC1);
          u32x4 v1 = __builtin_amdgcn_raw_buffer_load_b128(rws, (l16 & ~256u) + 1280, WS_KV + t * 2048, SC1);
#pragma unroll
          for (int i = 0; i < 4; ++i) { v0[i] = hi ? pad : v0[i]; v1[i] = hi ? pad : v1[i]; }
          f32x16 s = __builtin_amdgcn_mfma_f32_32x32x16_bf16(__builtin_bit_cast(bf16x8, kf), qf, zero16, 0, 0, 0);
          f32x8 p0, p1;
#pragma unroll
          for (int i = 0; i < 8; ++i) { p0[i] = __builtin_amdgcn_exp2f(s[i] - m); p1[i] = __builtin_amdgcn_exp2f(s[8 + i] - m); }
          F = __builtin_amdgcn_smfmac_f32_32x32x32_bf16(__builtin_bit_cast(bf16x8, v0), __builtin_bit_cast(bf16x16, p0), F, 0xDDDD, 0, 0);
          F = __builtin_amdgcn_smfmac_f32_32x32x32_bf16(__builtin_bit_cast(bf16x8, v1), __builtin_bit_cast(bf16x16, p1), F, 0xDDDD, 0, 0);
        }
        const float iv = __builtin_amdgcn_rcpf(F[8]) * lds_ld<float>(L_G + 256 + (w >> 1) * 128 + (lane & 31) * 4);
        ST128(((u32x4){pk_bf16(F[0] * iv, F[1] * iv), pk_bf16(F[2] * iv, F[3] * iv), pk_bf16(F[4] * iv, F[5] * iv), pk_bf16(F[6] * iv, F[7] * iv)}), l16, so + qi * 1024);
      }
    }
    TSDUMP();
    return;
  }

  asm volatile("s_load_dword s100, %0, 0x800" :: "s"(ws) : "memory", "s100");
  LANE_VARS
  const u32x4 rx = mkrsrc(x, 2048u * 1024u);
  const int sp = (bi >> 1) & 1, isV = bi & 1;
  const int jg = (xcd & 1) * 2 + sp, xg = (jg + w) & 3;
  const unsigned vx0 = (unsigned)(t0 + xg * 16 + (lane >> 3)) * 1024u + (unsigned)(((lane & 7) ^ g4) * 16), vx1 = (vx0 ^ 64u) + 7168u;
  const unsigned aj = L_X(0) + (xg * 16 + r16) * 128 + ((g4 ^ (r16 >> 1)) * 16);
  if (w == 0) {
    const u32x4 rkv = mkrsrc(isV ? wv : wk, 16384u), rex = mkrsrc(gw, 1024u);
    TS(31);
#pragma nounroll
    for (unsigned j = 0; j < 8; ++j) {
      DMA2(L_EW + j * 2048, l16, l16, rkv, ((j + rot) & 7u) << 11);
      DMA2(L_X(j) + xg * 2048, vx0, vx1, rx, ((j + rot) & 7u) << 7);
    }
    asm volatile("s_mov_b32 m0, %0\n\ts_nop 0\n\tbuffer_load_dwordx4 %1, %2, 0 offen lds" :: "s"(L_GW), "v"((l16 + rot * 128) & 1023u), "s"(rex) : "memory");
    TS(30);
  }
  unsigned vz = 0u, v1 = 1u;
  const unsigned cst = (lane & 1) ? 0u : ones;
  u32x4 cst4 = {cst, cst, cst, cst};
  const unsigned acs = L_KV + (lane >> 1) * KVS + 2048 + (lane & 1) * 16;
  unsigned am = aK + (6 + 3 * w) * KVS, amc = acs + (6 + 3 * w) * KVS, af0 = acs + 18 * KVS, af1 = acs + 30 * KVS;
  const unsigned rb = L_RING + (unsigned)(w - 1) * 16384u;
  unsigned va0 = (unsigned)(lane >> 3) * 3072u + (unsigned)(w - 1) * 1024u + vsw, va1 = va0 + 23552u, va2 = va1 + 23552u, va3 = va2 + 23552u;
  unsigned aW0 = rb + awf, aW1 = rb + (awf ^ 64u), ax0 = L_X(0) + ax, ax1 = L_X(0) + (ax ^ 32u), ax2 = L_X(0) + (ax ^ 64u), ax3 = L_X(0) + (ax ^ 96u);
  unsigned ap = L_PL + (w == 1 ? 5120 : w * 2560 - 4608) + r32 * 32 + h * 16, apg = C_PG + (w - 1) * 4;
  unsigned ah = L_PL + h * 2560 + r32 * 16, l4 = lane * 4, lak = L_PL + w * 512 + r16 * 64 + g4 * 8, laq = L_PL + 5120 + w * 512 + r16 * 32 + g4 * 8;
  unsigned lav = L_PL + 2560 + w * 512 + g4 * 256 + (r16 >> 2) * 64 + (r16 & 3) * 8, lag = L_G + w * 64 + r16 * 4, lvo = gs * 8 + (h * 32 + r16) * 16;
  f32x4 mk;
#pragma unroll
  for (int r = 0; r < 4; ++r) mk[r] = ((unsigned)(4 * g4 + r - (r16 >> 1) - 1) < 8u) ? ((tb | w | h) ? 0.f : -4.0e9f) : -3.0e38f;
  unsigned gaw = L_GA + w * 8192 + l16, gar = L_GA + l16, vout = r32 * 1024 + h * 32, rdy = C_RDY, bit = 1u << (2 * w), bi2 = 2u << (2 * w), v4 = 4u;
  PIN(ah); PIN(lak); PIN(laq); PIN(lav); PIN(lag); PIN(lvo); PIN(mk); PIN(gaw); PIN(gar); PIN(vout); PIN(rdy); PIN(bit); PIN(bi2); PIN(v4); PIN(l4);
  asm volatile(REP(16, "v_mov_b32 v[224+i_], 0\n\t") REP(4, "v_mov_b32 v[240+i_], -1\n\t") ::: "memory");
  f32x16 acc;
  asm volatile(REP(16, "v_mov_b32 v[16+i_], 0\n\t") : "={v[16:31]}"(acc));
  PIN(vz); PIN(v1); PIN(cst4); PIN(am); PIN(amc); PIN(af0); PIN(af1); PIN(va0); PIN(va1); PIN(va2); PIN(va3); PIN(aW0); PIN(aW1); PIN(ax0); PIN(ax1); PIN(ax2); PIN(ax3); PIN(ap); PIN(apg);
  const unsigned se = WS_KT + w * 6144, sm = WS_KT + 24576 + w * 12288;
  unsigned vT, ae = aK + w * 3072 + ((3 * w) >> 1) * 32, ae1 = ae + 1024 + (w & 1) * 32;
  PIN(ae); PIN(ae1);
  if (w == 0) {
    asm volatile("s_barrier" ::: "memory");
    TS(1);
    GETT();
    const unsigned ak = L_EW + (g4 * 8 + (r16 >> 2)) * 32 + (lane & 3) * 8;
    const int slot = (bi >> 2) * 8 + xcd;
    const int u = isV ? gs : h, fl = (isV ? h : gs) * 32 + sp * 16 + r16;
    unsigned at = slot < 18 ? u * 1024 + fl * 16 + WS_KT + slot * 4096 + isV * 2048 : WS_END, au = fl * 16 + u * 8 + WS_KV + slot * 2048 + isV * 1024;
    float sc = isV ? 1.0f : 0.25f * LOG2E;
    vT = T;
    PIN(at); PIN(au); PIN(sc); PIN(vT);
    f32x4 pa = {0.f, 0.f, 0.f, 0.f}, pb = pa;
#define KVM(a, b) REP(4, ".if i_>0\n\ts_waitcnt lgkmcnt(21-6*i_)\n\t.endif\n\tv_mfma_f32_16x16x32_bf16 %[pa], v[" a "+8*i_:" a "+8*i_+3], v[" b "+8*i_:" b "+8*i_+3], %[pa]\n\t" \
                         ".if i_>0\n\ts_waitcnt lgkmcnt(18-6*i_)\n\t.endif\n\tv_mfma_f32_16x16x32_bf16 %[pb], v[" a "+8*i_+4:" a "+8*i_+7], v[" b "+8*i_+4:" b "+8*i_+7], %[pb]\n\t")
    asm volatile(
      REPV("j_", 2,
        "s_waitcnt vmcnt(17-16*j_)\n\ts_mov_b64 exec, 1\n\tv_mov_b32 v192, 0x1111<<(16*j_)\n\tds_add_u32 %[z], v192 offset:" STR(C_Q) "\n\ts_mov_b64 exec, -1\n\t"
        REP(4, "ds_read_b64_tr_b16 v[192+8*i_:192+8*i_+1], %[ak] offset:j_*8192+i_*2048\n\tds_read_b64_tr_b16 v[192+8*i_+2:192+8*i_+3], %[ak] offset:j_*8192+i_*2048+128\n\t"
               "ds_read_b128 v[128+32*j_+8*i_:128+32*j_+8*i_+3], %[a0] offset:j_*32768+i_*8192\n\t"
               "ds_read_b64_tr_b16 v[192+8*i_+4:192+8*i_+5], %[ak] offset:j_*8192+i_*2048+1024\n\tds_read_b64_tr_b16 v[192+8*i_+6:192+8*i_+7], %[ak] offset:j_*8192+i_*2048+1152\n\t"
               "ds_read_b128 v[128+32*j_+8*i_+4:128+32*j_+8*i_+7], %[a1] offset:j_*32768+i_*8192\n\t")
        "s_waitcnt lgkmcnt(15)\n\ts_cmp_lg_u32 %[v], 0\n\ts_cbranch_scc1 1f\n\t"
        KVM("192", "128+32*j_") "s_branch 2f\n\t1:\n\t" KVM("128+32*j_", "192") "2:\n\t")
      "s_waitcnt vmcnt(0)\n\t"
      REP(8, "ds_read_b128 v[192+4*i_:192+4*i_+3], %[ag] offset:" STR(L_GW) "+i_*64\n\t") "s_nop 1\n\t"
      : [pa] "+v"(pa), [pb] "+v"(pb) : [z] "v"(vz), [ak] "v"(ak), [a0] "v"(aj), [a1] "v"(aj ^ 64), [v] "s"(isV), [ag] "v"(g4 * 16) : "memory", "scc");
    const f32x4 p = (pa + pb) * sc;
    u32x2 o; o[0] = pk_bf16(p[0], p[1]); o[1] = pk_bf16(p[2], p[3]);
    ST128(((u32x4){o[0], vT, o[1], vT}), at, 0);
    __builtin_amdgcn_raw_buffer_store_b64(o, rws, au, 0, SC1);
    POST(C_Q, vz, 8u);
    TS(2);
    f32x4 gz = {0.f, 0.f, 0.f, 0.f};
    asm volatile("s_waitcnt lgkmcnt(0)\n\ts_nop 1\n\t"
      REP(8, "v_mfma_f32_16x16x32_bf16 %[gz], v[192+4*i_:192+4*i_+3], v[128+4*i_:128+4*i_+3], %[gz]\n\t"
             "ds_read_b128 v[192+4*i_:192+4*i_+3], %[ag] offset:" STR(L_GW) "+512+i_*64\n\t")
      REP(8, "s_waitcnt lgkmcnt(7-i_)\n\tv_mfma_f32_16x16x32_bf16 %[gz], v[192+4*i_:192+4*i_+3], v[160+4*i_:160+4*i_+3], %[gz]\n\t")
      "s_nop 7\n\t"
      : [gz] "+v"(gz) : [ag] "v"(g4 * 16) : "memory");
    const float g = __builtin_amdgcn_rcpf(1.f + __builtin_amdgcn_exp2f(-gz[0] * LOG2E));
    TS(3);
    unsigned so = se;
    asm volatile("s_waitcnt vmcnt(0)\n\ts_mov_b64 exec, 1\n\tbuffer_store_dword %[vT], off, %[rs], %[fo] sc1\n\t"
                 "s_mov_b64 exec, 0xffff\n\tbuffer_store_dword %[g], %[l4], %[rs], %[go] offen sc1\n\ts_mov_b64 exec, -1\n\t"
                 TSA(27)
                 GLDT("128", 6, "%[va]", "%[rs]", "%[so]") "s_mov_b32 %[so], %[sm]\n\t" GLDT("152", 12, "%[va]", "%[rs]", "%[so]")
                 : [so] "+s"(so) TSOP
                 : [vT] "v"(vT), [rs] "s"(rw4), [fo] "s"(bid * 4), [g] "v"(g), [l4] "v"(l4), [go] "s"(WS_G + (t0 + jg * 16) * 4), [va] "v"(l16), [sm] "s"(sm) : "memory", "scc");
  } else {
    const u32x4 rw = mkrsrc(wq, 512u * 1024u);
    const unsigned n0 = (w * 5 - 4) >> 1;
    const unsigned vw0 = (n0 * 8 + (lane >> 3)) * 1024u + vsw;
    unsigned inc = 1u, c = rot, so;
    TS(31);
    asm volatile(
      REPX("j_", 8+QD,
        ".if j_<8\n\t"
        "s_and_b32 %[c], %[c], 7\n\ts_add_u32 m0, %[mw], (81920+j_*8192)&131071\n\ts_lshl_b32 %[so], %[c], 16\n\t"
        "buffer_load_dwordx4 %[w0], %[rw], %[so] offen lds\n\tbuffer_load_dwordx4 %[w1], %[rw], %[so] offen offset:1024 lds\n\t"
        "buffer_load_dwordx4 %[w2], %[rw], %[so] offen offset:2048 lds\n\t"
        "s_add_u32 m0, %[mx], j_*8192\n\ts_lshl_b32 %[so], %[c], 7\n\ts_add_u32 %[c], %[c], 1\n\t"
        "buffer_load_dwordx4 %[x0], %[rx], %[so] offen lds\n\tbuffer_load_dwordx4 %[x1], %[rx], %[so] offen offset:1024 lds\n\t"
        ".set n_, 5*" STR(QD) "\n\t"
        ".else\n\t.set n_, 5*(7+" STR(QD) "-j_)\n\t.endif\n\t"
        ".if j_==" STR(B0C) "\n\ts_barrier\n\t" TSA(1) ".endif\n\t"
        ".if j_==7\n\t" TSA(30) ".endif\n\t"
        ".if j_==" STR(EJ) "+" STR(QD) "\n\ts_mov_b32 %[so], %[se]\n\t" TSA(27) GLDT("128", 6, "%[va]", "%[rs]", "%[so]") ".endif\n\t"
        ".if j_==" STR(MJ) "+" STR(QD) "\n\ts_mov_b32 %[so], %[sm]\n\t" GLDT("152", 12, "%[va]", "%[rs]", "%[so]") ".endif\n\t"
        ".if j_>=" STR(EJ) "+" STR(QD) "\n\t.set n_, n_+6\n\t.endif\n\t"
        ".if j_>=" STR(MJ) "+" STR(QD) "\n\t.set n_, n_+12\n\t.endif\n\t"
        ".if j_>=" STR(QD) "\n\t"
        "s_waitcnt vmcnt(n_)\n\ts_mov_b64 exec, 1\n\tds_add_u32 %[z], %[inc] offset:" STR(C_Q) "\n\ts_mov_b64 exec, -1\n\tv_lshlrev_b32 %[inc], 4, %[inc]\n\t"
        ".endif\n\t"
        ".if j_==" STR(QD) "\n\t" TSA(2) ".endif\n\t"
        ".if j_==" STR(QD) "+3\n\t" TSA(28) ".endif\n\t")
      : [c] "+s"(c), [so] "=&s"(so), [inc] "+v"(inc) TSOP
      : [mw] "s"(32768u + n0 * 1024u), [mx] "s"(L_X(0) + xg * 2048), [w0] "v"(vw0), [w1] "v"(vw0 + 7168u), [w2] "v"(vw0 + 14336u), [rw] "s"(rw),
        [x0] "v"(vx0), [x1] "v"(vx1), [rx] "s"(rx), [z] "v"(vz), [se] "s"(se), [sm] "s"(sm), [va] "v"(l16), [rs] "s"(rw4)
      : "memory", "scc");
    TS(3);
    GETT();
    unsigned t, v, spin = 4000000u;
    asm volatile("1:\n\tds_read_b32 %1, %3 offset:" STR(C_Q) "\n\ts_waitcnt lgkmcnt(0)\n\tv_readfirstlane_b32 %0, %1\n\ts_bitcmp1_b32 %0, 3\n\ts_cbranch_scc1 2f\n\t"
                 "s_sub_u32 %2, %2, 1\n\ts_cbranch_scc0 1b\n\t2:" : "=&s"(t), "=&v"(v), "+s"(spin) : "v"(vz) : "memory", "scc");
    if (w == 1 && lane < 12) lds_st<u32x4>(acs, cst4);
  }
  VMW(12);
  TS(4);
  TAGGET(128, 3, 6, se);
  asm volatile("ds_write_b128 %0, v[128:131]\n\tds_write_b128 %1, v[136:139]\n\tds_write_b128 %0, v[144:147] offset:2080" :: "v"(ae), "v"(ae1) : "memory");
  VMW(0);
  TAGGET(152, 6, 12, sm);
  TS(5);
#define GDMA(q) "s_add_u32 m0, %[rb], " q "*4096\n\ts_add_u32 %[tmp], %[so], 98304\n\t" \
      "buffer_load_dwordx4 %[a0], s[88:91], %[so] offen lds\n\tbuffer_load_dwordx4 %[a1], s[88:91], %[so] offen offset:1024 lds\n\t" \
      "buffer_load_dwordx4 %[a2], s[88:91], %[so] offen offset:2048 lds\n\tbuffer_load_dwordx4 %[a3], s[88:91], %[so] offen offset:3072 lds\n\t" \
      "s_cmp_lg_u32 %[tmp], 0x180000\n\ts_cselect_b32 %[so], %[tmp], 0\n\t"
  unsigned sa = ((2u * rot) & 15u) * 98304u;
  RAW_BARRIER();
  KVPUT(152, 8, 6, am, amc);
  POST(C_KV, vz, v1);
  TS(7);
  const int ct = tb * 2 + (hg >> 2);
  const int nt = (hg & 3) * 4 + w;
  const unsigned sh = WS_HL + (tb * 32 + hg * 4) * 1024;
  if (w == 0) {
#define FLAGS(fo) do { unsigned spin = 200000u; \
      asm volatile("1:\n\tbuffer_load_dwordx4 v[128:131], %[va], %[rs], 0 offen offset:" #fo " sc1\n\ts_waitcnt vmcnt(0)\n\t" \
                   "v_bitop3_b32 v128, v128, v129, %[T] bitop3:0x7e\n\tv_bitop3_b32 v130, v130, v131, %[T] bitop3:0x7e\n\t" \
                   "v_or_b32 v128, v128, v130\n\tv_cmp_ne_u32 vcc, 0, v128\n\ts_cbranch_vccz 2f\n\t" \
                   "s_sleep 4\n\ts_sub_u32 %[sp], %[sp], 1\n\ts_cbranch_scc0 1b\n\t2:\n\t" \
                   : [sp] "+s"(spin) : [va] "v"(l16), [rs] "s"(rw4), [T] "s"(T) : "memory", "scc", "vcc"); } while (0)
#define FILL(n, t, o) do { unsigned ml = L_KV + (t) * KVS, sk = WS_KV + (t) * 2048, n_ = n; \
      asm volatile("s_bfm_b64 exec, 2*" #n ", 0\n\tds_write_b128 %[ac], %[c4] offset:" #o "\n\ts_mov_b64 exec, -1\n\t" \
                   "1:\n\ts_mov_b32 m0, %[ml]\n\ts_add_u32 %[ml], %[ml], 2080\n\t" \
                   "buffer_load_dwordx4 %[va], %[rs], %[sk] offen sc1 lds\n\tbuffer_load_dwordx4 %[va], %[rs], %[sk] offen offset:1024 sc1 lds\n\t" \
                   "s_add_u32 %[sk], %[sk], 2048\n\ts_sub_u32 %[nn], %[nn], 1\n\ts_cmp_lg_u32 %[nn], 0\n\ts_cbranch_scc1 1b" \
                   : [ml] "+s"(ml), [sk] "+s"(sk), [nn] "+s"(n_) : [ac] "v"(t < 54 ? af0 : af1), [c4] "v"(cst4), [va] "v"(l16), [rs] "s"(rw4) : "memory", "scc"); } while (0)
    FLAGS(0);
    TS(8);
    {
      unsigned so = WS_O + (tb * 64 + hg * 4) * 1024;
      asm volatile("buffer_inv sc1\n\t"
                   REPV("j_", 5, REP(4, "buffer_store_dwordx4 v[240:243], %[va], %[rs], %[so] offen offset:i_*1024 sc1\n\t")
                        ".if j_==3\n\ts_mov_b32 %[so], %[sh]\n\t.elseif j_==1\n\ts_add_u32 %[so], %[so], 0x1f8000\n\t.else\n\ts_add_u32 %[so], %[so], 0x8000\n\t.endif\n\t")
                   "s_waitcnt vmcnt(0)\n\ts_mov_b64 exec, 1\n\tbuffer_store_dword %[vT], off, %[rs], %[fo] offset:1024 sc1\n\ts_mov_b64 exec, -1"
                   : [so] "+s"(so) : [va] "v"(l16), [rs] "s"(rw4), [sh] "s"(sh), [vT] "v"(vT), [fo] "s"(bid * 4) : "memory", "scc");
    }
    TS(9);
    wait_min<C_PG, 3, 2>(vz, 65536u);
    FILL(12, 18, 0);
    VMW(0);
    POST(C_KV, vz, v4);
    TS(13);
    FLAGS(1024);
    if (bid == 0) asm volatile("s_mov_b64 exec, 1\n\tbuffer_store_dword %0, off, %1, 0 offset:2048 sc1\n\ts_mov_b64 exec, -1" :: "v"(vT), "s"(rw4) : "memory");
    asm volatile("buffer_load_dword v128, %0, %1, %2 offen sc1" :: "v"(l4), "s"(rw4), "s"(WS_G + t0 * 4) : "memory");
    wait_min<C_PG, 3, 2>(vz, 98304u);
    FILL(12, 30, 24960);
    asm volatile("s_waitcnt vmcnt(24)\n\tv_sub_f32 v129, 1.0, v128\n\tds_write2_b32 %0, v128, v129 offset0:32 offset1:96\n\ts_waitcnt vmcnt(0)" :: "v"(l4) : "memory");
    POST(C_KV, vz, v4);
    wait_min<C_PG, 3, 2>(vz, 114688u);
    FILL(12, 42, 49920);
    TS(10);
    wait_min<C_PG, 3, 1>(vz, 131072u);
    PRIO(P_LA0);
    if (tb) {
      unsigned spin = 200000u;
      asm volatile("1:\n\t" REP(4, "buffer_load_dwordx4 v[128+4*i_:128+4*i_+3], %[va], %[rs], %[so] offen offset:i_*1024 sc1\n\t")
                   "s_waitcnt vmcnt(0)\n\t" FULLT("128", "v144") "s_cbranch_vccz 2f\n\ts_sub_u32 %[sp], %[sp], 1\n\ts_cbranch_scc0 1b\n\t2:\n\t"
                   REP(4, "ds_write_b128 %[ah], v[128+4*i_:128+4*i_+3] offset:i_*7168\n\t")
                   : [sp] "+s"(spin) : [va] "v"(l16), [rs] "s"(rw4), [so] "s"(sh - 32 * 1024), [ah] "v"(ah) : "memory", "scc", "vcc");
    } else {
      asm volatile("s_waitcnt vmcnt(0)\n\t" REP(4, "ds_write_b128 %0, v[224:227] offset:i_*7168\n\t") :: "v"(ah) : "memory");
    }
    POST(C_KV, vz, v4);
    TS(12);
    FILL(10, 54, 49920);
    wait_ge<C_PL, 1>(vz, 3);
  } else {
#define GRD(f, wo, xr, xo) \
      "ds_read_b64_tr_b16 v[" f ":" f "+1], %[w0] offset:" wo "\n\tds_read_b64_tr_b16 v[" f "+2:" f "+3], %[w0] offset:" wo "+512\n\t" \
      "ds_read_b128 v[" f "+8:" f "+11], " xr " offset:" xo "\n\t" \
      "ds_read_b64_tr_b16 v[" f "+4:" f "+5], %[w1] offset:" wo "\n\tds_read_b64_tr_b16 v[" f "+6:" f "+7], %[w1] offset:" wo "+512\n\t" \
      "ds_read_b128 v[" f "+12:" f "+15], " xr " offset:" xo "+4096\n\t"
#define GM1(a, f, i, j, c) "v_mfma_f32_32x32x16_bf16 v[" a "], v[" f "+" i ":" f "+" i "+3], v[" f "+" j ":" f "+" j "+3], " c "\n\t"
#define GMM(f) GM1("128:143", f, "0", "8", "v[128:143]") GM1("160:175", f, "4", "8", "v[160:175]") GM1("144:159", f, "0", "12", "v[144:159]") GM1("176:191", f, "4", "12", "v[176:191]")
#define GST(q, xb, xa, xo, lab) GRD("208", q "*4096+2048", xb, "(" q ">>1)*8192") "s_waitcnt lgkmcnt(6)\n\t" GMM("192") lab \
      "s_waitcnt vmcnt(8)\n\t" GRD("192", "((" q "+1)&3)*4096", xa, xo) "s_waitcnt lgkmcnt(6)\n\t" GDMA(q) GMM("208")
    unsigned cnt = 4, tmp;
    asm volatile(
      "s_mov_b64 s[88:89], %[wp]\n\ts_mov_b32 s90, 0x180000\n\ts_mov_b32 s91, 0x20000\n\t"
      REP(4, GDMA("i_"))
      "s_waitcnt vmcnt(12)\n\t"
      TSA(9)
      GRD("192", "0", "%[x0]", "0") GRD("208", "2048", "%[x1]", "0")
      "s_waitcnt lgkmcnt(6)\n\t"
      GM1("128:143", "192", "0", "8", "0") GM1("160:175", "192", "4", "8", "0") GM1("144:159", "192", "0", "12", "0") GM1("176:191", "192", "4", "12", "0")
      "s_branch 2f\n\t"
      "1:\n\t"
      GST("0", "%[x1]", "%[x2]", "0", "2:\n\t") GST("1", "%[x3]", "%[x0]", "8192", "") GST("2", "%[x1]", "%[x2]", "8192", "") GST("3", "%[x3]", "%[x0]", "16384", "")
      "s_setprio 2\n\t"
      "v_add_u32 %[x0], 0x4000, %[x0]\n\tv_add_u32 %[x1], 0x4000, %[x1]\n\tv_add_u32 %[x2], 0x4000, %[x2]\n\tv_add_u32 %[x3], 0x4000, %[x3]\n\t"
      "s_setprio 0\n\t"
      "s_sub_u32 %[cnt], %[cnt], 1\n\t"
      "ds_write_b32 %[pg], %[x0]\n\t"
      "s_cmp_gt_u32 %[cnt], 1\n\t"
      "s_cselect_b32 s90, s90, 0\n\t"
      "s_cmp_lg_u32 %[cnt], 0\n\t"
      "s_cbranch_scc1 1b\n\t"
      "ds_read_b32 v192, %[z] offset:" STR(C_KV) "\n\t"
      "s_waitcnt vmcnt(0) lgkmcnt(0)\n\t"
      "ds_write_b32 %[pg], %[ap]\n\t"
      TSA(10)
      "s_nop 7\n\t"
      "s_setprio " STR(P_PK) "\n\t"
      REP(32, "v_cvt_pk_bf16_f32 v[128+(i_>>2)*8+(i_&3)], v[128+2*i_], v[128+2*i_+1]\n\t")
      "4:\n\tv_readfirstlane_b32 %[tmp], v192\n\ts_cmp_ge_u32 %[tmp], 8\n\ts_cbranch_scc1 5f\n\t"
      "s_sleep 2\n\tds_read_b32 v192, %[z] offset:" STR(C_KV) "\n\ts_waitcnt lgkmcnt(0)\n\ts_add_u32 %[cnt], %[cnt], 1024\n\ts_cbranch_scc0 4b\n\t5:\n\t"
      "s_setprio " STR(P_PL) "\n\t"
      "s_cmp_eq_u32 %[rb], " STR(L_RING) "\n\ts_cbranch_scc1 3f\n\t"
      "s_mov_b64 exec, %[up]\n\t"
      REP(4, "buffer_store_dwordx4 v[144+(i_>>1)*32+(i_&1)*8:144+(i_>>1)*32+(i_&1)*8+3], %[ap], %[rs], %[sh] offen offset:i_*1024 sc1\n\t")
      "s_mov_b64 exec, -1\n\t3:\n\t"
      : [so] "+s"(sa), [cnt] "+s"(cnt), [tmp] "=&s"(tmp), [x0] "+v"(ax0), [x1] "+v"(ax1), [x2] "+v"(ax2), [x3] "+v"(ax3) TSOP
      : [wp] "s"(wqkv), [rb] "s"(rb), [a0] "v"(va0), [a1] "v"(va1), [a2] "v"(va2), [a3] "v"(va3), [w0] "v"(aW0), [w1] "v"(aW1), [pg] "v"(apg), [ap] "v"(ap),
        [z] "v"(vz), [up] "s"(0xffff0000ffff0000ull), [rs] "s"(rw4), [sh] "s"(sh - L_PL - w * 2048 + 3072)
      : "memory", "scc");
    wait_min<C_PG, 3, 1>(vz, 131072u);
    asm volatile(REP(8, "ds_write_b128 %0, v[128+8*i_:128+8*i_+3] offset:((i_>>2)*2+(i_&1))*7168+((i_>>1)&1)*1024\n\t") :: "v"(ap) : "memory");
    POST(C_PL, vz, v1);
    if (tmp < 12) wait_ge<C_KV, 1>(vz, 12);
    wait_ge<C_PL, 1>(vz, 3);
    PRIO(P_LA);
  }
  TS(11);
  GLDN(128, 24, WS_WF + nt * 32768);
  {
    u32x16 c0, c1, c2, c3; u32x4 c4;
    unsigned so = __builtin_amdgcn_readfirstlane(WS_O + ((tb * 2 + (w >> 1)) * 32 + hg * 4) * 1024 + (w & 1) * 256), sw = WS_WF + nt * 32768 + 24576, gt;
#define MM16(d, a, b, c) "v_mfma_f32_16x16x32_bf16 v[" d "+4*i_:" d "+4*i_+3], " a ", v[" b "+4*i_:" b "+4*i_+3], " c "\n\t"
#define BC(i) "v[56+(" i "&2):57+(" i "&2)]"
    asm volatile(
      "s_mov_b32 s92, 0x3eb8aa3b\n\t"
      "s_mov_b64 exec, %[ev]\n\t" REP(4, "ds_read_b64 v[224+4*i_:224+4*i_+1], %[aq] offset:i_*7168\n\t")
      "s_not_b64 exec, exec\n\t" REP(4, "ds_read_b64 v[224+4*i_+2:224+4*i_+3], %[aq] offset:i_*7168\n\t")
      "s_mov_b64 exec, -1\n\t"
      REP(8, "ds_read_b64 v[64+2*i_:64+2*i_+1], %[ak] offset:(i_>>1)*7168+(i_&1)*32\n\t")
      REP(4, "s_waitcnt lgkmcnt(6-2*i_)\n\t" MM16("96", "v[64+4*i_:64+4*i_+3]", "224", "%[mk]"))
      REP(8, "ds_read_b64_tr_b16 v[80+2*i_:80+2*i_+1], %[av] offset:(i_>>1)*7168+(i_&1)*32\n\t")
      "ds_read_b32 %[gt], %[ag]\n\t"
      REP(4, "v_max3_f32 v[56+i_], v[96+4*i_], v[97+4*i_], v[98+4*i_]\n\t")
      REP(4, "v_max_f32 v[56+i_], v[56+i_], v[99+4*i_]\n\t")
      "v_permlane32_swap_b32 v56, v57\n\ts_nop 0\n\tv_permlane32_swap_b32 v58, v59\n\t"
      "v_max_f32 v56, v56, v57\n\tv_max_f32 v58, v58, v59\n\ts_nop 1\n\t"
      "v_permlane16_swap_b32 v56, v58\n\tv_max_f32 v56, v56, v58\n\t"
      "v_mul_f32 v57, 0xbeb8aa3b, v56\n\tv_mul_f32 v59, 0xbeb8aa3b, v56\n\ts_nop 1\n\t"
      "v_permlane16_swap_b32 v57, v59\n\tv_mov_b32 v56, v57\n\tv_mov_b32 v58, v59\n\ts_nop 0\n\t"
      "v_permlane32_swap_b32 v56, v57\n\tv_permlane32_swap_b32 v58, v59\n\t"
      REP(8, ".if (i_&2)==0\n\tv_pk_fma_f32 v[96+2*i_:97+2*i_], v[96+2*i_:97+2*i_], s[92:93], " BC("(i_>>1)") " op_sel:[0,0,0] op_sel_hi:[1,0,0]\n\t"
             ".else\n\tv_pk_fma_f32 v[96+2*i_:97+2*i_], v[96+2*i_:97+2*i_], s[92:93], " BC("(i_>>1)") " op_sel:[0,0,1] op_sel_hi:[1,0,1]\n\t.endif\n\t")
      REP(16, "v_exp_f32 v[96+i_], v[96+i_]\n\t")
      "s_mov_b64 exec, %[ev]\n\t" REP(8, "v_cvt_pk_bf16_f32 v[224+(i_>>1)*4+(i_&1)], v[96+2*i_], v[97+2*i_]\n\t")
      "s_not_b64 exec, exec\n\t" REP(8, "v_cvt_pk_bf16_f32 v[226+(i_>>1)*4+(i_&1)], v[96+2*i_], v[97+2*i_]\n\t")
      "s_mov_b64 exec, -1\n\t"
      REP(4, MM16("112", "%[one]", "224", "0"))
      "s_waitcnt lgkmcnt(0)\n\t"
      REP(4, MM16("64", "v[80+4*i_:80+4*i_+3]", "224", "0"))
      REP(4, "v_rcp_f32 v[56+i_], v[112+4*i_]\n\t")
      REP(4, "v_mul_f32 v[56+i_], v[56+i_], %[gt]\n\t")
      REP(8, ".if (i_&2)==0\n\tv_pk_mul_f32 v[64+2*i_:65+2*i_], v[64+2*i_:65+2*i_], " BC("(i_>>1)") " op_sel:[0,0] op_sel_hi:[1,0]\n\t"
             ".else\n\tv_pk_mul_f32 v[64+2*i_:65+2*i_], v[64+2*i_:65+2*i_], " BC("(i_>>1)") " op_sel:[0,1] op_sel_hi:[1,1]\n\t.endif\n\t")
      REP(8, "v_cvt_pk_bf16_f32 v[80+i_], v[64+2*i_], v[65+2*i_]\n\t")
      REP(4, "buffer_store_dwordx2 v[80+2*i_:80+2*i_+1], %[vo], %[rs], %[so] offen offset:i_*1024 sc1\n\t")
      GLDT("224", 8, "%[va]", "%[rs]", "%[sw]")
      : "=&{v[64:79]}"(c0), "=&{v[80:95]}"(c1), "=&{v[96:111]}"(c2), "=&{v[112:127]}"(c3), "=&{v[56:59]}"(c4), [gt] "=&v"(gt), [sw] "+s"(sw)
      : [so] "s"(so), [ev] "s"(0x5555555555555555ull), [aq] "v"(laq), [ak] "v"(lak), [av] "v"(lav), [ag] "v"(lag), [mk] "v"(mk), [one] "v"(cst4), [vo] "v"(lvo), [rs] "s"(rw4), [va] "v"(l16)
      : "memory", "scc");
  }
  PRIO(P_TA);
  TS(14);
  if (w == 0) { VMW(36); POST(C_KV, vz, v4); POST(C_PL, vz, v1); }
  wait_ge<C_PL, 2>(vz, 4);
  wait_min<C_LP, 4, 2>(vz, L_KV + 24 * KVS);
  TS(15);
  {
#define TCHK(b, r, o, bit) \
      "s_bitcmp1_b32 %[pend], " b "\n\ts_cbranch_scc0 1f\n\t" \
      FULLT(r, "%[tv]") "s_cbranch_vccnz 1f\n\t" \
      REP(4, "ds_write_b128 %[aw], v[" r "+4*i_:" r "+4*i_+3] offset:" o "+i_*1024\n\t") \
      "s_mov_b64 exec, 1\n\tds_or_b32 %[ar], " bit "\n\ts_mov_b64 exec, -1\n\t" \
      "s_bitset0_b32 %[pend], " b "\n\t1:\n\t"
    u32x16 c0, c1, c4; u32x8 c5;
    unsigned so, sb = WS_O + (ct * 32 + w * 8) * 1024, spin = 400000u, pend, done, todo, tv;
    asm volatile(
      "s_mov_b64 s[88:89], %[out]\n\ts_mov_b32 s90, 0\n\ts_mov_b32 s91, 0x20000\n\t"
      "10:\n\t"
      "s_mov_b32 %[pend], 7\n\ts_mov_b32 %[done], 0\n\t"
      "11:\n\t"
      "s_bitcmp1_b32 %[pend], 0\n\ts_cbranch_scc0 1f\n\t"
      "s_mov_b32 %[so], %[sb]\n\t" GLDT("64", 4, "%[va]", "%[rs]", "%[so]")
      "1:\n\t"
      "s_bitcmp1_b32 %[pend], 1\n\ts_cbranch_scc0 1f\n\t"
      "s_add_u32 %[so], %[sb], 0x1000\n\t" GLDT("80", 4, "%[va]", "%[rs]", "%[so]")
      "1:\n\t"
      "s_bitcmp1_b32 %[pend], 2\n\ts_cbranch_scc1 12f\n\t"
      "ds_read_b32 %[tv], %[ar]\n\ts_waitcnt lgkmcnt(0)\n\tv_readfirstlane_b32 %[so], %[tv]\n\t"
      "s_andn2_b32 %[todo], %[so], %[done]\n\ts_cbranch_scc0 12f\n\t"
      "s_mov_b32 %[done], %[so]\n\ts_mov_b32 vcc_lo, s90\n\ts_mov_b32 vcc_hi, 0\n\t"
      REPV("g_", 8,
        "s_bitcmp1_b32 %[todo], g_\n\ts_cbranch_scc0 3f\n\t"
        REP(4, "ds_read_b128 v[96+4*i_:96+4*i_+3], %[rd] offset:g_*4096+i_*1024\n\t")
        "2:\n\t"
        REP(4, ".if g_<7\n\ts_waitcnt lgkmcnt(3)\n\t.else\n\ts_waitcnt lgkmcnt(3-i_)\n\t.endif\n\t"
               "v_mfma_f32_32x32x16_bf16 v[16:31], v[128+16*g_+4*i_:128+16*g_+4*i_+3], v[96+4*i_:96+4*i_+3], v[16:31]\n\t"
               ".if g_<7\n\tds_read_b128 v[96+4*i_:96+4*i_+3], %[rd] offset:g_*4096+4096+i_*1024\n\t.endif\n\t")
        "s_cbranch_vccnz 4f\n\ts_add_u32 %[so], %[wb], g_*4096\n\t"
        REP(4, "buffer_load_dwordx4 v[128+16*g_+4*i_:128+16*g_+4*i_+3], %[va], %[rs], %[so] offen offset:i_*1024 sc1\n\t")
        "4:\n\t"
        ".if g_<7\n\ts_bitcmp1_b32 %[todo], g_+1\n\ts_cbranch_scc1 2f\n\t.endif\n\t"
        "3:\n\t")
      "s_cmp_eq_u32 %[done], 0xff\n\ts_cbranch_scc1 13f\n\t"
      "12:\n\t"
      "s_and_b32 %[pend], %[pend], 3\n\ts_cbranch_scc0 14f\n\t"
      "s_cmp_eq_u32 %[pend], 3\n\ts_cbranch_scc1 1f\n\ts_waitcnt vmcnt(0)\n\t1:\n\ts_waitcnt vmcnt(4)\n\ts_setprio " STR(P_CK) "\n\t"
      TCHK("0", "64", "0", "%[bit]")
      "s_waitcnt vmcnt(0)\n\t"
      TCHK("1", "80", "4096", "%[bi2]")
      "s_setprio " STR(P_TA) "\n\t"
      "14:\n\t"
      "s_cmp_lg_u32 s90, 0\n\ts_cbranch_scc1 1f\n\ts_sleep " STR(ASLP) "\n\t1:\n\t"
      "s_sub_u32 %[spin], %[spin], 1\n\ts_cbranch_scc0 11b\n\t"
      "13:\n\t"
      "s_nop 11\n\t"
      REP(8, "v_cvt_pk_bf16_f32 v[112+i_], v[16+2*i_], v[17+2*i_]\n\t")
      "buffer_store_dwordx4 v[112:115], %[vo], s[88:91], %[oo] offen\n\t"
      "buffer_store_dwordx4 v[116:119], %[vo], s[88:91], %[oo] offen offset:16\n\t"
      "s_cmp_lg_u32 s90, 0\n\ts_cbranch_scc1 15f\n\t"
      TSA(16)
      "v_add_u32 %[aw], 0x8000, %[aw]\n\tv_add_u32 %[rd], 0x8000, %[rd]\n\tv_add_u32 %[ar], 4, %[ar]\n\t"
      "s_add_u32 %[sb], %[sb], 0x200000\n\ts_mov_b32 s90, 0x200000\n\ts_mov_b32 %[spin], 4000000\n\t"
      "1:\n\tds_read_b128 v[96:99], %[ar] offset:" STR(C_LP) "-" STR(C_RDY) "-4\n\ts_waitcnt lgkmcnt(0)\n\t"
      "v_min3_u32 v96, v96, v97, v98\n\tv_min_u32 v96, v96, v99\n\ts_nop 0\n\tv_readfirstlane_b32 %[so], v96\n\t"
      "s_cmp_ge_u32 %[so], " STR(BTRIG) "\n\ts_cbranch_scc1 2f\n\ts_sleep 1\n\ts_sub_u32 %[spin], %[spin], 1\n\ts_cbranch_scc0 1b\n\t2:\n\t"
      "s_sleep " STR(BSLP) "\n\ts_mov_b32 %[spin], 400000\n\t"
      TSA(17)
      "s_branch 10b\n\t"
      "15:\n\t"
      : "+{v[16:31]}"(acc), "=&{v[96:111]}"(c4), "=&{v[112:119]}"(c5), "=&{v[64:79]}"(c0), "=&{v[80:95]}"(c1),
        [so] "=&s"(so), [sb] "+s"(sb), [spin] "+s"(spin), [pend] "=&s"(pend), [done] "=&s"(done), [todo] "=&s"(todo), [tv] "=&v"(tv),
        [aw] "+v"(gaw), [rd] "+v"(gar), [ar] "+v"(rdy) TSOP
      : [va] "v"(l16), [rs] "s"(rw4), [out] "s"(out), [vo] "v"(vout), [oo] "s"(ct * 32768 + nt * 64), [wb] "s"(WS_WF + (16 + nt) * 32768), [bit] "v"(bit), [bi2] "v"(bi2)
      : "memory", "scc", "vcc");
  }
  TS(26);
  TSDUMP();
}

extern "C" void kernel_launch(void* const* d_in, const int* in_sizes, int n_in,
                              void* d_out, int out_size, void* d_ws, size_t ws_size,
                              hipStream_t stream) {
  (void)hipFuncSetAttribute((const void*)fused, hipFuncAttributeMaxDynamicSharedMemorySize, L_TOT);
  fused<<<256, 512, L_TOT, stream>>>((const __bf16*)d_in[0], (const __bf16*)d_in[1], (const __bf16*)d_in[2], (const __bf16*)d_in[4],
                                 (const __bf16*)d_in[5], (const __bf16*)d_in[6], (const __bf16*)d_in[7], (const __bf16*)d_in[8],
                                 (u8*)d_ws, (__bf16*)d_out);
}
```

```cpp
#include <hip/hip_runtime.h>
#include <hip/hip_bf16.h>

typedef __bf16 bf16x2 __attribute__((ext_vector_type(2)));
typedef __bf16 bf16x4 __attribute__((ext_vector_type(4)));
typedef __bf16 bf16x8 __attribute__((ext_vector_type(8)));
typedef __bf16 bf16x16 __attribute__((ext_vector_type(16)));
typedef short s16x4 __attribute__((ext_vector_type(4)));
typedef float f32x4 __attribute__((ext_vector_type(4)));
typedef float f32x8 __attribute__((ext_vector_type(8)));
typedef float f32x16 __attribute__((ext_vector_type(16)));
typedef unsigned u32x2 __attribute__((ext_vector_type(2)));
typedef unsigned u32x4 __attribute__((ext_vector_type(4)));
typedef unsigned u32x8 __attribute__((ext_vector_type(8)));
typedef unsigned u32x16 __attribute__((ext_vector_type(16)));
typedef unsigned char u8;
typedef __attribute__((address_space(3))) u8 lds_u8;

#define STR2(x) #x
#define STR(x) STR2(x)
#define LOG2E 1.4426950408889634f
#define SC1 16
#define QD 3
#define NTM 0
#define NT0 ""
#define NT1 ""
#define NT2 ""
#define NTG ""
#define NTF ""
#define CPF 1
#define CLR 2
#define MJ 8
#define MPRE 1
#define P_PK 2
#define P_LA0 2
#define P_LA 2
#define P_L0 0
#define P_W 0
#define P_F 2
#define P_PL 0
#define P_CK 2
#define BTRIG 0xd0000000
#define P_TA 0
#define P_TB 0
#define ASLP 4
#define BSLP 0

#define WS_FLAG1 0u
#define WS_EPOCH 2048u
#define WS_G     4096u
#define WS_KV    (64u << 10)
#define WS_KT    (256u << 10)
#define WS_WF    (1u << 20)
#define WS_HL    (2u << 20)
#define WS_O     (4u << 20)
#define WS_END   (8u << 20)
#define TS(i) do {} while (0)
#define TSA(i) ""
#define TSG(o, r, sh) ""
#define TSDUMP() do {} while (0)
#define TS0() do {} while (0)
#define TSI()
#define TSOP
#define TSQ(r) do {} while (0)
#define TSQC() do {} while (0)

#define C_KV   0
#define C_PL   4
#define C_RDY  8
#define C_PG   16
#define C_LP   32
#define C_Q    48
#define L_G    128
#define L_GW   1024
#define L_KV   2048
#define KVS    2080
#define L_EW   2048
#define L_TW   18432
#define L_X(j) (49152u + ((j) << 13))
#define L_WQ(j) (32768u + ((81920u + ((j) << 13)) & 131071u))
#define L_RING 114688
#define L_PL   135168
#define L_GA   2048
#define L_TOT  163840

__device__ __forceinline__ unsigned pk_bf16(float lo, float hi) {
  bf16x2 v; v[0] = (__bf16)lo; v[1] = (__bf16)hi;
  return __builtin_bit_cast(unsigned, v);
}
__device__ __forceinline__ u32x4 mkrsrc(unsigned long long a, unsigned bytes) {
  u32x4 r;
  r[0] = (unsigned)a; r[1] = (unsigned)(a >> 32) & 0xffffu; r[2] = bytes; r[3] = 0x00020000u;
  return r;
}
__device__ __forceinline__ bf16x4 tr_read(unsigned a) {
  return __builtin_bit_cast(bf16x4, __builtin_amdgcn_ds_read_tr16_b64_v4i16((__attribute__((address_space(3))) s16x4*)(lds_u8*)(unsigned long long)a));
}
__device__ __forceinline__ bf16x8 cat8(bf16x4 a, bf16x4 b) { return __builtin_shufflevector(a, b, 0, 1, 2, 3, 4, 5, 6, 7); }
template <typename T> __device__ __forceinline__ T lds_ld(unsigned a) { return *(__attribute__((address_space(3))) T*)(lds_u8*)(unsigned long long)a; }
template <typename T> __device__ __forceinline__ void lds_st(unsigned a, T v) { *(__attribute__((address_space(3))) T*)(lds_u8*)(unsigned long long)a = v; }
#define RAW_BARRIER() do { __builtin_amdgcn_s_waitcnt(0xC07F); asm volatile("s_waitcnt lgkmcnt(0)\n\ts_barrier" ::: "memory"); } while (0)
#define VMW(n) asm volatile("s_waitcnt vmcnt(%0)" :: "n"(n) : "memory")
#define ST128(d, vo, so) __builtin_amdgcn_raw_buffer_store_b128((d), rws, (vo) + (so), 0, SC1)
#define SCHED_FENCE() __builtin_amdgcn_sched_barrier(0)
#define PRIO(n) asm volatile("s_setprio " STR(n) ::: "memory")
__device__ __forceinline__ float swap_lo(unsigned a) { return __builtin_bit_cast(float, a); }
#define POST(c, z, v) asm volatile("s_mov_b64 exec, 1\n\tds_add_u32 %0, %1 offset:" STR(c) "\n\ts_mov_b64 exec, -1" :: "v"(z), "v"(v) : "memory")
template <int C, int SLEEP> __device__ __forceinline__ void wait_ge(unsigned z, unsigned target) {
  unsigned t, v, spin = 4000000u;
  asm volatile("1:\n\tds_read_b32 %1, %3 offset:%6\n\ts_waitcnt lgkmcnt(0)\n\tv_readfirstlane_b32 %0, %1\n\ts_cmp_ge_u32 %0, %4\n\ts_cbranch_scc1 2f\n\t"
               "s_sleep %5\n\ts_sub_u32 %2, %2, 1\n\ts_cbranch_scc0 1b\n\t2:"
               : "=&s"(t), "=&v"(v), "+s"(spin) : "v"(z), "s"(target), "n"(SLEEP), "n"(C) : "memory", "scc");
}
template <int C, int N, int SLEEP> __device__ __forceinline__ void wait_min(unsigned z, unsigned target) {
  unsigned t, spin = 4000000u; u32x4 v;
  asm volatile("1:\n\tds_read_b128 v[124:127], %3 offset:%6\n\ts_waitcnt lgkmcnt(0)\n\tv_min3_u32 v124, v124, v125, v126\n\t.if %7==4\n\tv_min_u32 v124, v124, v127\n\t.endif\n\t"
               "s_nop 0\n\tv_readfirstlane_b32 %0, v124\n\t"
               "s_cmp_ge_u32 %0, %4\n\ts_cbranch_scc1 2f\n\ts_sleep %5\n\ts_sub_u32 %2, %2, 1\n\ts_cbranch_scc0 1b\n\t2:"
               : "=&s"(t), "=&{v[124:127]}"(v), "+s"(spin) : "v"(z), "s"(target), "n"(SLEEP), "n"(C), "n"(N) : "memory", "scc");
}
#define DMA2(m, v0, v1, rs, so) asm volatile("s_mov_b32 m0, %0\n\ts_nop 0\n\t" \
    "buffer_load_dwordx4 %1, %3, %4 offen " NT0 " lds\n\tbuffer_load_dwordx4 %2, %3, %4 offen offset:1024 " NT0 " lds" \
    :: "s"(m), "v"(v0), "v"(v1), "s"(rs), "s"(so) : "memory")
#define REPV(v, n, body) ".set " v ", 0\n\t.rept " #n "\n\t" body ".set " v ", " v "+1\n\t.endr\n\t"
#define REPX(v, n, body) REPV(v, n, body)
#define REP(n, body) REPV("i_", n, body)
#define GLDT(r, n, va, rs, so) REP(n, \
    "buffer_load_dwordx4 v[" r "+4*i_:" r "+4*i_+3], " va ", " rs ", " so " offen offset:(i_&3)*1024 sc1\n\t" \
    ".if (i_&3)==3\n\ts_add_u32 " so ", " so ", 0x1000\n\t.endif\n\t")
#define GLDN(r, n, so) do { unsigned so_ = (so); asm volatile(GLDT(#r, n, "%1", "%2", "%0") : "+s"(so_) : "v"(l16), "s"(rw4) : "memory", "scc"); } while (0)
#define TAGT(r, n, t) REP(n, \
    "v_bitop3_b32 v[" r "+8*i_+1], v[" r "+8*i_+1], v[" r "+8*i_+3], " t " bitop3:0x7e\n\t" \
    "v_bitop3_b32 v[" r "+8*i_+5], v[" r "+8*i_+5], v[" r "+8*i_+7], " t " bitop3:0x7e\n\t" \
    ".if i_==0\n\tv_or_b32 v[" r "+7], v[" r "+1], v[" r "+5]\n\t.else\n\t" \
    "v_or3_b32 v[" r "+7], v[" r "+7], v[" r "+8*i_+1], v[" r "+8*i_+5]\n\t.endif\n\t") \
    "v_cmp_ne_u32 vcc, 0, v[" r "+7]\n\t"
#define TAGP(r, n) REP(n, \
    "v_mov_b32 v[" r "+8*i_+1], v[" r "+8*i_+2]\n\t" \
    "v_pk_mov_b32 v[" r "+8*i_+2:" r "+8*i_+3], v[" r "+8*i_+4:" r "+8*i_+5], v[" r "+8*i_+6:" r "+8*i_+7] op_sel:[0,0]\n\t")
#define TAGGET(r, n, n2, so) do { unsigned so_, spin_ = 200000u; asm volatile("1:\n\t" TAGT(#r, n, "%[T]") TAGP(#r, n) "s_cbranch_vccz 2f\n\t" \
    "s_mov_b32 %[o], %[b]\n\t" GLDT(#r, n2, "%[va]", "%[rs]", "%[o]") "s_waitcnt vmcnt(0)\n\ts_sub_u32 %[sp], %[sp], 1\n\ts_cbranch_scc0 1b\n\t2:" \
    : [o] "=&s"(so_), [sp] "+s"(spin_) : [b] "s"(so), [va] "v"(l16), [rs] "s"(rw4), [T] "s"(T) : "memory", "scc", "vcc"); } while (0)
#define FULLT(r, t) "v_max3_u32 " t ", v[" r "], v[" r "+2], v[" r "+4]\n\tv_max3_u32 " t ", " t ", v[" r "+6], v[" r "+8]\n\t" \
    "v_max3_u32 " t ", " t ", v[" r "+10], v[" r "+12]\n\tv_max_u32 " t ", " t ", v[" r "+14]\n\tv_cmp_eq_u32 vcc, -1, " t "\n\t"
#define KVPUT(r, st, n, a, c) asm volatile(REP(n, "ds_write_b128 %0, v[" #r "+" #st "*i_:" #r "+" #st "*i_+3] offset:(i_>>1)*2080+(i_&1)*1024\n\t") \
    "s_mov_b64 exec, (1<<" #n ")-1\n\tds_write_b128 %1, %2\n\ts_mov_b64 exec, -1" :: "v"(a), "v"(c), "v"(cst4) : "memory")
#define GETT() asm volatile("s_waitcnt lgkmcnt(0)\n\ts_mul_i32 %0, s100, 1664525\n\ts_add_u32 %0, %0, 1013904223\n\ts_or_b32 %0, %0, 1" : "=s"(T) :: "memory", "scc")

#define E1(r) "v_exp_f32 v[128+" #r "], v[128+" #r "]\n\t"
#define E4(a, b, c, d) E1(a) E1(b) E1(c) E1(d)
#define PVM(d, a, b) "v_smfmac_f32_32x32x32_bf16 v[" d "], v[" a "], v[" b "], v232\n\t"
#define QKM(d, k, q) "v_mfma_f32_32x32x16_bf16 v[" d "], v[" k "], " q ", 0\n\t"
#define RDL(d, a, t, c) "ds_read_b128 v[" d "], " a " offset:(" t ")*2080+" c "\n\t"
#define DUAL(t, W, X, Ka, V0a, V1a, Kb, V1b) \
  E1(0) E1(1) W PVM("176:191", V1b, "152:159") RDL(V1b, "v235", #t "+1", "1280") E4(2, 3, 4, 5) E1(6) \
  QKM("144:159", Ka, "%[q1]") RDL(Ka, "v233", #t "+2", "0") E4(7, 8, 9, 10) E1(11) E1(12) \
  PVM("160:175", V0a, "128:135") E4(13, 14, 15, 16) E1(17) \
  PVM("160:175", V1a, "136:143") X E4(18, 19, 20, 21) E1(22) \
  QKM("128:143", Kb, "%[q0]") E4(23, 24, 25, 26) E1(27) E1(28) \
  PVM("176:191", V0a, "144:151") RDL(V0a, "v234", #t "+2", "1024") E1(29) E1(30) E1(31)
#define LW1 "s_waitcnt lgkmcnt(1)\n\t"
#define DUALX(t, W, X) DUAL(t, W, X, "192:195", "200:203", "204:207", "196:199", "212:215")
#define DUALY(t, W, X) DUAL(t, W, X, "196:199", "208:211", "212:215", "192:195", "204:207")
__device__ __forceinline__ int lane_id() {
  int l;
  asm volatile("v_mbcnt_lo_u32_b32 %0, -1, 0\n\tv_mbcnt_hi_u32_b32 %0, -1, %0" : "=v"(l));
  return l;
}
#define LANE_VARS \
  TSI() \
  const int lane = lane_id() & 63; \
  const int r32 = lane & 31, h = lane >> 5, r16 = lane & 15, g4 = lane >> 4, gs = g4 & 1; \
  const unsigned l16 = lane * 16; \
  const unsigned aK = L_KV + l16; \
  const unsigned ax = r32 * 128 + ((h ^ ((r32 >> 1) & 7)) * 16); \
  const unsigned awf = (h * 8 + (r16 >> 2)) * 128 + (r16 >> 3) * 64 + gs * 32 + (lane & 3) * 8; \
  const unsigned vsw = ((lane & 7) ^ (gs << 2)) * 16 + hg * 128;
#define PIN(x) asm volatile("" : "+v"(x))

__global__ void __launch_bounds__(512, 2) __attribute__((amdgpu_num_vgpr(64), amdgpu_num_sgpr(88)))
fused(const void*, const void*, const void*, const void*, const void*, const void*, const void*, const void*, void*, void*) {
  TS0();
  asm volatile(
               "s_getpc_b64 s[88:89]\n\t.Lfused_a:\n\tv_min_u32 v132, .Lfused_b-.Lfused_a, %2\n\tv_cmpx_lt_u32 vcc, 501, %3\n\t"
               "global_load_dword v255, v132, s[88:89]\n\ts_mov_b64 exec, -1\n\t"
               "v_mov_b64 v[128:129], 0\n\tv_mov_b64 v[130:131], 0\n\tds_write_b128 %0, v[128:131]\n\ts_waitcnt lgkmcnt(0)\n\t"
               "s_load_dword s100, %1, 0\n\ts_load_dword s101, %1, 0x40\n\ts_barrier"
               :: "v"((threadIdx.x & 7) * 16), "s"(__builtin_amdgcn_kernarg_segment_ptr()), "v"((threadIdx.x + ((blockIdx.x >> 3) & 15) * 10 - 502) * 128), "v"(threadIdx.x)
               : "memory", "vcc", "v255", "s100", "s101");
  const int wid = __builtin_amdgcn_readfirstlane(threadIdx.x >> 6);
  const int bid = blockIdx.x, xcd = bid & 7, bi = bid >> 3;
  const int tb = (xcd >> 1) * 8 + (bi >> 2);
  const int hg = (xcd & 1) * 4 + (bi & 3);
  const unsigned rot = (bi >> 2) + 2 * (bi & 3);
  const int t0 = tb * 64;
#define ARG(i) ((unsigned long long)ka[2 * (i)] | (unsigned long long)ka[2 * (i) + 1] << 32)
#define ARGL() \
  u32x16 ka; u32x4 kb; \
  asm volatile("s_load_dwordx16 %0, %2, 0\n\ts_load_dwordx4 %1, %2, 0x40" : "=&s"(ka), "=&s"(kb) : "s"(__builtin_amdgcn_kernarg_segment_ptr()) : "memory");
#define ARGS() \
  asm volatile("s_waitcnt lgkmcnt(0)" : "+s"(ka), "+s"(kb) :: "memory"); \
  u8* const ws = (u8*)((unsigned long long)kb[0] | (unsigned long long)kb[1] << 32); \
  const __amdgpu_buffer_rsrc_t rws = __builtin_amdgcn_make_buffer_rsrc(ws, 0, WS_END, 0x00020000); \
  const u32x4 rw4 = mkrsrc((unsigned long long)ws, WS_END);
  unsigned T;
  const int w = wid & 3;
  const unsigned ones = 0x3F803F80u;
  f32x16 zero16;
#pragma unroll
  for (int i = 0; i < 16; ++i) zero16[i] = 0.f;

  if (wid >= 4) {
    ARGL()
    LANE_VARS
    const unsigned fn = (bid & 3) * 4 + w, fk = (bid >> 2) & 31;
    const bool hi = (lane & 16) != 0, on = (lane & 27) == 16;
    unsigned vf = (lane >> 2) * 1024 + (lane & 3) * 16;
    {
      const unsigned aV0 = hi ? (on ? L_KV + 1024u : L_KV + 1040u) : aK, aV1 = hi ? aV0 - 256u : aK;
      asm volatile(REP(20, "v_mov_b64 v[152+2*i_:153+2*i_], 0\n\t") REP(2, "v_mov_b64 v[212+2*i_:213+2*i_], 0\n\t") REP(2, "v_mov_b64 v[240+2*i_:241+2*i_], -1\n\t")
                   "v_mov_b32 v232, 0xdddddddd\n\tv_mov_b32 v233, %1\n\tv_mov_b32 v234, %2\n\tv_mov_b32 v235, %3\n\t"
                   "v_mov_b32 v236, 0\n\tv_mov_b32 v237, %4\n\tv_mov_b32 v238, %5\n\tv_mov_b32 v239, %6\n\t"
                   : "+v"(vf) : "v"(aK), "v"(aV0), "v"(aV1), "v"(L_G + 256 + (w >> 1) * 128 + r32 * 4), "s"(C_LP + w * 4), "v"(l16) : "memory");
    }
    ARGS()
    const u32x4 rwo = mkrsrc(bid < 128 ? ARG(2) : ARG(6), 512u * 1024u);
    asm volatile("s_mov_b32 m0, %0\n\ts_nop 0\n\tbuffer_load_dwordx4 %1, %2, %3 offen " NTF " lds"
                 :: "s"(L_TW + w * 1024), "v"(vf), "s"(rwo), "s"(fk * 16384 + fn * 64) : "memory");
#define CLEARS() asm volatile(REP(4, "buffer_store_dwordx4 v[240:243], v239, %0, %1 offen offset:i_*1024 sc1\n\t") "buffer_store_dwordx4 v[240:243], v239, %0, %2 offen sc1" \
                 :: "s"(rw4), "s"(WS_O + (w >> 1) * 0x200000 + ((tb * 2 + (w & 1)) * 32 + hg * 4) * 1024), "s"(WS_HL + (tb * 32 + hg * 4 + w) * 1024) : "memory")
    TS(1);
    VMW(CLR == 0 ? 5 : 0);
    {
      const unsigned ao = L_TW + w * 1024 + (h * 4 + (r16 >> 2)) * 64 + (lane & 1) * 32 + ((lane >> 1) & 1) * 8 + gs * 16;
      ST128(__builtin_bit_cast(u32x4, cat8(tr_read(ao), tr_read(ao + 512))), l16, WS_WF + (((bid >> 7) * 16 + fn) * 32 + fk) * 1024);
    }
    TS(2);
    u32x4 aQ, aQ1;
    {
      u32x16 c0, c1, c2, c3, c4;
      unsigned x0 = L_X(0) + ax + (w >> 1) * 4096, x1 = x0 ^ 32u, x2 = x0 ^ 64u, x3 = x0 ^ 96u, vw, qt, bt = 2u, cn = 0u, wo = 81920u, spin = 4000000u;
#define QPOLL "s_bitcmp1_b32 %[cn], %[bt]\n\ts_cbranch_scc1 3f\n\t" \
      "2:\n\tds_read_b32 %[q0], v236 offset:" STR(C_Q) "\n\ts_waitcnt lgkmcnt(0)\n\tv_readfirstlane_b32 %[cn], %[q0]\n\t" \
      "s_bitcmp1_b32 %[cn], %[bt]\n\ts_cbranch_scc1 3f\n\ts_sub_u32 %[spin], %[spin], 1\n\ts_cbranch_scc0 2b\n\ts_mov_b32 %[spin], 0\n\t" \
      "3:\n\ts_add_u32 %[bt], %[bt], 4\n\t"
#define QRD1(f, i, o) "ds_read_b64_tr_b16 v[" f "+" #i "*4:" f "+" #i "*4+1], %[vw] offset:" o "+" #i "*2048\n\t" \
      "ds_read_b64_tr_b16 v[" f "+" #i "*4+2:" f "+" #i "*4+3], %[vw] offset:" o "+" #i "*2048+512\n\t" \
      "ds_read_b128 v[" f "+16+" #i "*4:" f "+16+" #i "*4+3], %[x" #i "] offset:" o "\n\t"
#define QRD(f, o) QRD1(f, 0, o) QRD1(f, 1, o) QRD1(f, 2, o) QRD1(f, 3, o)
#define QMM(f, n) REP(4, "s_waitcnt lgkmcnt(" n ")\n\tv_mfma_f32_32x32x16_bf16 v[48:63], v[" f "+4*i_:" f "+4*i_+3], v[" f "+16+4*i_:" f "+16+4*i_+3], v[48:63]\n\t")
      asm volatile(
        REP(8, "v_mov_b64 v[48+2*i_:49+2*i_], 0\n\t")
        "1:\n\t"
        "v_add_u32 %[vw], %[wo], %[aw]\n\t"
        QPOLL QRD("64", "0")
        "s_cmp_eq_u32 %[bt], 6\n\ts_cbranch_scc1 4f\n\t"
        QMM("96", "12")
        "4:\n\t"
        QPOLL QRD("96", "8192") QMM("64", "12")
        "v_add_u32 %[x0], 0x4000, %[x0]\n\tv_add_u32 %[x1], 0x4000, %[x1]\n\tv_add_u32 %[x2], 0x4000, %[x2]\n\tv_add_u32 %[x3], 0x4000, %[x3]\n\t"
        "s_add_u32 %[wo], %[wo], 0x4000\n\ts_and_b32 %[wo], %[wo], 0x1ffff\n\t"
        "s_cmp_lg_u32 %[bt], 34\n\ts_cbranch_scc1 1b\n\t"
        QMM("96", "9-3*i_")
        "s_nop 11\n\t"
        REP(8, "v_cvt_pk_bf16_f32 v[40+i_], v[48+2*i_], v[49+2*i_]\n\t")
        : "=&{v[40:43]}"(aQ), "=&{v[44:47]}"(aQ1), "=&{v[48:63]}"(c4), "=&{v[64:79]}"(c0), "=&{v[80:95]}"(c1), "=&{v[96:111]}"(c2), "=&{v[112:127]}"(c3),
          [x0] "+v"(x0), [x1] "+v"(x1), [x2] "+v"(x2), [x3] "+v"(x3), [vw] "=&v"(vw), [q0] "=&v"(qt), [bt] "+s"(bt), [cn] "+s"(cn), [wo] "+s"(wo), [spin] "+s"(spin)
        : [aw] "v"(32768u + (awf ^ ((w & 1) * 64)))
        : "memory", "scc");
    }
    TS(6);
    const unsigned so = WS_O + (2u << 20) + ((tb * 2 + (w >> 1)) * 32 + hg * 4 + (w & 1) * 2) * 1024;
    unsigned long long bad0, bad1;
    {
#define LFIN(d, o, bad, c) \
      "v_rcp_f32 v240, v[" d "+8]\n\t" \
      "v_maximum3_f32 v241, |v[" d "]|, |v[" d "+1]|, |v[" d "+2]|\n\t" \
      "v_maximum3_f32 v242, |v[" d "+3]|, |v[" d "+4]|, |v[" d "+5]|\n\t" \
      "v_maximum3_f32 v241, v241, |v[" d "+6]|, |v[" d "+7]|\n\t" \
      "v_mul_f32 v240, v240, v243\n\t" \
      "v_maximum3_f32 v241, v241, v242, v[" d "+8]\n\t" \
      "v_cmp_nle_f32 vcc, %[lo], v[" d "+8]\n\t" \
      "v_cmp_ngt_f32 " bad ", %[hi], v241\n\t" \
      REP(8, "v_mul_f32 v[" d "+i_], v[" d "+i_], v240\n\t") \
      "s_or_b64 " bad ", " bad ", vcc\n\t" \
      REP(4, "v_cvt_pk_bf16_f32 v[" o "+i_], v[" d "+2*i_], v[" d "+2*i_+1]\n\t") \
      "s_cbranch_scc1 5f\n\t" \
      "buffer_store_dwordx4 v[" o ":" o "+3], v239, %[rs], %[so] offen offset:" c " sc1\n\t" \
      "5:\n\t"
      unsigned tgt = 0u, spin = 4000000u, tmp;
      asm volatile(
        "s_waitcnt vmcnt(0) lgkmcnt(0)\n\ts_setprio 1\n\ts_barrier\n\t"
        TSA(7)
        RDL("192:195", "v233", "0", "0") RDL("196:199", "v233", "1", "0") RDL("200:203", "v234", "0", "1024") RDL("204:207", "v235", "0", "1280") RDL("208:211", "v234", "1", "1024")
        "s_waitcnt lgkmcnt(4)\n\t"
        QKM("128:143", "192:195", "%[q0]")
        "s_nop 11\n\t"
        "1:\n\t"
        DUALX(0, LW1, "ds_write_b32 v238, v233\n\t") DUALY(1, LW1, "ds_read_b32 v243, v237\n\t") DUALX(2, LW1, "s_add_u32 %[tgt], %[tgt], 4\n\t") DUALY(3, LW1, "ds_read_b32 v244, v236\n\t")
        TSG(6, "%[tgt]", 1)
        LW1
        "3:\n\t"
        "v_readfirstlane_b32 %[tmp], v244\n\t"
        "s_cmp_ge_u32 %[tmp], %[tgt]\n\t"
        "s_cbranch_scc0 6f\n\t"
        "2:\n\t"
        DUALX(4, "", "") DUALY(5, LW1, "") DUALX(6, LW1, "") DUALY(7, LW1, "") DUALX(8, LW1, "") DUALY(9, LW1, "") DUALX(10, LW1, "") DUALY(11, LW1, "")
        "v_add_u32 v233, 0x6180, v233\n\t"
        "v_add_u32 v234, 0x6180, v234\n\t"
        "v_add_u32 v235, 0x6180, v235\n\t"
        "s_branch 1b\n\t"
        "7:\n\t"
        "ds_read_b32 v244, v236\n\t"
        "s_waitcnt lgkmcnt(0)\n\t"
        TSG(7, "%[tgt]", 1)
        "s_sub_u32 %[spin], %[spin], 1\n\t"
        "s_cbranch_scc0 3b\n\t"
        "s_mov_b32 %[spin], 0\n\t"
        "s_branch 2b\n\t"
        "6:\n\t"
        "s_cmp_eq_u32 %[tgt], 24\n\t"
        "s_cbranch_scc0 7b\n\t"
        PVM("176:191", "212:215", "152:159")
        LFIN("160", "216", "%[b0]", "0")
        LFIN("176", "224", "%[b1]", "1024")
        ".Lfused_b:\n\t"
        "ds_write_b32 v238, v232\n\t"
        "s_waitcnt lgkmcnt(0)\n\t"
        : [tgt] "+s"(tgt), [spin] "+s"(spin), [tmp] "=&s"(tmp), [b0] "=&s"(bad0), [b1] "=&s"(bad1) TSOP
        : [q0] "v"(aQ), [q1] "v"(aQ1), [lo] "s"(5.4e-20f), [hi] "s"(1.0e36f), [rs] "s"(rw4), [so] "s"(so) : "memory", "scc", "vcc");
    }
    TS(20);
    if ((bad0 | bad1) != 0ull) {
      const int lane = lane_id() & 63;
      const unsigned l16 = lane * 16;
      const bool hi = (lane & 16) != 0, on = (lane & 27) == 16;
#pragma nounroll
      for (int qi = 0; qi < 2; ++qi) {
        if ((qi ? bad1 : bad0) == 0ull) continue;
        const bf16x8 qf = __builtin_bit_cast(bf16x8, qi ? aQ1 : aQ);
        float m = -3.0e38f;
#pragma nounroll
        for (int t = 0; t < 64; ++t) {
          const u32x4 kf = __builtin_amdgcn_raw_buffer_load_b128(rws, l16, WS_KV + t * 2048, SC1);
          f32x16 s = __builtin_amdgcn_mfma_f32_32x32x16_bf16(__builtin_bit_cast(bf16x8, kf), qf, zero16, 0, 0, 0);
#pragma unroll
          for (int i = 0; i < 16; ++i) m = fmaxf(m, s[i]);
        }
        { auto r = __builtin_amdgcn_permlane32_swap(__builtin_bit_cast(unsigned, m), __builtin_bit_cast(unsigned, m), false, false);
          m = fmaxf(swap_lo(r[0]), swap_lo(r[1])); }
        f32x16 F = zero16;
        const unsigned pad = on ? ones : 0u;
#pragma nounroll
        for (int t = 0; t < 64; ++t) {
          const u32x4 kf = __builtin_amdgcn_raw_buffer_load_b128(rws, l16, WS_KV + t * 2048, SC1);
          u32x4 v0 = __builtin_amdgcn_raw_buffer_load_b128(rws, (l16 & ~256u) + 1024, WS_KV + t * 2048, SC1);
          u32x4 v1 = __builtin_amdgcn_raw_buffer_load_b128(rws, (l16 & ~256u) + 1280, WS_KV + t * 2048, SC1);
#pragma unroll
          for (int i = 0; i < 4; ++i) { v0[i] = hi ? pad : v0[i]; v1[i] = hi ? pad : v1[i]; }
          f32x16 s = __builtin_amdgcn_mfma_f32_32x32x16_bf16(__builtin_bit_cast(bf16x8, kf), qf, zero16, 0, 0, 0);
          f32x8 p0, p1;
#pragma unroll
          for (int i = 0; i < 8; ++i) { p0[i] = __builtin_amdgcn_exp2f(s[i] - m); p1[i] = __builtin_amdgcn_exp2f(s[8 + i] - m); }
          F = __builtin_amdgcn_smfmac_f32_32x32x32_bf16(__builtin_bit_cast(bf16x8, v0), __builtin_bit_cast(bf16x16, p0), F, 0xDDDD, 0, 0);
          F = __builtin_amdgcn_smfmac_f32_32x32x32_bf16(__builtin_bit_cast(bf16x8, v1), __builtin_bit_cast(bf16x16, p1), F, 0xDDDD, 0, 0);
        }
        const float iv = __builtin_amdgcn_rcpf(F[8]) * lds_ld<float>(L_G + 256 + (w >> 1) * 128 + (lane & 31) * 4);
        ST128(((u32x4){pk_bf16(F[0] * iv, F[1] * iv), pk_bf16(F[2] * iv, F[3] * iv), pk_bf16(F[4] * iv, F[5] * iv), pk_bf16(F[6] * iv, F[7] * iv)}), l16, so + qi * 1024);
      }
    }
    TSDUMP();
    return;
  }

  ARGL()
  LANE_VARS
  const int sp = (bi >> 1) & 1, isV = bi & 1;
  const int jg = (xcd & 1) * 2 + sp, xg = (jg + w) & 3;
  unsigned vx0 = (unsigned)(t0 + xg * 16 + (lane >> 3)) * 1024u + (unsigned)(((lane & 7) ^ g4) * 16), vx1 = (vx0 ^ 64u) + 7168u;
  const unsigned aj = L_X(0) + (xg * 16 + r16) * 128 + ((g4 ^ (r16 >> 1)) * 16);
  const unsigned n0 = (w * 5 - 4) >> 1;
  unsigned vw0 = (n0 * 8 + (lane >> 3)) * 1024u + vsw, vw1 = vw0 + 7168u, vw2 = vw0 + 14336u, vg = (l16 + rot * 128) & 1023u;
  PIN(vx0); PIN(vx1); PIN(vw0); PIN(vw1); PIN(vw2); PIN(vg);
  ARGS()
  const u32x4 rx = mkrsrc(ARG(0), 2048u * 1024u), rw = mkrsrc(ARG(3), 512u * 1024u);
  unsigned c = rot;
  TS(31);
#define QREQ(nt) "s_and_b32 %[c], %[c], 7\n\ts_add_u32 m0, %[mw], (81920+j_*8192)&131071\n\ts_lshl_b32 %[so], %[c], 16\n\t" \
        "buffer_load_dwordx4 %[w0], %[rw], %[so] offen " nt " lds\n\tbuffer_load_dwordx4 %[w1], %[rw], %[so] offen offset:1024 " nt " lds\n\t" \
        "buffer_load_dwordx4 %[w2], %[rw], %[so] offen offset:2048 " nt " lds\n\t" \
        "s_add_u32 m0, %[mx], j_*8192\n\ts_lshl_b32 %[so], %[c], 7\n\ts_add_u32 %[c], %[c], 1\n\t" \
        "buffer_load_dwordx4 %[x0], %[rx], %[so] offen " nt " lds\n\tbuffer_load_dwordx4 %[x1], %[rx], %[so] offen offset:1024 " nt " lds\n\t"
#define QOPS [mw] "s"(32768u + n0 * 1024u), [mx] "s"(L_X(0) + xg * 2048), [w0] "v"(vw0), [w1] "v"(vw1), [w2] "v"(vw2), [rw] "s"(rw), [x0] "v"(vx0), [x1] "v"(vx1), [rx] "s"(rx)
  if (w == 0) {
    const u32x4 rkv = mkrsrc(isV ? ARG(5) : ARG(4), 16384u), rex = mkrsrc(ARG(7), 1024u);
#pragma nounroll
    for (unsigned j = 0; j < 8; ++j) {
      DMA2(L_EW + j * 2048, l16, l16, rkv, ((j + rot) & 7u) << 11);
      DMA2(L_X(j) + xg * 2048, vx0, vx1, rx, ((j + rot) & 7u) << 7);
    }
    asm volatile("s_mov_b32 m0, %0\n\ts_nop 0\n\tbuffer_load_dwordx4 %1, %2, 0 offen " NT0 " lds" :: "s"(L_GW), "v"(vg), "s"(rex) : "memory");
    TS(30);
    asm volatile("s_load_dword s100, %0, 0x800" :: "s"(ws) : "memory", "s100");
  }
  unsigned vz = 0u, v1 = 1u;
  const unsigned cst = (lane & 1) ? 0u : ones;
  u32x4 cst4 = {cst, cst, cst, cst};
  const unsigned acs = L_KV + (lane >> 1) * KVS + 2048 + (lane & 1) * 16;
  unsigned am = aK + (w ? 2 + 4 * w : 0) * KVS, amc = acs + (w ? 2 + 4 * w : 0) * KVS;
  unsigned rb = (unsigned)w * 16384u;
  asm volatile("s_add_u32 %0, %0, %1" : "+s"(rb) : "n"(L_RING - 16384) : "scc");
  unsigned va0 = (unsigned)(lane >> 3) * 3072u + (unsigned)(w - 1) * 1024u + vsw, va1 = va0 + 23552u, va2 = va1 + 23552u, va3 = va2 + 23552u;
  unsigned aW0 = rb + awf, aW1 = rb + (awf ^ 64u), ax0 = L_X(0) + ax, ax1 = L_X(0) + (ax ^ 32u), ax2 = L_X(0) + (ax ^ 64u), ax3 = L_X(0) + (ax ^ 96u);
  unsigned ap = L_PL + (w == 1 ? 5120 : w * 2560 - 4608) + r32 * 32 + h * 16, apg = C_PG + (w - 1) * 4;
  unsigned ah = L_PL + h * 2560 + r32 * 16, l4 = lane * 4, lak = L_PL + w * 512 + r16 * 64 + g4 * 8, laq = L_PL + 5120 + w * 512 + r16 * 32 + g4 * 8;
  unsigned lav = L_PL + 2560 + w * 512 + g4 * 256 + (r16 >> 2) * 64 + (r16 & 3) * 8, lag = L_G + w * 64 + r16 * 4, lvo = gs * 8 + (h * 32 + r16) * 16;
  f32x4 mk;
#pragma unroll
  for (int r = 0; r < 4; ++r) mk[r] = ((unsigned)(4 * g4 + r - (r16 >> 1) - 1) < 8u) ? ((tb | w | h) ? 0.f : -4.0e9f) : -3.0e38f;
  unsigned gaw = L_GA + w * 8192 + l16, gar = L_GA + l16, vout = r32 * 1024 + h * 32, rdy = C_RDY, bit = 1u << (2 * w + (w & 2) * 2), bi2 = 2u << (2 * w + (w & 2) * 2), v4 = 4u;
  PIN(ah); PIN(lak); PIN(laq); PIN(lav); PIN(lag); PIN(lvo); PIN(mk); PIN(gaw); PIN(gar); PIN(vout); PIN(rdy); PIN(bit); PIN(bi2); PIN(v4); PIN(l4);
  f32x16 acc, acd;
  asm volatile(REP(8, "v_mov_b64 v[224+2*i_:225+2*i_], 0\n\t") REP(2, "v_mov_b64 v[240+2*i_:241+2*i_], -1\n\t") REP(16, "v_mov_b64 v[16+2*i_:17+2*i_], 0\n\t") : "={v[16:31]}"(acc), "={v[32:47]}"(acd) :: "memory");
  PIN(vz); PIN(v1); PIN(cst4); PIN(am); PIN(amc); PIN(va0); PIN(va1); PIN(va2); PIN(va3); PIN(aW0); PIN(aW1); PIN(ax0); PIN(ax1); PIN(ax2); PIN(ax3); PIN(ap); PIN(apg);
  const unsigned sm = WS_KT + 8192 + w * 16384;
  unsigned vT;
  if (w != 0) {
    unsigned so;
    TS(1);
    asm volatile(REPX("j_", QD, QREQ(NT1)) : [c] "+s"(c), [so] "=&s"(so) : QOPS : "memory", "scc");
    TS(30);
    asm volatile("s_load_dword s100, %0, 0x800" :: "s"(ws) : "memory", "s100");
  }
  if (w == 0) {
    GETT();
    const unsigned ak = L_EW + (g4 * 8 + (r16 >> 2)) * 32 + (lane & 3) * 8;
    const int slot = (bi >> 2) * 8 + xcd;
    const int u = isV ? gs : h, fl = (isV ? h : gs) * 32 + sp * 16 + r16;
    unsigned at = slot < 18 ? u * 1024 + fl * 16 + WS_KT + slot * 4096 + isV * 2048 : WS_END, au = fl * 16 + u * 8 + WS_KV + slot * 2048 + isV * 1024;
    float sc = isV ? 1.0f : 0.25f * LOG2E;
    vT = T;
    PIN(at); PIN(au); PIN(sc); PIN(vT);
    f32x4 pa = {0.f, 0.f, 0.f, 0.f}, pb = pa;
#define KVM(a, b) REP(4, ".if i_>0\n\ts_waitcnt lgkmcnt(21-6*i_)\n\t.endif\n\tv_mfma_f32_16x16x32_bf16 %[pa], v[" a "+8*i_:" a "+8*i_+3], v[" b "+8*i_:" b "+8*i_+3], %[pa]\n\t" \
                         ".if i_>0\n\ts_waitcnt lgkmcnt(18-6*i_)\n\t.endif\n\tv_mfma_f32_16x16x32_bf16 %[pb], v[" a "+8*i_+4:" a "+8*i_+7], v[" b "+8*i_+4:" b "+8*i_+7], %[pb]\n\t")
    asm volatile(
      REPV("j_", 2,
        "s_waitcnt vmcnt(17-16*j_)\n\ts_mov_b64 exec, 1\n\tv_mov_b32 v192, 0x1111<<(16*j_)\n\tds_add_u32 %[z], v192 offset:" STR(C_Q) "\n\ts_mov_b64 exec, -1\n\t"
        REP(4, "ds_read_b64_tr_b16 v[192+8*i_:192+8*i_+1], %[ak] offset:j_*8192+i_*2048\n\tds_read_b64_tr_b16 v[192+8*i_+2:192+8*i_+3], %[ak] offset:j_*8192+i_*2048+128\n\t"
               "ds_read_b128 v[128+32*j_+8*i_:128+32*j_+8*i_+3], %[a0] offset:j_*32768+i_*8192\n\t"
               "ds_read_b64_tr_b16 v[192+8*i_+4:192+8*i_+5], %[ak] offset:j_*8192+i_*2048+1024\n\tds_read_b64_tr_b16 v[192+8*i_+6:192+8*i_+7], %[ak] offset:j_*8192+i_*2048+1152\n\t"
               "ds_read_b128 v[128+32*j_+8*i_+4:128+32*j_+8*i_+7], %[a1] offset:j_*32768+i_*8192\n\t")
        "s_waitcnt lgkmcnt(15)\n\ts_cmp_lg_u32 %[v], 0\n\ts_cbranch_scc1 1f\n\t"
        KVM("192", "128+32*j_") "s_branch 2f\n\t1:\n\t" KVM("128+32*j_", "192") "2:\n\t")
      "s_waitcnt vmcnt(0)\n\t"
      REP(8, "ds_read_b128 v[192+4*i_:192+4*i_+3], %[ag] offset:" STR(L_GW) "+i_*64\n\t") "s_nop 1\n\t"
      : [pa] "+v"(pa), [pb] "+v"(pb) : [z] "v"(vz), [ak] "v"(ak), [a0] "v"(aj), [a1] "v"(aj ^ 64), [v] "s"(isV), [ag] "v"(g4 * 16) : "memory", "scc");
    const f32x4 p = (pa + pb) * sc;
    u32x2 o; o[0] = pk_bf16(p[0], p[1]); o[1] = pk_bf16(p[2], p[3]);
    ST128(((u32x4){o[0], vT, o[1], vT}), at, 0);
    __builtin_amdgcn_raw_buffer_store_b64(o, rws, au, 0, SC1);
    TS(2);
    f32x4 gz = {0.f, 0.f, 0.f, 0.f};
    asm volatile("s_waitcnt lgkmcnt(0)\n\ts_nop 1\n\t"
      REP(8, "v_mfma_f32_16x16x32_bf16 %[gz], v[192+4*i_:192+4*i_+3], v[128+4*i_:128+4*i_+3], %[gz]\n\t"
             "ds_read_b128 v[192+4*i_:192+4*i_+3], %[ag] offset:" STR(L_GW) "+512+i_*64\n\t")
      REP(8, "s_waitcnt lgkmcnt(7-i_)\n\tv_mfma_f32_16x16x32_bf16 %[gz], v[192+4*i_:192+4*i_+3], v[160+4*i_:160+4*i_+3], %[gz]\n\t")
      "s_nop 7\n\t"
      : [gz] "+v"(gz) : [ag] "v"(g4 * 16) : "memory");
    const float g = __builtin_amdgcn_rcpf(1.f + __builtin_amdgcn_exp2f(-gz[0] * LOG2E));
    TS(3);
    unsigned so = WS_KT;
    asm volatile("s_waitcnt vmcnt(0)\n\ts_mov_b64 exec, 1\n\tbuffer_store_dword %[vT], off, %[rs], %[fo] sc1\n\t"
                 "s_mov_b64 exec, %[gm]\n\tbuffer_store_dword %[g], %[l4], %[rs], %[go] offen sc1\n\ts_mov_b64 exec, -1\n\t"
                 TSA(27)
                 GLDT("128", 24, "%[va]", "%[rs]", "%[so]") "s_waitcnt vmcnt(0)\n\t"
                 : [so] "+s"(so) TSOP
                 : [vT] "v"(vT), [rs] "s"(rw4), [fo] "s"(bid * 4), [g] "v"(g), [l4] "v"(l4), [go] "s"(WS_G + (t0 + jg * 16) * 4), [gm] "s"(0xffull << (8 * isV)), [va] "v"(l16) : "memory", "scc");
    TS(4);
    TAGGET(128, 12, 24, WS_KT);
    asm volatile("buffer_load_dwordx4 v[244:247], %0, %1, 0 offen sc1" :: "v"(l16), "s"(rw4) : "memory");
    KVPUT(128, 8, 12, am, amc);
    POST(C_KV, vz, v1);
  } else {
    unsigned inc = 1u, so;
    asm volatile(
      ".set j_, " STR(QD) "\n\t.rept 9\n\t"
        ".if j_<8\n\t" QREQ(NT2) ".set n_, 5*" STR(QD) "\n\t"
        ".else\n\t.set n_, 5*(7+" STR(QD) "-j_)\n\t.endif\n\t"
        ".if j_==7\n\t" TSA(29) ".endif\n\t"
        ".if j_==" STR(MJ) "+" STR(QD) "\n\ts_mov_b32 %[so], %[sm]\n\t" TSA(27) GLDT("128", 16, "%[va]", "%[rs]", "%[so]") ".endif\n\t"
        ".if j_>=" STR(MJ) "+" STR(QD) "\n\t.set n_, n_+16\n\t.endif\n\t"
        ".if j_<8+" STR(QD) "\n\t"
        "s_waitcnt vmcnt(n_)\n\ts_mov_b64 exec, 1\n\tds_add_u32 %[z], %[inc] offset:" STR(C_Q) "\n\ts_mov_b64 exec, -1\n\tv_lshlrev_b32 %[inc], 4, %[inc]\n\t"
        ".endif\n\t"
        ".if j_==" STR(QD) "\n\t" TSA(2) ".endif\n\t"
        ".if j_==" STR(QD) "+3\n\t" TSA(28) ".endif\n\t"
      ".set j_, j_+1\n\t.endr\n\t"
      : [c] "+s"(c), [so] "=&s"(so), [inc] "+v"(inc) TSOP
      : QOPS, [z] "v"(vz), [sm] "s"(sm), [va] "v"(l16), [rs] "s"(rw4)
      : "memory", "scc");
    TS(3);
    GETT();
    VMW(0);
    TS(4);
    TAGGET(128, 8, 16, sm);
  }
  TS(5);
#define GDMA(q) "s_add_u32 m0, %[rb], " q "*4096\n\ts_add_u32 %[tmp], %[so], 98304\n\t" \
      "buffer_load_dwordx4 %[a0], s[88:91], %[so] offen " NTG " lds\n\tbuffer_load_dwordx4 %[a1], s[88:91], %[so] offen offset:1024 " NTG " lds\n\t" \
      "buffer_load_dwordx4 %[a2], s[88:91], %[so] offen offset:2048 " NTG " lds\n\tbuffer_load_dwordx4 %[a3], s[88:91], %[so] offen offset:3072 " NTG " lds\n\t" \
      "s_cmp_lg_u32 %[tmp], 0x180000\n\ts_cselect_b32 %[so], %[tmp], 0\n\t"
  unsigned sa = ((2u * rot) & 15u) * 98304u;
  RAW_BARRIER();
  TS(7);
  const int ct = tb * 2 + (hg >> 2);
  const int nt = (hg & 3) * 4 + w;
  const unsigned sh = WS_HL + (tb * 32 + hg * 4) * 1024;
  if (w == 0) {
#define FLAGS(fo, n) do { unsigned spin = 200000u; \
      asm volatile("s_waitcnt vmcnt(%[n_])\n\t1:\n\t" \
                   "v_bitop3_b32 v244, v244, v245, %[T] bitop3:0x7e\n\tv_bitop3_b32 v246, v246, v247, %[T] bitop3:0x7e\n\t" \
                   "v_or_b32 v244, v244, v246\n\tv_cmp_ne_u32 vcc, 0, v244\n\ts_cbranch_vccz 2f\n\t" \
                   "buffer_load_dwordx4 v[244:247], %[va], %[rs], 0 offen offset:" #fo " sc1\n\ts_waitcnt vmcnt(0)\n\t" \
                   "s_sub_u32 %[sp], %[sp], 1\n\ts_cbranch_scc0 1b\n\t2:\n\t" \
                   : [sp] "+s"(spin) : [va] "v"(l16), [rs] "s"(rw4), [T] "s"(T), [n_] "n"(n) : "memory", "scc", "vcc"); } while (0)
#define FLAG2() asm volatile("s_mov_b64 exec, 1\n\tbuffer_store_dword %0, off, %1, %2 offset:1024 sc1\n\ts_mov_b64 exec, -1" :: "v"(vT), "s"(rw4), "s"(bid * 4) : "memory")
    PRIO(2);
    FLAGS(0, (CLR < 2));
    am += 18 * KVS; amc += 18 * KVS;
    PIN(am); PIN(amc);
    PRIO(P_W);
    TS(8);
    GLDN(128, 24, WS_KV + 18 * 2048);
    {
      unsigned so = WS_O + (tb * 64 + hg * 4) * 1024;
      asm volatile(REPV("j_", 5, REP(4, "buffer_store_dwordx4 v[240:243], %[va], %[rs], %[so] offen offset:i_*1024 sc1\n\t")
                        ".if j_==3\n\ts_mov_b32 %[so], %[sh]\n\t.elseif j_==1\n\ts_add_u32 %[so], %[so], 0x1f8000\n\t.else\n\ts_add_u32 %[so], %[so], 0x8000\n\t.endif\n\t")
                   "s_waitcnt vmcnt(0)"
                   : [so] "+s"(so) : [va] "v"(l16), [rs] "s"(rw4), [sh] "s"(sh) : "memory", "scc");
    }
    FLAG2();
    TS(6);
    wait_min<C_PG, 3, 2>(vz, 65536u);
    asm volatile("buffer_load_dwordx4 v[244:247], %0, %1, 0 offen offset:1024 sc1" :: "v"(l16), "s"(rw4) : "memory");
    VMW(1);
    PRIO(P_F);
    TS(9);
    KVPUT(128, 4, 24, am, amc);
    POST(C_KV, vz, v4);
    TS(13);
    FLAGS(1024, 0);
    if (bid == 0) asm volatile("s_mov_b64 exec, 1\n\tbuffer_store_dword %0, off, %1, 0 offset:2048 sc1\n\ts_mov_b64 exec, -1" :: "v"(vT), "s"(rw4) : "memory");
    asm volatile("buffer_load_dword v244, %0, %1, %2 offen sc1\n\ts_waitcnt lgkmcnt(0)" :: "v"(l4), "s"(rw4), "s"(WS_G + t0 * 4) : "memory");
#pragma nounroll
    for (unsigned b = 1; b < 3; ++b) {
      GLDN(128, 24, WS_KV + (18 + 12 * b) * 2048);
      am += 12 * KVS; amc += 12 * KVS;
      PIN(am); PIN(amc);
      asm volatile("s_waitcnt vmcnt(24)\n\tv_sub_f32 v245, 1.0, v244\n\tds_write2_b32 %0, v244, v245 offset0:32 offset1:96" :: "v"(l4) : "memory");
      PRIO(P_W);
      wait_min<C_PG, 3, 2>(vz, 81920u + b * 16384u);
      VMW(0);
      PRIO(P_F);
      KVPUT(128, 4, 24, am, amc);
      POST(C_KV, vz, v4);
      asm volatile("s_waitcnt lgkmcnt(0)" ::: "memory");
    }
    GLDN(128, 20, WS_KV + 54 * 2048);
    am += 12 * KVS; amc += 12 * KVS;
    PIN(am); PIN(amc);
    TS(10);
    PRIO(P_W);
    wait_min<C_PG, 3, 1>(vz, 131072u);
    PRIO(P_F);
    if (tb) asm volatile(REP(4, "buffer_load_dwordx4 v[208+4*i_:208+4*i_+3], %0, %1, %2 offen offset:i_*1024 sc1\n\t") "s_waitcnt vmcnt(4)" :: "v"(l16), "s"(rw4), "s"(sh - 32 * 1024) : "memory");
    else VMW(0);
    KVPUT(128, 4, 20, am, amc);
    POST(C_KV, vz, v4);
    TS(12);
    PRIO(P_LA0);
    if (tb) {
      unsigned spin = 200000u;
      asm volatile("1:\n\ts_waitcnt vmcnt(0)\n\t" FULLT("208", "v244") "s_cbranch_vccz 2f\n\t"
                   REP(4, "buffer_load_dwordx4 v[208+4*i_:208+4*i_+3], %[va], %[rs], %[so] offen offset:i_*1024 sc1\n\t")
                   "s_sub_u32 %[sp], %[sp], 1\n\ts_cbranch_scc0 1b\n\t2:\n\t"
                   REP(4, "ds_write_b128 %[ah], v[208+4*i_:208+4*i_+3] offset:i_*7168\n\t")
                   : [sp] "+s"(spin) : [va] "v"(l16), [rs] "s"(rw4), [so] "s"(sh - 32 * 1024), [ah] "v"(ah) : "memory", "scc", "vcc");
    } else {
      asm volatile(REP(4, "ds_write_b128 %0, v[224:227] offset:i_*7168\n\t") :: "v"(ah) : "memory");
    }
    wait_ge<C_PL, 1>(vz, 3);
  } else {
#define GRD(f, wo, xr, xo) \
      "ds_read_b64_tr_b16 v[" f ":" f "+1], %[w0] offset:" wo "\n\tds_read_b64_tr_b16 v[" f "+2:" f "+3], %[w0] offset:" wo "+512\n\t" \
      "ds_read_b128 v[" f "+8:" f "+11], " xr " offset:" xo "\n\t" \
      "ds_read_b64_tr_b16 v[" f "+4:" f "+5], %[w1] offset:" wo "\n\tds_read_b64_tr_b16 v[" f "+6:" f "+7], %[w1] offset:" wo "+512\n\t" \
      "ds_read_b128 v[" f "+12:" f "+15], " xr " offset:" xo "+4096\n\t"
#define GM1(a, f, i, j, c) "v_mfma_f32_32x32x16_bf16 v[" a "], v[" f "+" i ":" f "+" i "+3], v[" f "+" j ":" f "+" j "+3], " c "\n\t"
#define GMM(f) GM1("128:143", f, "0", "8", "v[128:143]") GM1("160:175", f, "4", "8", "v[160:175]") GM1("144:159", f, "0", "12", "v[144:159]") GM1("176:191", f, "4", "12", "v[176:191]")
#define GST(q, xb, xa, xo, lab) GRD("208", q "*4096+2048", xb, "(" q ">>1)*8192") "s_waitcnt lgkmcnt(6)\n\t" GMM("192") lab \
      "s_waitcnt vmcnt(8)\n\t" GRD("192", "((" q "+1)&3)*4096", xa, xo) "s_waitcnt lgkmcnt(6)\n\t" GDMA(q) GMM("208")
    unsigned cnt = 4, tmp;
    KVPUT(128, 8, 8, am, amc);
    POST(C_KV, vz, v1);
    TS(6);
    asm volatile("s_mov_b64 s[88:89], %[wp]\n\ts_mov_b32 s90, 0x180000\n\ts_mov_b32 s91, 0x20000\n\t" REP(4, GDMA("i_"))
                 : [so] "+s"(sa), [tmp] "=&s"(tmp) : [wp] "s"(ARG(1)), [rb] "s"(rb), [a0] "v"(va0), [a1] "v"(va1), [a2] "v"(va2), [a3] "v"(va3) : "memory", "scc");
    TS(8);
    asm volatile(
      "s_waitcnt vmcnt(12)\n\t"
      TSA(9)
      GRD("192", "0", "%[x0]", "0") GRD("208", "2048", "%[x1]", "0")
      "s_waitcnt lgkmcnt(6)\n\t"
      GM1("128:143", "192", "0", "8", "0") GM1("160:175", "192", "4", "8", "0") GM1("144:159", "192", "0", "12", "0") GM1("176:191", "192", "4", "12", "0")
      "s_branch 2f\n\t"
      "1:\n\t"
      GST("0", "%[x1]", "%[x2]", "0", "2:\n\t") GST("1", "%[x3]", "%[x0]", "8192", "") GST("2", "%[x1]", "%[x2]", "8192", "") GST("3", "%[x3]", "%[x0]", "16384", "")
      "s_setprio 2\n\t"
      "v_add_u32 %[x0], 0x4000, %[x0]\n\tv_add_u32 %[x1], 0x4000, %[x1]\n\tv_add_u32 %[x2], 0x4000, %[x2]\n\tv_add_u32 %[x3], 0x4000, %[x3]\n\t"
      "s_setprio 0\n\t"
      "s_sub_u32 %[cnt], %[cnt], 1\n\t"
      "ds_write_b32 %[pg], %[x0]\n\t"
      "s_cmp_gt_u32 %[cnt], 1\n\t"
      "s_cselect_b32 s90, s90, 0\n\t"
      "s_cmp_lg_u32 %[cnt], 0\n\t"
      "s_cbranch_scc1 1b\n\t"
      "ds_read_b32 v192, %[z] offset:" STR(C_KV) "\n\t"
      "s_waitcnt vmcnt(0) lgkmcnt(0)\n\t"
      "ds_write_b32 %[pg], %[ap]\n\t"
      TSA(10)
      "s_nop 7\n\t"
      "s_setprio " STR(P_PK) "\n\t"
      REP(32, "v_cvt_pk_bf16_f32 v[128+(i_>>2)*8+(i_&3)], v[128+2*i_], v[128+2*i_+1]\n\t")
      "4:\n\tv_readfirstlane_b32 %[tmp], v192\n\ts_cmp_ge_u32 %[tmp], 8\n\ts_cbranch_scc1 5f\n\t"
      "s_sleep 2\n\tds_read_b32 v192, %[z] offset:" STR(C_KV) "\n\ts_waitcnt lgkmcnt(0)\n\ts_add_u32 %[cnt], %[cnt], 1024\n\ts_cbranch_scc0 4b\n\t5:\n\t"
      "s_setprio " STR(P_PL) "\n\t"
      "s_cmp_eq_u32 %[rb], " STR(L_RING) "\n\ts_cbranch_scc1 3f\n\t"
      "s_mov_b64 exec, %[up]\n\t"
      REP(4, "buffer_store_dwordx4 v[144+(i_>>1)*32+(i_&1)*8:144+(i_>>1)*32+(i_&1)*8+3], %[ap], %[rs], %[sh] offen offset:i_*1024 sc1\n\t")
      "s_mov_b64 exec, -1\n\t3:\n\t"
      : [so] "+s"(sa), [cnt] "+s"(cnt), [tmp] "=&s"(tmp), [x0] "+v"(ax0), [x1] "+v"(ax1), [x2] "+v"(ax2), [x3] "+v"(ax3) TSOP
      : [rb] "s"(rb), [a0] "v"(va0), [a1] "v"(va1), [a2] "v"(va2), [a3] "v"(va3), [w0] "v"(aW0), [w1] "v"(aW1), [pg] "v"(apg), [ap] "v"(ap),
        [z] "v"(vz), [up] "s"(0xffff0000ffff0000ull), [rs] "s"(rw4), [sh] "s"(sh - L_PL - w * 2048 + 3072)
      : "memory", "scc");
    wait_min<C_PG, 3, 1>(vz, 131072u);
    asm volatile(REP(8, "ds_write_b128 %0, v[128+8*i_:128+8*i_+3] offset:((i_>>2)*2+(i_&1))*7168+((i_>>1)&1)*1024\n\t") :: "v"(ap) : "memory");
    POST(C_PL, vz, v1);
    if (tmp < 12) wait_ge<C_KV, 1>(vz, 12);
    wait_ge<C_PL, 1>(vz, 3);
    PRIO(P_LA);
  }
  TS(11);
  GLDN(128, 24, WS_WF + nt * 32768);
  {
    u32x16 c0, c1, c2, c3; u32x4 c4;
    unsigned so = __builtin_amdgcn_readfirstlane(WS_O + ((tb * 2 + (w >> 1)) * 32 + hg * 4) * 1024 + (w & 1) * 256), sw = WS_WF + nt * 32768 + 24576, gt;
#define MM16(d, a, b, c) "v_mfma_f32_16x16x32_bf16 v[" d "+4*i_:" d "+4*i_+3], " a ", v[" b "+4*i_:" b "+4*i_+3], " c "\n\t"
#define BC(i) "v[56+(" i "&2):57+(" i "&2)]"
    asm volatile(
      "s_mov_b32 s92, 0x3eb8aa3b\n\t"
      "s_mov_b64 exec, %[ev]\n\t" REP(4, "ds_read_b64 v[224+4*i_:224+4*i_+1], %[aq] offset:i_*7168\n\t")
      "s_not_b64 exec, exec\n\t" REP(4, "ds_read_b64 v[224+4*i_+2:224+4*i_+3], %[aq] offset:i_*7168\n\t")
      "s_mov_b64 exec, -1\n\t"
      REP(8, "ds_read_b64 v[64+2*i_:64+2*i_+1], %[ak] offset:(i_>>1)*7168+(i_&1)*32\n\t")
      REP(4, "s_waitcnt lgkmcnt(6-2*i_)\n\t" MM16("96", "v[64+4*i_:64+4*i_+3]", "224", "%[mk]"))
      REP(8, "ds_read_b64_tr_b16 v[80+2*i_:80+2*i_+1], %[av] offset:(i_>>1)*7168+(i_&1)*32\n\t")
      "ds_read_b32 %[gt], %[ag]\n\t"
      REP(4, "v_max3_f32 v[56+i_], v[96+4*i_], v[97+4*i_], v[98+4*i_]\n\t")
      REP(4, "v_max_f32 v[56+i_], v[56+i_], v[99+4*i_]\n\t")
      "v_permlane32_swap_b32 v56, v57\n\ts_nop 0\n\tv_permlane32_swap_b32 v58, v59\n\t"
      "v_max_f32 v56, v56, v57\n\tv_max_f32 v58, v58, v59\n\ts_nop 1\n\t"
      "v_permlane16_swap_b32 v56, v58\n\tv_max_f32 v56, v56, v58\n\t"
      "v_mul_f32 v57, 0xbeb8aa3b, v56\n\tv_mul_f32 v59, 0xbeb8aa3b, v56\n\ts_nop 1\n\t"
      "v_permlane16_swap_b32 v57, v59\n\tv_mov_b32 v56, v57\n\tv_mov_b32 v58, v59\n\ts_nop 0\n\t"
      "v_permlane32_swap_b32 v56, v57\n\tv_permlane32_swap_b32 v58, v59\n\t"
      REP(8, ".if (i_&2)==0\n\tv_pk_fma_f32 v[96+2*i_:97+2*i_], v[96+2*i_:97+2*i_], s[92:93], " BC("(i_>>1)") " op_sel:[0,0,0] op_sel_hi:[1,0,0]\n\t"
             ".else\n\tv_pk_fma_f32 v[96+2*i_:97+2*i_], v[96+2*i_:97+2*i_], s[92:93], " BC("(i_>>1)") " op_sel:[0,0,1] op_sel_hi:[1,0,1]\n\t.endif\n\t")
      REP(16, "v_exp_f32 v[96+i_], v[96+i_]\n\t")
      "s_mov_b64 exec, %[ev]\n\t" REP(8, "v_cvt_pk_bf16_f32 v[224+(i_>>1)*4+(i_&1)], v[96+2*i_], v[97+2*i_]\n\t")
      "s_not_b64 exec, exec\n\t" REP(8, "v_cvt_pk_bf16_f32 v[226+(i_>>1)*4+(i_&1)], v[96+2*i_], v[97+2*i_]\n\t")
      "s_mov_b64 exec, -1\n\t"
      REP(4, MM16("112", "%[one]", "224", "0"))
      "s_waitcnt lgkmcnt(0)\n\t"
      REP(4, MM16("64", "v[80+4*i_:80+4*i_+3]", "224", "0"))
      REP(4, "v_rcp_f32 v[56+i_], v[112+4*i_]\n\t")
      REP(4, "v_mul_f32 v[56+i_], v[56+i_], %[gt]\n\t")
      REP(8, ".if (i_&2)==0\n\tv_pk_mul_f32 v[64+2*i_:65+2*i_], v[64+2*i_:65+2*i_], " BC("(i_>>1)") " op_sel:[0,0] op_sel_hi:[1,0]\n\t"
             ".else\n\tv_pk_mul_f32 v[64+2*i_:65+2*i_], v[64+2*i_:65+2*i_], " BC("(i_>>1)") " op_sel:[0,1] op_sel_hi:[1,1]\n\t.endif\n\t")
      REP(8, "v_cvt_pk_bf16_f32 v[80+i_], v[64+2*i_], v[65+2*i_]\n\t")
      REP(4, "buffer_store_dwordx2 v[80+2*i_:80+2*i_+1], %[vo], %[rs], %[so] offen offset:i_*1024 sc1\n\t")
      GLDT("224", 8, "%[va]", "%[rs]", "%[sw]")
      : "=&{v[64:79]}"(c0), "=&{v[80:95]}"(c1), "=&{v[96:111]}"(c2), "=&{v[112:127]}"(c3), "=&{v[56:59]}"(c4), [gt] "=&v"(gt), [sw] "+s"(sw)
      : [so] "s"(so), [ev] "s"(0x5555555555555555ull), [aq] "v"(laq), [ak] "v"(lak), [av] "v"(lav), [ag] "v"(lag), [mk] "v"(mk), [one] "v"(cst4), [vo] "v"(lvo), [rs] "s"(rw4), [va] "v"(l16)
      : "memory", "scc");
  }
  PRIO(P_TA);
  TS(14);
  if (w == 0) POST(C_PL, vz, v1);
  wait_ge<C_PL, 2>(vz, 4);
  wait_min<C_LP, 4, 2>(vz, L_KV + 24 * KVS);
  TS(15);
  {
#define TCHK(b, r, o, bit) \
      "s_bitcmp1_b32 %[pend], " b "\n\ts_cbranch_scc0 1f\n\t" \
      FULLT(r, "%[tv]") "s_cbranch_vccnz 1f\n\t" \
      REP(4, "ds_write_b128 %[aw], v[" r "+4*i_:" r "+4*i_+3] offset:" o "+i_*1024\n\t") \
      "s_mov_b64 exec, 1\n\tds_or_b32 %[ar], " bit "\n\ts_mov_b64 exec, -1\n\t" \
      "s_bitset0_b32 %[pend], " b "\n\t1:\n\t"
    u32x16 c0, c1, c4; u32x8 c5;
    unsigned so, sb = WS_O + (ct * 32 + w * 8) * 1024, spin = 400000u, pend, done, todo, tv;
    asm volatile(
      "s_mov_b64 s[88:89], %[out]\n\ts_mov_b32 s90, 0\n\ts_mov_b32 s91, 0x20000\n\t"
      "10:\n\t"
      "s_mov_b32 %[pend], 7\n\ts_mov_b32 %[done], 0\n\t"
      "11:\n\t"
      "s_bitcmp1_b32 %[pend], 0\n\ts_cbranch_scc0 1f\n\t"
      "s_mov_b32 %[so], %[sb]\n\t" GLDT("64", 4, "%[va]", "%[rs]", "%[so]")
      "1:\n\t"
      "s_bitcmp1_b32 %[pend], 1\n\ts_cbranch_scc0 1f\n\t"
      "s_add_u32 %[so], %[sb], 0x1000\n\t" GLDT("80", 4, "%[va]", "%[rs]", "%[so]")
      "1:\n\t"
      "s_bitcmp1_b32 %[pend], 2\n\ts_cbranch_scc1 12f\n\t"
      "ds_read_b32 %[tv], %[ar]\n\ts_waitcnt lgkmcnt(0)\n\tv_readfirstlane_b32 %[so], %[tv]\n\t"
      "s_add_u32 %[todo], %[so], 0x101\n\ts_andn2_b32 %[so], %[so], %[todo]\n\t"
      "s_andn2_b32 %[todo], %[so], %[done]\n\ts_cbranch_scc0 12f\n\t"
      "s_mov_b32 %[done], %[so]\n\ts_mov_b32 vcc_lo, s90\n\ts_mov_b32 vcc_hi, 0\n\t"
      REPV("g_", 8,
        "s_bitcmp1_b32 %[todo], g_+(g_&4)\n\ts_cbranch_scc0 3f\n\t"
        REP(4, "ds_read_b128 v[96+4*i_:96+4*i_+3], %[rd] offset:g_*4096+i_*1024\n\t")
        "2:\n\t"
        REP(4, ".if g_<7\n\ts_waitcnt lgkmcnt(3)\n\t.else\n\ts_waitcnt lgkmcnt(3-i_)\n\t.endif\n\t"
               "v_mfma_f32_32x32x16_bf16 v[16+(g_&4)*4:31+(g_&4)*4], v[128+16*g_+4*i_:128+16*g_+4*i_+3], v[96+4*i_:96+4*i_+3], v[16+(g_&4)*4:31+(g_&4)*4]\n\t"
               ".if g_<7\n\tds_read_b128 v[96+4*i_:96+4*i_+3], %[rd] offset:g_*4096+4096+i_*1024\n\t.endif\n\t")
        "s_cbranch_vccnz 4f\n\ts_add_u32 %[so], %[wb], g_*4096\n\t"
        REP(4, "buffer_load_dwordx4 v[128+16*g_+4*i_:128+16*g_+4*i_+3], %[va], %[rs], %[so] offen offset:i_*1024 sc1\n\t")
        "4:\n\t"
        ".if g_<7\n\ts_bitcmp1_b32 %[todo], g_+1+((g_+1)&4)\n\ts_cbranch_scc1 2f\n\t.endif\n\t"
        "3:\n\t")
      "s_cmp_eq_u32 %[done], 0xf0f\n\ts_cbranch_scc1 13f\n\t"
      "12:\n\t"
      "s_and_b32 %[pend], %[pend], 3\n\ts_cbranch_scc0 14f\n\t"
      "s_cmp_eq_u32 %[pend], 3\n\ts_cbranch_scc1 1f\n\ts_waitcnt vmcnt(0)\n\t1:\n\ts_waitcnt vmcnt(4)\n\ts_setprio " STR(P_CK) "\n\t"
      TCHK("0", "64", "0", "%[bit]")
      "s_waitcnt vmcnt(0)\n\t"
      TCHK("1", "80", "4096", "%[bi2]")
      "s_setprio " STR(P_TA) "\n\t"
      "14:\n\t"
      "s_cmp_lg_u32 s90, 0\n\ts_cbranch_scc1 1f\n\ts_sleep " STR(ASLP) "\n\t1:\n\t"
      "s_sub_u32 %[spin], %[spin], 1\n\ts_cbranch_scc0 11b\n\t"
      "13:\n\t"
      "s_nop 11\n\t"
      REP(8, "v_pk_add_f32 v[96+2*i_:97+2*i_], v[16+2*i_:17+2*i_], v[32+2*i_:33+2*i_]\n\t")
      REP(8, "v_cvt_pk_bf16_f32 v[112+i_], v[96+2*i_], v[97+2*i_]\n\t")
      "buffer_store_dwordx4 v[112:115], %[vo], s[88:91], %[oo] offen\n\t"
      "buffer_store_dwordx4 v[116:119], %[vo], s[88:91], %[oo] offen offset:16\n\t"
      "s_cmp_lg_u32 s90, 0\n\ts_cbranch_scc1 15f\n\t"
      TSA(16)
      "v_add_u32 %[aw], 0x8000, %[aw]\n\tv_add_u32 %[rd], 0x8000, %[rd]\n\tv_add_u32 %[ar], 4, %[ar]\n\t"
      "s_add_u32 %[sb], %[sb], 0x200000\n\ts_mov_b32 s90, 0x200000\n\ts_mov_b32 %[spin], 4000000\n\t"
      "1:\n\tds_read_b128 v[96:99], %[ar] offset:" STR(C_LP) "-" STR(C_RDY) "-4\n\ts_waitcnt lgkmcnt(0)\n\t"
      "v_min3_u32 v96, v96, v97, v98\n\tv_min_u32 v96, v96, v99\n\ts_nop 0\n\tv_readfirstlane_b32 %[so], v96\n\t"
      "s_cmp_ge_u32 %[so], " STR(BTRIG) "\n\ts_cbranch_scc1 2f\n\ts_sleep 1\n\ts_sub_u32 %[spin], %[spin], 1\n\ts_cbranch_scc0 1b\n\t2:\n\t"
      "s_sleep " STR(BSLP) "\n\ts_mov_b32 %[spin], 400000\n\t"
      TSA(17)
      "s_branch 10b\n\t"
      "15:\n\t"
      : "+{v[16:31]}"(acc), "+{v[32:47]}"(acd), "=&{v[96:111]}"(c4), "=&{v[112:119]}"(c5), "=&{v[64:79]}"(c0), "=&{v[80:95]}"(c1),
        [so] "=&s"(so), [sb] "+s"(sb), [spin] "+s"(spin), [pend] "=&s"(pend), [done] "=&s"(done), [todo] "=&s"(todo), [tv] "=&v"(tv),
        [aw] "+v"(gaw), [rd] "+v"(gar), [ar] "+v"(rdy) TSOP
      : [va] "v"(l16), [rs] "s"(rw4), [out] "s"((unsigned long long)kb[2] | (unsigned long long)kb[3] << 32), [vo] "v"(vout), [oo] "s"(ct * 32768 + nt * 64), [wb] "s"(WS_WF + (16 + nt) * 32768), [bit] "v"(bit), [bi2] "v"(bi2)
      : "memory", "scc", "vcc");
  }
  TS(26);
  TSDUMP();
}

extern "C" void kernel_launch(void* const* d_in, const int* in_sizes, int n_in,
                              void* d_out, int out_size, void* d_ws, size_t ws_size,
                              hipStream_t stream) {
  (void)hipFuncSetAttribute((const void*)fused, hipFuncAttributeMaxDynamicSharedMemorySize, L_TOT);
  fused<<<256, 512, L_TOT, stream>>>(d_in[0], d_in[1], d_in[2], d_in[4], d_in[5], d_in[6], d_in[7], d_in[8], d_ws, d_out);
}
```

```cpp
#include <hip/hip_runtime.h>
#include <hip/hip_bf16.h>

typedef __bf16 bf16x2 __attribute__((ext_vector_type(2)));
typedef __bf16 bf16x4 __attribute__((ext_vector_type(4)));
typedef __bf16 bf16x8 __attribute__((ext_vector_type(8)));
typedef __bf16 bf16x16 __attribute__((ext_vector_type(16)));
typedef short s16x4 __attribute__((ext_vector_type(4)));
typedef float f32x4 __attribute__((ext_vector_type(4)));
typedef float f32x8 __attribute__((ext_vector_type(8)));
typedef float f32x16 __attribute__((ext_vector_type(16)));
typedef unsigned u32x2 __attribute__((ext_vector_type(2)));
typedef unsigned u32x4 __attribute__((ext_vector_type(4)));
typedef unsigned u32x8 __attribute__((ext_vector_type(8)));
typedef unsigned u32x16 __attribute__((ext_vector_type(16)));
typedef unsigned char u8;
typedef __attribute__((address_space(3))) u8 lds_u8;

#define STR2(x) #x
#define STR(x) STR2(x)
#define LOG2E 1.4426950408889634f
#define SC1 16
#define QD 3
#define NTM 0
#define NT0 ""
#define NT1 ""
#define NT2 ""
#define NTG ""
#define NTF ""
#define CPF 1
#define CLR 2
#define MJ 8
#define MPRE 1
#define GP 0
#define PFN 16
#define PFJ 7
#define HP 0
#define HX 0
#define WQARG 3
#define XOFF 0
#define SWZ 2
#define KSL 1
#define P_PK 2
#define P_LA0 2
#define P_LA 2
#define P_L0 0
#define P_W 0
#define P_F 0
#define P_PL 0
#define P_CK 2
#define BTRIG 0xd0000000
#define P_TA 0
#define P_TB 0
#define ASLP 4
#define BSLP 0

#define WS_FLAG1 0u
#define WS_EPOCH 2048u
#define WS_G     4096u
#define WS_KV    (64u << 10)
#define WS_KT    (256u << 10)
#define WS_WF    (1u << 20)
#define WS_HL    (2u << 20)
#define WS_O     (4u << 20)
#define WS_END   (8u << 20)
#define TS(i) do {} while (0)
#define TSA(i) ""
#define TSG(o, r, sh) ""
#define TSDUMP() do {} while (0)
#define TS0() do {} while (0)
#define TSI()
#define TSOP
#define TSQ(r) do {} while (0)
#define TSQC() do {} while (0)

#define C_KV   0
#define C_PL   4
#define C_RDY  8
#define C_PG   16
#define C_LP   32
#define C_Q    48
#define L_G    128
#define L_GW   1024
#define L_KV   2048
#define KVS    2080
#define L_EW   2048
#define L_TW   18432
#define L_X(j) (49152u + ((j) << 13))
#define L_WQ(j) (32768u + ((81920u + ((j) << 13)) & 131071u))
#define L_RING 114688
#define L_PL   135168
#define L_GA   2048
#define L_TOT  163840

__device__ __forceinline__ unsigned pk_bf16(float lo, float hi) {
  bf16x2 v; v[0] = (__bf16)lo; v[1] = (__bf16)hi;
  return __builtin_bit_cast(unsigned, v);
}
__device__ __forceinline__ u32x4 mkrsrc(unsigned long long a, unsigned bytes) {
  u32x4 r;
  r[0] = (unsigned)a; r[1] = (unsigned)(a >> 32) & 0xffffu; r[2] = bytes; r[3] = 0x00020000u;
  return r;
}
__device__ __forceinline__ bf16x4 tr_read(unsigned a) {
  return __builtin_bit_cast(bf16x4, __builtin_amdgcn_ds_read_tr16_b64_v4i16((__attribute__((address_space(3))) s16x4*)(lds_u8*)(unsigned long long)a));
}
__device__ __forceinline__ bf16x8 cat8(bf16x4 a, bf16x4 b) { return __builtin_shufflevector(a, b, 0, 1, 2, 3, 4, 5, 6, 7); }
template <typename T> __device__ __forceinline__ T lds_ld(unsigned a) { return *(__attribute__((address_space(3))) T*)(lds_u8*)(unsigned long long)a; }
template <typename T> __device__ __forceinline__ void lds_st(unsigned a, T v) { *(__attribute__((address_space(3))) T*)(lds_u8*)(unsigned long long)a = v; }
#define RAW_BARRIER() do { __builtin_amdgcn_s_waitcnt(0xC07F); asm volatile("s_waitcnt lgkmcnt(0)\n\ts_barrier" ::: "memory"); } while (0)
#define VMW(n) asm volatile("s_waitcnt vmcnt(%0)" :: "n"(n) : "memory")
#define ST128(d, vo, so) __builtin_amdgcn_raw_buffer_store_b128((d), rws, (vo) + (so), 0, SC1)
#define SCHED_FENCE() __builtin_amdgcn_sched_barrier(0)
#define PRIO(n) asm volatile("s_setprio " STR(n) ::: "memory")
__device__ __forceinline__ float swap_lo(unsigned a) { return __builtin_bit_cast(float, a); }
#define POST(c, z, v) asm volatile("s_mov_b64 exec, 1\n\tds_add_u32 %0, %1 offset:" STR(c) "\n\ts_mov_b64 exec, -1" :: "v"(z), "v"(v) : "memory")
template <int C, int SLEEP> __device__ __forceinline__ void wait_ge(unsigned z, unsigned target) {
  unsigned t, v, spin = 4000000u;
  asm volatile("1:\n\tds_read_b32 %1, %3 offset:%6\n\ts_waitcnt lgkmcnt(0)\n\tv_readfirstlane_b32 %0, %1\n\ts_cmp_ge_u32 %0, %4\n\ts_cbranch_scc1 2f\n\t"
               "s_sleep %5\n\ts_sub_u32 %2, %2, 1\n\ts_cbranch_scc0 1b\n\t2:"
               : "=&s"(t), "=&v"(v), "+s"(spin) : "v"(z), "s"(target), "n"(SLEEP), "n"(C) : "memory", "scc");
}
template <int C, int N, int SLEEP> __device__ __forceinline__ void wait_min(unsigned z, unsigned target) {
  unsigned t, spin = 4000000u; u32x4 v;
  asm volatile("1:\n\tds_read_b128 v[124:127], %3 offset:%6\n\ts_waitcnt lgkmcnt(0)\n\tv_min3_u32 v124, v124, v125, v126\n\t.if %7==4\n\tv_min_u32 v124, v124, v127\n\t.endif\n\t"
               "s_nop 0\n\tv_readfirstlane_b32 %0, v124\n\t"
               "s_cmp_ge_u32 %0, %4\n\ts_cbranch_scc1 2f\n\ts_sleep %5\n\ts_sub_u32 %2, %2, 1\n\ts_cbranch_scc0 1b\n\t2:"
               : "=&s"(t), "=&{v[124:127]}"(v), "+s"(spin) : "v"(z), "s"(target), "n"(SLEEP), "n"(C), "n"(N) : "memory", "scc");
}
#define DMA2(m, v0, v1, rs, so) asm volatile("s_mov_b32 m0, %0\n\ts_nop 0\n\t" \
    "buffer_load_dwordx4 %1, %3, %4 offen " NT0 " lds\n\tbuffer_load_dwordx4 %2, %3, %4 offen offset:1024 " NT0 " lds" \
    :: "s"(m), "v"(v0), "v"(v1), "s"(rs), "s"(so) : "memory")
#define REPV(v, n, body) ".set " v ", 0\n\t.rept " #n "\n\t" body ".set " v ", " v "+1\n\t.endr\n\t"
#define REPX(v, n, body) REPV(v, n, body)
#define REP(n, body) REPV("i_", n, body)
#define GLDT(r, n, va, rs, so) REP(n, \
    "buffer_load_dwordx4 v[" r "+4*i_:" r "+4*i_+3], " va ", " rs ", " so " offen offset:(i_&3)*1024 sc1\n\t" \
    ".if (i_&3)==3\n\ts_add_u32 " so ", " so ", 0x1000\n\t.endif\n\t")
#define GLDN(r, n, so) do { unsigned so_ = (so); asm volatile(GLDT(#r, n, "%1", "%2", "%0") : "+s"(so_) : "v"(l16), "s"(rw4) : "memory", "scc"); } while (0)
#define TAGT(r, n, t) REP(n, \
    "v_bitop3_b32 v[" r "+8*i_+1], v[" r "+8*i_+1], v[" r "+8*i_+3], " t " bitop3:0x7e\n\t" \
    "v_bitop3_b32 v[" r "+8*i_+5], v[" r "+8*i_+5], v[" r "+8*i_+7], " t " bitop3:0x7e\n\t" \
    ".if i_==0\n\tv_or_b32 v[" r "+7], v[" r "+1], v[" r "+5]\n\t.else\n\t" \
    "v_or3_b32 v[" r "+7], v[" r "+7], v[" r "+8*i_+1], v[" r "+8*i_+5]\n\t.endif\n\t") \
    "v_cmp_ne_u32 vcc, 0, v[" r "+7]\n\t"
#define TAGP(r, n) REP(n, \
    "v_mov_b32 v[" r "+8*i_+1], v[" r "+8*i_+2]\n\t" \
    "v_pk_mov_b32 v[" r "+8*i_+2:" r "+8*i_+3], v[" r "+8*i_+4:" r "+8*i_+5], v[" r "+8*i_+6:" r "+8*i_+7] op_sel:[0,0]\n\t")
#define TAGGET(r, n, n2, so) do { unsigned so_, spin_ = 200000u; asm volatile("1:\n\t" TAGT(#r, n, "%[T]") TAGP(#r, n) "s_cbranch_vccz 2f\n\t" \
    "s_mov_b32 %[o], %[b]\n\t" GLDT(#r, n2, "%[va]", "%[rs]", "%[o]") "s_waitcnt vmcnt(0)\n\ts_sub_u32 %[sp], %[sp], 1\n\ts_cbranch_scc0 1b\n\t2:" \
    : [o] "=&s"(so_), [sp] "+s"(spin_) : [b] "s"(so), [va] "v"(l16), [rs] "s"(rw4), [T] "s"(T) : "memory", "scc", "vcc"); } while (0)
#define FULLT(r, t) "v_max3_u32 " t ", v[" r "], v[" r "+2], v[" r "+4]\n\tv_max3_u32 " t ", " t ", v[" r "+6], v[" r "+8]\n\t" \
    "v_max3_u32 " t ", " t ", v[" r "+10], v[" r "+12]\n\tv_max_u32 " t ", " t ", v[" r "+14]\n\tv_cmp_eq_u32 vcc, -1, " t "\n\t"
#define KVPUT(r, st, n, a, c) asm volatile(REP(n, "ds_write_b128 %0, v[" #r "+" #st "*i_:" #r "+" #st "*i_+3] offset:(i_>>1)*2080+(i_&1)*1024\n\t") \
    "s_mov_b64 exec, (1<<" #n ")-1\n\tds_write_b128 %1, %2\n\ts_mov_b64 exec, -1" :: "v"(a), "v"(c), "v"(cst4) : "memory")
#define KVPUTS(r, st, n, a, c) asm volatile(REP(n, "ds_write_b128 %0, v[" #r "+" #st "*i_:" #r "+" #st "*i_+3] offset:(i_>>1)*2080+(i_&1)*1024\n\t" \
    ".if (i_&1) && " STR(KSL) "\n\ts_sleep " STR(KSL) "\n\t.endif\n\t") \
    "s_mov_b64 exec, (1<<" #n ")-1\n\tds_write_b128 %1, %2\n\ts_mov_b64 exec, -1" :: "v"(a), "v"(c), "v"(cst4) : "memory")
#define GETT() asm volatile("s_waitcnt lgkmcnt(0)\n\ts_mul_i32 %0, s100, 1664525\n\ts_add_u32 %0, %0, 1013904223\n\ts_or_b32 %0, %0, 1" : "=s"(T) :: "memory", "scc")

#define E1(r) "v_exp_f32 v[128+" #r "], v[128+" #r "]\n\t"
#define E4(a, b, c, d) E1(a) E1(b) E1(c) E1(d)
#define PVM(d, a, b) "v_smfmac_f32_32x32x32_bf16 v[" d "], v[" a "], v[" b "], v232\n\t"
#define QKM(d, k, q) "v_mfma_f32_32x32x16_bf16 v[" d "], v[" k "], " q ", 0\n\t"
#define RDL(d, a, t, c) "ds_read_b128 v[" d "], " a " offset:(" t ")*2080+" c "\n\t"
#define DUAL(t, W, X, Ka, V0a, V1a, Kb, V1b) \
  E1(0) E1(1) W PVM("176:191", V1b, "152:159") RDL(V1b, "v235", #t "+1", "1280") E4(2, 3, 4, 5) E1(6) \
  QKM("144:159", Ka, "%[q1]") RDL(Ka, "v233", #t "+2", "0") E4(7, 8, 9, 10) E1(11) E1(12) \
  PVM("160:175", V0a, "128:135") E4(13, 14, 15, 16) E1(17) \
  PVM("160:175", V1a, "136:143") X E4(18, 19, 20, 21) E1(22) \
  QKM("128:143", Kb, "%[q0]") E4(23, 24, 25, 26) E1(27) E1(28) \
  PVM("176:191", V0a, "144:151") RDL(V0a, "v234", #t "+2", "1024") E1(29) E1(30) E1(31)
#define LW1 "s_waitcnt lgkmcnt(1)\n\t"
#define DUALX(t, W, X) DUAL(t, W, X, "192:195", "200:203", "204:207", "196:199", "212:215")
#define DUALY(t, W, X) DUAL(t, W, X, "196:199", "208:211", "212:215", "192:195", "204:207")
__device__ __forceinline__ int lane_id() {
  int l;
  asm volatile("v_mbcnt_lo_u32_b32 %0, -1, 0\n\tv_mbcnt_hi_u32_b32 %0, -1, %0" : "=v"(l));
  return l;
}
#define LANE_VARS \
  TSI() \
  const int lane = lane_id() & 63; \
  const int r32 = lane & 31, h = lane >> 5, r16 = lane & 15, g4 = lane >> 4, gs = g4 & 1; \
  const unsigned l16 = lane * 16; \
  const unsigned aK = L_KV + l16; \
  const unsigned ax = r32 * 128 + ((h ^ ((r32 >> 1) & 7)) * 16); \
  const unsigned awf = (h * 8 + (r16 >> 2)) * 128 + (r16 >> 3) * 64 + gs * 32 + (lane & 3) * 8; \
  const unsigned vsw = ((lane & 7) ^ (gs << 2)) * 16 + hg * 128;
#define PIN(x) asm volatile("" : "+v"(x))

__global__ void __launch_bounds__(512, 2) __attribute__((amdgpu_num_vgpr(64), amdgpu_num_sgpr(88)))
fused(const void*, const void*, const void*, const void*, const void*, const void*, const void*, const void*, void*, void*) {
  TS0();
  asm volatile(
               "s_getpc_b64 s[88:89]\n\t.Lfused_a:\n\tv_min_u32 v132, .Lfused_b-.Lfused_a, %2\n\tv_cmpx_lt_u32 vcc, 501, %3\n\t"
               "global_load_dword v255, v132, s[88:89]\n\ts_mov_b64 exec, -1\n\t"
               "v_mov_b64 v[128:129], 0\n\tv_mov_b64 v[130:131], 0\n\tds_write_b128 %0, v[128:131]\n\ts_waitcnt lgkmcnt(0)\n\t"
               "s_load_dword s100, %1, 0\n\ts_load_dword s101, %1, 0x40\n\ts_barrier"
               :: "v"((threadIdx.x & 7) * 16), "s"(__builtin_amdgcn_kernarg_segment_ptr()), "v"((threadIdx.x + ((blockIdx.x >> 3) & 15) * 10 - 502) * 128), "v"(threadIdx.x)
               : "memory", "vcc", "v255", "s100", "s101");
  const int wid = __builtin_amdgcn_readfirstlane(threadIdx.x >> 6);
  const int bid = blockIdx.x, xcd = bid & 7, bi = bid >> 3;
  const int tb = (xcd >> 1) * 8 + (bi >> 2);
  const int hg = ((xcd & 1) ^ HX) * 4 + ((bi + HP) & 3);
  const unsigned rot = (2 * ((bi >> 2) + bi)) & 6;
  const int t0 = tb * 64;
#define ARG(i) ((unsigned long long)ka[2 * (i)] | (unsigned long long)ka[2 * (i) + 1] << 32)
#define ARGL() \
  u32x16 ka; u32x4 kb; \
  asm volatile("s_load_dwordx16 %0, %2, 0\n\ts_load_dwordx4 %1, %2, 0x40" : "=&s"(ka), "=&s"(kb) : "s"(__builtin_amdgcn_kernarg_segment_ptr()) : "memory");
#define ARGS() \
  asm volatile("s_waitcnt lgkmcnt(0)" : "+s"(ka), "+s"(kb) :: "memory"); \
  u8* const ws = (u8*)((unsigned long long)kb[0] | (unsigned long long)kb[1] << 32); \
  const __amdgpu_buffer_rsrc_t rws = __builtin_amdgcn_make_buffer_rsrc(ws, 0, WS_END, 0x00020000); \
  const u32x4 rw4 = mkrsrc((unsigned long long)ws, WS_END);
  unsigned T;
  const int w = wid & 3;
  const unsigned ones = 0x3F803F80u;
  f32x16 zero16;
#pragma unroll
  for (int i = 0; i < 16; ++i) zero16[i] = 0.f;

  if (wid >= 4) {
    ARGL()
    LANE_VARS
    const unsigned fn = (bid & 3) * 4 + w, fk = (bid >> 2) & 31;
    const bool hi = (lane & 16) != 0, on = (lane & 27) == 16;
    unsigned vf = (lane >> 2) * 1024 + (lane & 3) * 16;
    {
      const unsigned aV0 = hi ? (on ? L_KV + 1024u : L_KV + 1040u) : aK, aV1 = hi ? aV0 - 256u : aK;
      asm volatile(REP(20, "v_mov_b64 v[152+2*i_:153+2*i_], 0\n\t") REP(2, "v_mov_b64 v[212+2*i_:213+2*i_], 0\n\t") REP(2, "v_mov_b64 v[240+2*i_:241+2*i_], -1\n\t")
                   "v_mov_b32 v232, 0xdddddddd\n\tv_mov_b32 v233, %1\n\tv_mov_b32 v234, %2\n\tv_mov_b32 v235, %3\n\t"
                   "v_mov_b32 v236, 0\n\tv_mov_b32 v237, %4\n\tv_mov_b32 v238, %5\n\tv_mov_b32 v239, %6\n\t"
                   : "+v"(vf) : "v"(aK), "v"(aV0), "v"(aV1), "v"(L_G + 256 + (w >> 1) * 128 + r32 * 4), "s"(C_LP + w * 4), "v"(l16) : "memory");
    }
    ARGS()
    const u32x4 rwo = mkrsrc(bid < 128 ? ARG(2) : ARG(6), 512u * 1024u);
    asm volatile("s_mov_b32 m0, %0\n\ts_nop 0\n\tbuffer_load_dwordx4 %1, %2, %3 offen " NTF " lds"
                 :: "s"(L_TW + w * 1024), "v"(vf), "s"(rwo), "s"(fk * 16384 + fn * 64) : "memory");
#define CLEARS() asm volatile(REP(4, "buffer_store_dwordx4 v[240:243], v239, %0, %1 offen offset:i_*1024 sc1\n\t") "buffer_store_dwordx4 v[240:243], v239, %0, %2 offen sc1" \
                 :: "s"(rw4), "s"(WS_O + (w >> 1) * 0x200000 + ((tb * 2 + (w & 1)) * 32 + hg * 4) * 1024), "s"(WS_HL + (tb * 32 + hg * 4 + w) * 1024) : "memory")
    TS(1);
    VMW(CLR == 0 ? 5 : 0);
    {
      const unsigned ao = L_TW + w * 1024 + (h * 4 + (r16 >> 2)) * 64 + (lane & 1) * 32 + ((lane >> 1) & 1) * 8 + gs * 16;
      ST128(__builtin_bit_cast(u32x4, cat8(tr_read(ao), tr_read(ao + 512))), l16, WS_WF + (((bid >> 7) * 16 + fn) * 32 + fk) * 1024);
    }
    TS(2);
    u32x4 aQ, aQ1;
    {
      u32x16 c0, c1, c2, c3, c4;
      const unsigned xb = L_X(0) + ax + (w >> 1) * 4096;
      unsigned x0, x1, x2, x3, xs = rot << 13, vw, qt, bt = 2u, cn = 0u, wo = 81920u, spin = 4000000u;
#define QPOLL "s_bitcmp1_b32 %[cn], %[bt]\n\ts_cbranch_scc1 3f\n\t" \
      "2:\n\tds_read_b32 %[q0], v236 offset:" STR(C_Q) "\n\ts_waitcnt lgkmcnt(0)\n\tv_readfirstlane_b32 %[cn], %[q0]\n\t" \
      "s_bitcmp1_b32 %[cn], %[bt]\n\ts_cbranch_scc1 3f\n\ts_sub_u32 %[spin], %[spin], 1\n\ts_cbranch_scc0 2b\n\ts_mov_b32 %[spin], 0\n\t" \
      "3:\n\ts_add_u32 %[bt], %[bt], 4\n\t"
#define QRD1(f, i, o) "ds_read_b64_tr_b16 v[" f "+" #i "*4:" f "+" #i "*4+1], %[vw] offset:" o "+" #i "*2048\n\t" \
      "ds_read_b64_tr_b16 v[" f "+" #i "*4+2:" f "+" #i "*4+3], %[vw] offset:" o "+" #i "*2048+512\n\t" \
      "ds_read_b128 v[" f "+16+" #i "*4:" f "+16+" #i "*4+3], %[x" #i "] offset:" o "\n\t"
#define QRD(f, o) QRD1(f, 0, o) QRD1(f, 1, o) QRD1(f, 2, o) QRD1(f, 3, o)
#define QMM(f, n) REP(4, "s_waitcnt lgkmcnt(" n ")\n\tv_mfma_f32_32x32x16_bf16 v[48:63], v[" f "+4*i_:" f "+4*i_+3], v[" f "+16+4*i_:" f "+16+4*i_+3], v[48:63]\n\t")
      asm volatile(
        REP(8, "v_mov_b64 v[48+2*i_:49+2*i_], 0\n\t")
        "1:\n\t"
        "v_add_u32 %[vw], %[wo], %[aw]\n\t"
        "v_add_u32 %[x0], %[xs], %[b0]\n\tv_add_u32 %[x1], %[xs], %[b1]\n\tv_add_u32 %[x2], %[xs], %[b2]\n\tv_add_u32 %[x3], %[xs], %[b3]\n\t"
        QPOLL QRD("64", "0")
        "s_cmp_eq_u32 %[bt], 6\n\ts_cbranch_scc1 4f\n\t"
        QMM("96", "12")
        "4:\n\t"
        QPOLL QRD("96", "8192") QMM("64", "12")
        "s_add_u32 %[xs], %[xs], 0x4000\n\ts_and_b32 %[xs], %[xs], 0xffff\n\t"
        "s_add_u32 %[wo], %[wo], 0x4000\n\ts_and_b32 %[wo], %[wo], 0x1ffff\n\t"
        "s_cmp_lg_u32 %[bt], 34\n\ts_cbranch_scc1 1b\n\t"
        QMM("96", "9-3*i_")
        "s_nop 11\n\t"
        REP(8, "v_cvt_pk_bf16_f32 v[40+i_], v[48+2*i_], v[49+2*i_]\n\t")
        : "=&{v[40:43]}"(aQ), "=&{v[44:47]}"(aQ1), "=&{v[48:63]}"(c4), "=&{v[64:79]}"(c0), "=&{v[80:95]}"(c1), "=&{v[96:111]}"(c2), "=&{v[112:127]}"(c3),
          [x0] "=&v"(x0), [x1] "=&v"(x1), [x2] "=&v"(x2), [x3] "=&v"(x3), [xs] "+s"(xs), [vw] "=&v"(vw), [q0] "=&v"(qt), [bt] "+s"(bt), [cn] "+s"(cn), [wo] "+s"(wo), [spin] "+s"(spin)
        : [aw] "v"(32768u + (awf ^ ((w & 1) * 64))), [b0] "v"(xb), [b1] "v"(xb ^ 32u), [b2] "v"(xb ^ 64u), [b3] "v"(xb ^ 96u)
        : "memory", "scc");
    }
    TS(6);
    const unsigned so = WS_O + (2u << 20) + ((tb * 2 + (w >> 1)) * 32 + hg * 4 + (w & 1) * 2) * 1024;
    unsigned long long bad0, bad1;
    {
#define LFIN(d, o, bad, c) \
      "v_rcp_f32 v240, v[" d "+8]\n\t" \
      "v_maximum3_f32 v241, |v[" d "]|, |v[" d "+1]|, |v[" d "+2]|\n\t" \
      "v_maximum3_f32 v242, |v[" d "+3]|, |v[" d "+4]|, |v[" d "+5]|\n\t" \
      "v_maximum3_f32 v241, v241, |v[" d "+6]|, |v[" d "+7]|\n\t" \
      "v_mul_f32 v240, v240, v243\n\t" \
      "v_maximum3_f32 v241, v241, v242, v[" d "+8]\n\t" \
      "v_cmp_nle_f32 vcc, %[lo], v[" d "+8]\n\t" \
      "v_cmp_ngt_f32 " bad ", %[hi], v241\n\t" \
      REP(8, "v_mul_f32 v[" d "+i_], v[" d "+i_], v240\n\t") \
      "s_or_b64 " bad ", " bad ", vcc\n\t" \
      REP(4, "v_cvt_pk_bf16_f32 v[" o "+i_], v[" d "+2*i_], v[" d "+2*i_+1]\n\t") \
      "s_cbranch_scc1 5f\n\t" \
      "buffer_store_dwordx4 v[" o ":" o "+3], v239, %[rs], %[so] offen offset:" c " sc1\n\t" \
      "5:\n\t"
      unsigned tgt = 0u, spin = 4000000u, tmp;
      asm volatile(
        "s_waitcnt vmcnt(0) lgkmcnt(0)\n\ts_setprio 1\n\ts_barrier\n\t"
        TSA(7)
        RDL("192:195", "v233", "0", "0") RDL("196:199", "v233", "1", "0") RDL("200:203", "v234", "0", "1024") RDL("204:207", "v235", "0", "1280") RDL("208:211", "v234", "1", "1024")
        "s_waitcnt lgkmcnt(4)\n\t"
        QKM("128:143", "192:195", "%[q0]")
        "s_nop 11\n\t"
        "1:\n\t"
        DUALX(0, LW1, "ds_write_b32 v238, v233\n\t") DUALY(1, LW1, "ds_read_b32 v243, v237\n\t") DUALX(2, LW1, "s_add_u32 %[tgt], %[tgt], 4\n\t") DUALY(3, LW1, "ds_read_b32 v244, v236\n\t")
        TSG(6, "%[tgt]", 1)
        LW1
        "3:\n\t"
        "v_readfirstlane_b32 %[tmp], v244\n\t"
        "s_cmp_ge_u32 %[tmp], %[tgt]\n\t"
        "s_cbranch_scc0 6f\n\t"
        "2:\n\t"
        DUALX(4, "", "") DUALY(5, LW1, "") DUALX(6, LW1, "") DUALY(7, LW1, "") DUALX(8, LW1, "") DUALY(9, LW1, "") DUALX(10, LW1, "") DUALY(11, LW1, "")
        "v_add_u32 v233, 0x6180, v233\n\t"
        "v_add_u32 v234, 0x6180, v234\n\t"
        "v_add_u32 v235, 0x6180, v235\n\t"
        "s_branch 1b\n\t"
        "7:\n\t"
        "ds_read_b32 v244, v236\n\t"
        "s_waitcnt lgkmcnt(0)\n\t"
        TSG(7, "%[tgt]", 1)
        "s_sub_u32 %[spin], %[spin], 1\n\t"
        "s_cbranch_scc0 3b\n\t"
        "s_mov_b32 %[spin], 0\n\t"
        "s_branch 2b\n\t"
        "6:\n\t"
        "s_cmp_eq_u32 %[tgt], 24\n\t"
        "s_cbranch_scc0 7b\n\t"
        PVM("176:191", "212:215", "152:159")
        LFIN("160", "216", "%[b0]", "0")
        LFIN("176", "224", "%[b1]", "1024")
        ".Lfused_b:\n\t"
        "ds_write_b32 v238, v232\n\t"
        "s_waitcnt lgkmcnt(0)\n\t"
        : [tgt] "+s"(tgt), [spin] "+s"(spin), [tmp] "=&s"(tmp), [b0] "=&s"(bad0), [b1] "=&s"(bad1) TSOP
        : [q0] "v"(aQ), [q1] "v"(aQ1), [lo] "s"(5.4e-20f), [hi] "s"(1.0e36f), [rs] "s"(rw4), [so] "s"(so) : "memory", "scc", "vcc");
    }
    TS(20);
    if ((bad0 | bad1) != 0ull) {
      const int lane = lane_id() & 63;
      const unsigned l16 = lane * 16;
      const bool hi = (lane & 16) != 0, on = (lane & 27) == 16;
#pragma nounroll
      for (int qi = 0; qi < 2; ++qi) {
        if ((qi ? bad1 : bad0) == 0ull) continue;
        const bf16x8 qf = __builtin_bit_cast(bf16x8, qi ? aQ1 : aQ);
        float m = -3.0e38f;
#pragma nounroll
        for (int t = 0; t < 64; ++t) {
          const u32x4 kf = __builtin_amdgcn_raw_buffer_load_b128(rws, l16, WS_KV + t * 2048, SC1);
          f32x16 s = __builtin_amdgcn_mfma_f32_32x32x16_bf16(__builtin_bit_cast(bf16x8, kf), qf, zero16, 0, 0, 0);
#pragma unroll
          for (int i = 0; i < 16; ++i) m = fmaxf(m, s[i]);
        }
        { auto r = __builtin_amdgcn_permlane32_swap(__builtin_bit_cast(unsigned, m), __builtin_bit_cast(unsigned, m), false, false);
          m = fmaxf(swap_lo(r[0]), swap_lo(r[1])); }
        f32x16 F = zero16;
        const unsigned pad = on ? ones : 0u;
#pragma nounroll
        for (int t = 0; t < 64; ++t) {
          const u32x4 kf = __builtin_amdgcn_raw_buffer_load_b128(rws, l16, WS_KV + t * 2048, SC1);
          u32x4 v0 = __builtin_amdgcn_raw_buffer_load_b128(rws, (l16 & ~256u) + 1024, WS_KV + t * 2048, SC1);
          u32x4 v1 = __builtin_amdgcn_raw_buffer_load_b128(rws, (l16 & ~256u) + 1280, WS_KV + t * 2048, SC1);
#pragma unroll
          for (int i = 0; i < 4; ++i) { v0[i] = hi ? pad : v0[i]; v1[i] = hi ? pad : v1[i]; }
          f32x16 s = __builtin_amdgcn_mfma_f32_32x32x16_bf16(__builtin_bit_cast(bf16x8, kf), qf, zero16, 0, 0, 0);
          f32x8 p0, p1;
#pragma unroll
          for (int i = 0; i < 8; ++i) { p0[i] = __builtin_amdgcn_exp2f(s[i] - m); p1[i] = __builtin_amdgcn_exp2f(s[8 + i] - m); }
          F = __builtin_amdgcn_smfmac_f32_32x32x32_bf16(__builtin_bit_cast(bf16x8, v0), __builtin_bit_cast(bf16x16, p0), F, 0xDDDD, 0, 0);
          F = __builtin_amdgcn_smfmac_f32_32x32x32_bf16(__builtin_bit_cast(bf16x8, v1), __builtin_bit_cast(bf16x16, p1), F, 0xDDDD, 0, 0);
        }
        const float iv = __builtin_amdgcn_rcpf(F[8]) * lds_ld<float>(L_G + 256 + (w >> 1) * 128 + (lane & 31) * 4);
        ST128(((u32x4){pk_bf16(F[0] * iv, F[1] * iv), pk_bf16(F[2] * iv, F[3] * iv), pk_bf16(F[4] * iv, F[5] * iv), pk_bf16(F[6] * iv, F[7] * iv)}), l16, so + qi * 1024);
      }
    }
    TSDUMP();
    return;
  }

  ARGL()
  LANE_VARS
  const int sp = (bi >> 1) & 1, isV = bi & 1;
  const int jg = (xcd & 1) * 2 + sp, xg = (jg + w) & 3;
  unsigned vx0 = (unsigned)(t0 + xg * 16 + (lane >> 3)) * 1024u + (unsigned)(((lane & 7) ^ g4) * 16), vx1 = (vx0 ^ 64u) + 7168u;
  const unsigned aj = L_X(0) + (xg * 16 + r16) * 128 + ((g4 ^ (r16 >> 1)) * 16);
  const unsigned n0 = (w * 5 - 4) >> 1;
  unsigned vw0 = (n0 * 8 + (lane >> 3)) * 1024u + vsw, vw1 = vw0 + 7168u, vw2 = vw0 + 14336u;
  PIN(vx0); PIN(vx1); PIN(vw0); PIN(vw1); PIN(vw2);
  ARGS()
  const u32x4 rx = mkrsrc(ARG(0), (XOFF == 1 && w) || (XOFF == 2 && !w) || XOFF == 3 ? 0u : 2048u * 1024u), rw = mkrsrc(ARG(WQARG), 512u * 1024u);
  unsigned c = rot;
  TS(31);
#define QREQ(nt) "s_and_b32 %[c], %[c], 7\n\ts_add_u32 m0, %[mw], (81920+j_*8192)&131071\n\ts_lshl_b32 %[so], %[c], 16\n\t" \
        "buffer_load_dwordx4 %[w0], %[rw], %[so] offen " nt " lds\n\tbuffer_load_dwordx4 %[w1], %[rw], %[so] offen offset:1024 " nt " lds\n\t" \
        "buffer_load_dwordx4 %[w2], %[rw], %[so] offen offset:2048 " nt " lds\n\t" \
        "s_lshr_b32 %[so], %[so], 3\n\ts_add_u32 m0, %[mx], %[so]\n\ts_lshl_b32 %[so], %[c], 7\n\ts_add_u32 %[c], %[c], 1\n\t" \
        "buffer_load_dwordx4 %[x0], %[rx], %[so] offen " nt " lds\n\tbuffer_load_dwordx4 %[x1], %[rx], %[so] offen offset:1024 " nt " lds\n\t"
#define QOPS [mw] "s"(32768u + n0 * 1024u), [mx] "s"(L_X(0) + xg * 2048), [w0] "v"(vw0), [w1] "v"(vw1), [w2] "v"(vw2), [rw] "s"(rw), [x0] "v"(vx0), [x1] "v"(vx1), [rx] "s"(rx)
  if (w == 0) {
    const u32x4 rkv = mkrsrc(isV ? ARG(5) : ARG(4), 16384u), rex = mkrsrc(ARG(7), 1024u);
#pragma nounroll
    for (unsigned j = 0; j < 8; ++j) {
      DMA2(L_EW + j * 2048, l16, l16, rkv, j << 11);
      DMA2(L_X(j) + xg * 2048, vx0, vx1, rx, j << 7);
    }
    asm volatile("s_mov_b32 m0, %0\n\ts_nop 0\n\tbuffer_load_dwordx4 %1, %2, 0 offen " NT0 " lds" :: "s"(L_GW), "v"(l16), "s"(rex) : "memory");
    TS(30);
    asm volatile("s_load_dword s100, %0, 0x800" :: "s"(ws) : "memory", "s100");
  }
  unsigned vz = 0u, v1 = 1u;
  const unsigned cst = (lane & 1) ? 0u : ones;
  u32x4 cst4 = {cst, cst, cst, cst};
  const unsigned acs = L_KV + (lane >> 1) * KVS + 2048 + (lane & 1) * 16;
  unsigned am = aK + (w ? 2 + 4 * w : 0) * KVS, amc = acs + (w ? 2 + 4 * w : 0) * KVS;
  unsigned rb = (unsigned)w * 16384u;
  asm volatile("s_add_u32 %0, %0, %1" : "+s"(rb) : "n"(L_RING - 16384) : "scc");
  unsigned va0 = (unsigned)(lane >> 3) * 3072u + (unsigned)(w - 1) * 1024u + vsw, va1 = va0 + 23552u, va2 = va1 + 23552u, va3 = va2 + 23552u;
  unsigned aW0 = rb + awf, aW1 = rb + (awf ^ 64u), ax0 = L_X(0) + ax, ax1 = L_X(0) + (ax ^ 32u), ax2 = L_X(0) + (ax ^ 64u), ax3 = L_X(0) + (ax ^ 96u);
  unsigned ap = L_PL + (w == 1 ? 5120 : w * 2560 - 4608) + r32 * 32 + h * 16, apg = C_PG + (w - 1) * 4;
  unsigned ah = L_PL + h * 2560 + r32 * 16, l4 = lane * 4, lak = L_PL + w * 512 + r16 * 64 + g4 * 8, laq = L_PL + 5120 + w * 512 + r16 * 32 + g4 * 8;
  unsigned lav = L_PL + 2560 + w * 512 + g4 * 256 + (r16 >> 2) * 64 + (r16 & 3) * 8, lag = L_G + w * 64 + r16 * 4, lvo = gs * 8 + (h * 32 + r16) * 16;
  f32x4 mk;
#pragma unroll
  for (int r = 0; r < 4; ++r) mk[r] = ((unsigned)(4 * g4 + r - (r16 >> 1) - 1) < 8u) ? ((tb | w | h) ? 0.f : -4.0e9f) : -3.0e38f;
  unsigned gaw = L_GA + w * 8192 + l16, gar = L_GA + l16, vout = r32 * 1024 + h * 32, rdy = C_RDY, bit = 1u << (2 * w + (w & 2) * 2), bi2 = 2u << (2 * w + (w & 2) * 2), v4 = 4u;
  PIN(ah); PIN(lak); PIN(laq); PIN(lav); PIN(lag); PIN(lvo); PIN(mk); PIN(gaw); PIN(gar); PIN(vout); PIN(rdy); PIN(bit); PIN(bi2); PIN(v4); PIN(l4);
  f32x16 acc, acd;
  asm volatile(REP(8, "v_mov_b64 v[224+2*i_:225+2*i_], 0\n\t") REP(2, "v_mov_b64 v[240+2*i_:241+2*i_], -1\n\t") REP(16, "v_mov_b64 v[16+2*i_:17+2*i_], 0\n\t") : "={v[16:31]}"(acc), "={v[32:47]}"(acd) :: "memory");
  PIN(vz); PIN(v1); PIN(cst4); PIN(am); PIN(amc); PIN(va0); PIN(va1); PIN(va2); PIN(va3); PIN(aW0); PIN(aW1); PIN(ax0); PIN(ax1); PIN(ax2); PIN(ax3); PIN(ap); PIN(apg);
  const unsigned sm = WS_KT + 8192 + w * 16384;
  unsigned vT;
  unsigned sa = 0u, arw = rb + l16, apf = ((bi >> 2) * PFN + lane) * 3072u + (unsigned)(w - 1) * 1024u + hg * 128;
  PIN(arw); PIN(apf);
#define GREG() asm volatile(REP(4, \
      "buffer_load_dwordx4 v[192+16*i_:195+16*i_], %[a0], s[88:91], %[so] offen\n\tbuffer_load_dwordx4 v[196+16*i_:199+16*i_], %[a1], s[88:91], %[so] offen offset:1024\n\t" \
      "buffer_load_dwordx4 v[200+16*i_:203+16*i_], %[a2], s[88:91], %[so] offen offset:2048\n\tbuffer_load_dwordx4 v[204+16*i_:207+16*i_], %[a3], s[88:91], %[so] offen offset:3072\n\t" \
      "s_add_u32 %[so], %[so], 98304\n\t") \
      : [so] "+s"(sa) : [a0] "v"(va0), [a1] "v"(va1), [a2] "v"(va2), [a3] "v"(va3) : "memory", "scc")
  if (w != 0) {
    unsigned so;
    TS(1);
    asm volatile(REPX("j_", QD, QREQ(NT1)) : [c] "+s"(c), [so] "=&s"(so) : QOPS : "memory", "scc");
    TS(30);
    asm volatile("s_load_dword s100, %0, 0x800\n\ts_mov_b64 s[88:89], %1\n\ts_mov_b32 s90, 0x180000\n\ts_mov_b32 s91, 0x20000" :: "s"(ws), "s"(ARG(1)) : "memory", "s100");
  }
  if (w == 0) {
    GETT();
    const unsigned ak = L_EW + (g4 * 8 + (r16 >> 2)) * 32 + (lane & 3) * 8;
    const int slot = (bi >> 2) * 8 + xcd;
    const int u = isV ? gs : h, fl = (isV ? h : gs) * 32 + sp * 16 + r16;
    unsigned at = slot < 18 ? u * 1024 + fl * 16 + WS_KT + slot * 4096 + isV * 2048 : WS_END, au = fl * 16 + u * 8 + WS_KV + slot * 2048 + isV * 1024;
    float sc = isV ? 1.0f : 0.25f * LOG2E;
    vT = T;
    unsigned qp = __builtin_rotateright32(0x1111u, 4u * rot);
    PIN(at); PIN(au); PIN(sc); PIN(vT); PIN(qp);
    f32x4 pa = {0.f, 0.f, 0.f, 0.f}, pb = pa;
#define KVM(a, b) REP(4, ".if i_>0\n\ts_waitcnt lgkmcnt(21-6*i_)\n\t.endif\n\tv_mfma_f32_16x16x32_bf16 %[pa], v[" a "+8*i_:" a "+8*i_+3], v[" b "+8*i_:" b "+8*i_+3], %[pa]\n\t" \
                         ".if i_>0\n\ts_waitcnt lgkmcnt(18-6*i_)\n\t.endif\n\tv_mfma_f32_16x16x32_bf16 %[pb], v[" a "+8*i_+4:" a "+8*i_+7], v[" b "+8*i_+4:" b "+8*i_+7], %[pb]\n\t")
    asm volatile(
      REPV("j_", 2,
        "s_waitcnt vmcnt(17-16*j_)\n\ts_mov_b64 exec, 1\n\tds_add_u32 %[z], %[qp] offset:" STR(C_Q) "\n\tv_xor_b32 %[qp], 0x11111111, %[qp]\n\ts_mov_b64 exec, -1\n\t"
        REP(4, "ds_read_b64_tr_b16 v[192+8*i_:192+8*i_+1], %[ak] offset:j_*8192+i_*2048\n\tds_read_b64_tr_b16 v[192+8*i_+2:192+8*i_+3], %[ak] offset:j_*8192+i_*2048+128\n\t"
               "ds_read_b128 v[128+32*j_+8*i_:128+32*j_+8*i_+3], %[a0] offset:j_*32768+i_*8192\n\t"
               "ds_read_b64_tr_b16 v[192+8*i_+4:192+8*i_+5], %[ak] offset:j_*8192+i_*2048+1024\n\tds_read_b64_tr_b16 v[192+8*i_+6:192+8*i_+7], %[ak] offset:j_*8192+i_*2048+1152\n\t"
               "ds_read_b128 v[128+32*j_+8*i_+4:128+32*j_+8*i_+7], %[a1] offset:j_*32768+i_*8192\n\t")
        "s_waitcnt lgkmcnt(15)\n\ts_cmp_lg_u32 %[v], 0\n\ts_cbranch_scc1 1f\n\t"
        KVM("192", "128+32*j_") "s_branch 2f\n\t1:\n\t" KVM("128+32*j_", "192") "2:\n\t")
      "s_waitcnt vmcnt(0)\n\t"
      REP(8, "ds_read_b128 v[192+4*i_:192+4*i_+3], %[ag] offset:" STR(L_GW) "+i_*64\n\t") "s_nop 1\n\t"
      : [pa] "+v"(pa), [pb] "+v"(pb), [qp] "+v"(qp) : [z] "v"(vz), [ak] "v"(ak), [a0] "v"(aj), [a1] "v"(aj ^ 64), [v] "s"(isV), [ag] "v"(g4 * 16) : "memory", "scc");
    const f32x4 p = (pa + pb) * sc;
    u32x2 o; o[0] = pk_bf16(p[0], p[1]); o[1] = pk_bf16(p[2], p[3]);
    ST128(((u32x4){o[0], vT, o[1], vT}), at, 0);
    __builtin_amdgcn_raw_buffer_store_b64(o, rws, au, 0, SC1);
    TS(2);
    f32x4 gz = {0.f, 0.f, 0.f, 0.f};
    asm volatile("s_waitcnt lgkmcnt(0)\n\ts_nop 1\n\t"
      REP(8, "v_mfma_f32_16x16x32_bf16 %[gz], v[192+4*i_:192+4*i_+3], v[128+4*i_:128+4*i_+3], %[gz]\n\t"
             "ds_read_b128 v[192+4*i_:192+4*i_+3], %[ag] offset:" STR(L_GW) "+512+i_*64\n\t")
      REP(8, "s_waitcnt lgkmcnt(7-i_)\n\tv_mfma_f32_16x16x32_bf16 %[gz], v[192+4*i_:192+4*i_+3], v[160+4*i_:160+4*i_+3], %[gz]\n\t")
      "s_nop 7\n\t"
      : [gz] "+v"(gz) : [ag] "v"(g4 * 16) : "memory");
    const float g = __builtin_amdgcn_rcpf(1.f + __builtin_amdgcn_exp2f(-gz[0] * LOG2E));
    TS(3);
    unsigned so = WS_KT;
    asm volatile("s_waitcnt vmcnt(0)\n\ts_mov_b64 exec, 1\n\tbuffer_store_dword %[vT], off, %[rs], %[fo] sc1\n\t"
                 "s_mov_b64 exec, %[gm]\n\tbuffer_store_dword %[g], %[l4], %[rs], %[go] offen sc1\n\ts_mov_b64 exec, -1\n\t"
                 TSA(27)
                 GLDT("128", 24, "%[va]", "%[rs]", "%[so]") "s_waitcnt vmcnt(0)\n\t"
                 : [so] "+s"(so) TSOP
                 : [vT] "v"(vT), [rs] "s"(rw4), [fo] "s"(bid * 4), [g] "v"(g), [l4] "v"(l4), [go] "s"(WS_G + (t0 + jg * 16) * 4), [gm] "s"(0xffull << (8 * isV)), [va] "v"(l16) : "memory", "scc");
    TS(4);
    TAGGET(128, 12, 24, WS_KT);
    asm volatile("buffer_load_dwordx4 v[244:247], %0, %1, 0 offen sc1" :: "v"(l16), "s"(rw4) : "memory");
    KVPUT(128, 8, 12, am, amc);
    POST(C_KV, vz, v1);
  } else {
    unsigned inc = 1u, so;
    asm volatile(
      ".set j_, " STR(QD) "\n\t.rept 9\n\t"
        ".if j_<8\n\t" QREQ(NT2) ".set n_, 5*" STR(QD) "\n\t"
        ".else\n\t.set n_, 5*(7+" STR(QD) "-j_)\n\t.endif\n\t"
        ".if j_==" STR(PFJ) " && " STR(PFN) "\n\ts_mov_b64 exec, %[pm]\n\tbuffer_load_dword v254, %[pf], s[88:91], 0 offen\n\ts_mov_b64 exec, -1\n\t.endif\n\t"
        ".if j_>=" STR(PFJ) " && j_<" STR(PFJ) "+" STR(QD) "+1 && " STR(PFN) "\n\t.set n_, n_+1\n\t.endif\n\t"
        ".if j_==7\n\t" TSA(29) ".endif\n\t"
        ".if j_==" STR(MJ) "+" STR(QD) "\n\ts_mov_b32 %[so], %[sm]\n\t" TSA(27) GLDT("128", 16, "%[va]", "%[rs]", "%[so]") ".endif\n\t"
        ".if j_>=" STR(MJ) "+" STR(QD) "\n\t.set n_, n_+16\n\t.endif\n\t"
        ".if j_<8+" STR(QD) "\n\t"
        "s_waitcnt vmcnt(n_)\n\ts_mov_b64 exec, 1\n\tds_add_u32 %[z], %[inc] offset:" STR(C_Q) "\n\ts_mov_b64 exec, -1\n\tv_lshlrev_b32 %[inc], 4, %[inc]\n\t"
        ".endif\n\t"
        ".if j_==" STR(QD) "\n\t" TSA(2) ".endif\n\t"
        ".if j_==" STR(QD) "+3\n\t" TSA(28) ".endif\n\t"
      ".set j_, j_+1\n\t.endr\n\t"
      : [c] "+s"(c), [so] "=&s"(so), [inc] "+v"(inc) TSOP
      : QOPS, [z] "v"(vz), [sm] "s"(sm), [va] "v"(l16), [rs] "s"(rw4), [pf] "v"(apf), [pm] "s"(~0ull >> ((64 - PFN) & 63))
      : "memory", "scc");
    TS(3);
    GETT();
    VMW(GP == 2 ? 16 : 0);
    TS(4);
    TAGGET(128, 8, 16, sm);
  }
  TS(5);
#define GDMA(q) "s_add_u32 m0, %[rb], " q "*4096\n\ts_nop 0\n\t" \
      "buffer_load_dwordx4 %[a0], s[88:91], %[so] offen " NTG " lds\n\tbuffer_load_dwordx4 %[a1], s[88:91], %[so] offen offset:1024 " NTG " lds\n\t" \
      "buffer_load_dwordx4 %[a2], s[88:91], %[so] offen offset:2048 " NTG " lds\n\tbuffer_load_dwordx4 %[a3], s[88:91], %[so] offen offset:3072 " NTG " lds\n\t" \
      "s_add_u32 %[so], %[so], 98304\n\t"
  RAW_BARRIER();
  TS(7);
  const int ct = tb * 2 + (hg >> 2);
  const int nt = (hg & 3) * 4 + w;
  const unsigned sh = WS_HL + (tb * 32 + hg * 4) * 1024;
  if (w == 0) {
#define FLAGS(fo, n) do { unsigned spin = 200000u; \
      asm volatile("s_waitcnt vmcnt(%[n_])\n\t1:\n\t" \
                   "v_bitop3_b32 v244, v244, v245, %[T] bitop3:0x7e\n\tv_bitop3_b32 v246, v246, v247, %[T] bitop3:0x7e\n\t" \
                   "v_or_b32 v244, v244, v246\n\tv_cmp_ne_u32 vcc, 0, v244\n\ts_cbranch_vccz 2f\n\t" \
                   "buffer_load_dwordx4 v[244:247], %[va], %[rs], 0 offen offset:" #fo " sc1\n\ts_waitcnt vmcnt(0)\n\t" \
                   "s_sub_u32 %[sp], %[sp], 1\n\ts_cbranch_scc0 1b\n\t2:\n\t" \
                   : [sp] "+s"(spin) : [va] "v"(l16), [rs] "s"(rw4), [T] "s"(T), [n_] "n"(n) : "memory", "scc", "vcc"); } while (0)
#define FLAG2() asm volatile("s_mov_b64 exec, 1\n\tbuffer_store_dword %0, off, %1, %2 offset:1024 sc1\n\ts_mov_b64 exec, -1" :: "v"(vT), "s"(rw4), "s"(bid * 4) : "memory")
    PRIO(2);
    FLAGS(0, (CLR < 2));
    am += 18 * KVS; amc += 18 * KVS;
    PIN(am); PIN(amc);
    PRIO(P_W);
    TS(8);
    GLDN(128, 24, WS_KV + 18 * 2048);
    {
      unsigned so = WS_O + (tb * 64 + hg * 4) * 1024;
      asm volatile(REPV("j_", 5, REP(4, "buffer_store_dwordx4 v[240:243], %[va], %[rs], %[so] offen offset:i_*1024 sc1\n\t")
                        ".if j_==3\n\ts_mov_b32 %[so], %[sh]\n\t.elseif j_==1\n\ts_add_u32 %[so], %[so], 0x1f8000\n\t.else\n\ts_add_u32 %[so], %[so], 0x8000\n\t.endif\n\t")
                   "s_waitcnt vmcnt(0)"
                   : [so] "+s"(so) : [va] "v"(l16), [rs] "s"(rw4), [sh] "s"(sh) : "memory", "scc");
    }
    FLAG2();
    TS(6);
    wait_min<C_PG, 3, 2>(vz, 65536u);
    asm volatile("buffer_load_dwordx4 v[244:247], %0, %1, 0 offen offset:1024 sc1" :: "v"(l16), "s"(rw4) : "memory");
    VMW(1);
    PRIO(P_F);
    TS(9);
    KVPUT(128, 4, 24, am, amc);
    POST(C_KV, vz, v4);
    TS(13);
    FLAGS(1024, 0);
    if (bid == 0) asm volatile("s_mov_b64 exec, 1\n\tbuffer_store_dword %0, off, %1, 0 offset:2048 sc1\n\ts_mov_b64 exec, -1" :: "v"(vT), "s"(rw4) : "memory");
    asm volatile("buffer_load_dword v244, %0, %1, %2 offen sc1\n\ts_waitcnt lgkmcnt(0)" :: "v"(l4), "s"(rw4), "s"(WS_G + t0 * 4) : "memory");
#pragma nounroll
    for (unsigned b = 1; b < 3; ++b) {
      GLDN(128, 24, WS_KV + (18 + 12 * b) * 2048);
      am += 12 * KVS; amc += 12 * KVS;
      PIN(am); PIN(amc);
      asm volatile("s_waitcnt vmcnt(24)\n\tv_sub_f32 v245, 1.0, v244\n\tds_write2_b32 %0, v244, v245 offset0:32 offset1:96" :: "v"(l4) : "memory");
      PRIO(P_W);
      wait_min<C_PG, 3, 2>(vz, 81920u + b * 16384u);
      VMW(0);
      PRIO(P_F);
      KVPUTS(128, 4, 24, am, amc);
      POST(C_KV, vz, v4);
      asm volatile("s_waitcnt lgkmcnt(0)" ::: "memory");
    }
    GLDN(128, 20, WS_KV + 54 * 2048);
    am += 12 * KVS; amc += 12 * KVS;
    PIN(am); PIN(amc);
    TS(10);
    PRIO(P_W);
    wait_min<C_PG, 3, 1>(vz, 131072u);
    PRIO(P_F);
    if (tb) asm volatile(REP(4, "buffer_load_dwordx4 v[208+4*i_:208+4*i_+3], %0, %1, %2 offen offset:i_*1024 sc1\n\t") "s_waitcnt vmcnt(4)" :: "v"(l16), "s"(rw4), "s"(sh - 32 * 1024) : "memory");
    else VMW(0);
    KVPUTS(128, 4, 20, am, amc);
    POST(C_KV, vz, v4);
    TS(12);
    PRIO(P_LA0);
    if (tb) {
      unsigned spin = 200000u;
      asm volatile("1:\n\ts_waitcnt vmcnt(0)\n\t" FULLT("208", "v244") "s_cbranch_vccz 2f\n\t"
                   REP(4, "buffer_load_dwordx4 v[208+4*i_:208+4*i_+3], %[va], %[rs], %[so] offen offset:i_*1024 sc1\n\t")
                   "s_sub_u32 %[sp], %[sp], 1\n\ts_cbranch_scc0 1b\n\t2:\n\t"
                   REP(4, "ds_write_b128 %[ah], v[208+4*i_:208+4*i_+3] offset:i_*7168\n\t")
                   : [sp] "+s"(spin) : [va] "v"(l16), [rs] "s"(rw4), [so] "s"(sh - 32 * 1024), [ah] "v"(ah) : "memory", "scc", "vcc");
    } else {
      asm volatile(REP(4, "ds_write_b128 %0, v[224:227] offset:i_*7168\n\t") :: "v"(ah) : "memory");
    }
    wait_ge<C_PL, 1>(vz, 3);
  } else {
#define GRD(f, wo, xr, xo) \
      "ds_read_b64_tr_b16 v[" f ":" f "+1], %[w0] offset:" wo "\n\tds_read_b64_tr_b16 v[" f "+2:" f "+3], %[w0] offset:" wo "+512\n\t" \
      "ds_read_b128 v[" f "+8:" f "+11], " xr " offset:" xo "\n\t" \
      "ds_read_b64_tr_b16 v[" f "+4:" f "+5], %[w1] offset:" wo "\n\tds_read_b64_tr_b16 v[" f "+6:" f "+7], %[w1] offset:" wo "+512\n\t" \
      "ds_read_b128 v[" f "+12:" f "+15], " xr " offset:" xo "+4096\n\t"
#define GM1(a, f, i, j, c) "v_mfma_f32_32x32x16_bf16 v[" a "], v[" f "+" i ":" f "+" i "+3], v[" f "+" j ":" f "+" j "+3], " c "\n\t"
#define GMM(f) GM1("128:143", f, "0", "8", "v[128:143]") GM1("160:175", f, "4", "8", "v[160:175]") GM1("144:159", f, "0", "12", "v[144:159]") GM1("176:191", f, "4", "12", "v[176:191]")
#define GST(q, xb, xa, xo, lab) GRD("208", q "*4096+2048", xb, "(" q ">>1)*8192") "s_waitcnt lgkmcnt(6)\n\t" GMM("192") lab \
      "s_waitcnt vmcnt(8)\n\t" GRD("192", "((" q "+1)&3)*4096", xa, xo) "s_waitcnt lgkmcnt(6)\n\t" GDMA(q) GMM("208")
    unsigned cnt = 4, tmp;
    KVPUT(128, 8, 8, am, amc);
    POST(C_KV, vz, v1);
    TS(6);
    asm volatile(REP(4, GDMA("i_"))
                 : [so] "+s"(sa) : [rb] "s"(rb), [a0] "v"(va0), [a1] "v"(va1), [a2] "v"(va2), [a3] "v"(va3) : "memory", "scc");
    TS(8);
    asm volatile(
      "s_waitcnt vmcnt(12)\n\t"
      TSA(9)
      GRD("192", "0", "%[x0]", "0") GRD("208", "2048", "%[x1]", "0")
      "s_waitcnt lgkmcnt(6)\n\t"
      GM1("128:143", "192", "0", "8", "0") GM1("160:175", "192", "4", "8", "0") GM1("144:159", "192", "0", "12", "0") GM1("176:191", "192", "4", "12", "0")
      "s_branch 2f\n\t"
      "1:\n\t"
      GST("0", "%[x1]", "%[x2]", "0", "2:\n\t") GST("1", "%[x3]", "%[x0]", "8192", "") GST("2", "%[x1]", "%[x2]", "8192", "") GST("3", "%[x3]", "%[x0]", "16384", "")
      "s_setprio 2\n\t"
      "v_add_u32 %[x0], 0x4000, %[x0]\n\tv_add_u32 %[x1], 0x4000, %[x1]\n\tv_add_u32 %[x2], 0x4000, %[x2]\n\tv_add_u32 %[x3], 0x4000, %[x3]\n\t"
      "s_setprio 0\n\t"
      "s_sub_u32 %[cnt], %[cnt], 1\n\t"
      "ds_write_b32 %[pg], %[x0]\n\t"
      "s_cmp_gt_u32 %[cnt], 1\n\t"
      "s_cselect_b32 s90, s90, 0\n\t"
      "s_cmp_lg_u32 %[cnt], 0\n\t"
      "s_cbranch_scc1 1b\n\t"
      "ds_read_b32 v192, %[z] offset:" STR(C_KV) "\n\t"
      "s_waitcnt vmcnt(0) lgkmcnt(0)\n\t"
      "ds_write_b32 %[pg], %[ap]\n\t"
      TSA(10)
      "s_nop 7\n\t"
      "s_setprio " STR(P_PK) "\n\t"
      REP(32, "v_cvt_pk_bf16_f32 v[128+(i_>>2)*8+(i_&3)], v[128+2*i_], v[128+2*i_+1]\n\t")
      "4:\n\tv_readfirstlane_b32 %[tmp], v192\n\ts_cmp_ge_u32 %[tmp], 8\n\ts_cbranch_scc1 5f\n\t"
      "s_sleep 2\n\tds_read_b32 v192, %[z] offset:" STR(C_KV) "\n\ts_waitcnt lgkmcnt(0)\n\ts_add_u32 %[cnt], %[cnt], 1024\n\ts_cbranch_scc0 4b\n\t5:\n\t"
      "s_setprio " STR(P_PL) "\n\t"
      "s_cmp_eq_u32 %[rb], " STR(L_RING) "\n\ts_cbranch_scc1 3f\n\t"
      "s_mov_b64 exec, %[up]\n\t"
      REP(4, "buffer_store_dwordx4 v[144+(i_>>1)*32+(i_&1)*8:144+(i_>>1)*32+(i_&1)*8+3], %[ap], %[rs], %[sh] offen offset:i_*1024 sc1\n\t")
      "s_mov_b64 exec, -1\n\t3:\n\t"
      : [so] "+s"(sa), [cnt] "+s"(cnt), [tmp] "=&s"(tmp), [x0] "+v"(ax0), [x1] "+v"(ax1), [x2] "+v"(ax2), [x3] "+v"(ax3) TSOP
      : [rb] "s"(rb), [a0] "v"(va0), [a1] "v"(va1), [a2] "v"(va2), [a3] "v"(va3), [w0] "v"(aW0), [w1] "v"(aW1), [pg] "v"(apg), [ap] "v"(ap),
        [z] "v"(vz), [up] "s"(0xffff0000ffff0000ull), [rs] "s"(rw4), [sh] "s"(sh - L_PL - w * 2048 + 3072)
      : "memory", "scc");
    wait_min<C_PG, 3, 1>(vz, 131072u);
    asm volatile(REP(8, "ds_write_b128 %0, v[128+8*i_:128+8*i_+3] offset:((i_>>2)*2+(i_&1))*7168+((i_>>1)&1)*1024\n\t") :: "v"(ap) : "memory");
    POST(C_PL, vz, v1);
    if (tmp < 12) wait_ge<C_KV, 1>(vz, 12);
    wait_ge<C_PL, 1>(vz, 3);
    PRIO(P_LA);
  }
  TS(11);
  GLDN(128, 24, WS_WF + nt * 32768);
  {
    u32x16 c0, c1, c2, c3; u32x4 c4;
    unsigned so = __builtin_amdgcn_readfirstlane(WS_O + ((tb * 2 + (w >> 1)) * 32 + hg * 4) * 1024 + (w & 1) * 256), sw = WS_WF + nt * 32768 + 24576, gt;
#define MM16(d, a, b, c) "v_mfma_f32_16x16x32_bf16 v[" d "+4*i_:" d "+4*i_+3], " a ", v[" b "+4*i_:" b "+4*i_+3], " c "\n\t"
#define BC(i) "v[56+(" i "&2):57+(" i "&2)]"
    asm volatile(
      "s_mov_b32 s92, 0x3eb8aa3b\n\t"
      "s_mov_b64 exec, %[ev]\n\t" REP(4, "ds_read_b64 v[224+4*i_:224+4*i_+1], %[aq] offset:i_*7168\n\t")
      "s_not_b64 exec, exec\n\t" REP(4, "ds_read_b64 v[224+4*i_+2:224+4*i_+3], %[aq] offset:i_*7168\n\t")
      "s_mov_b64 exec, -1\n\t"
      REP(8, "ds_read_b64 v[64+2*i_:64+2*i_+1], %[ak] offset:(i_>>1)*7168+(i_&1)*32\n\t")
      REP(4, "s_waitcnt lgkmcnt(6-2*i_)\n\t" MM16("96", "v[64+4*i_:64+4*i_+3]", "224", "%[mk]"))
      REP(8, "ds_read_b64_tr_b16 v[80+2*i_:80+2*i_+1], %[av] offset:(i_>>1)*7168+(i_&1)*32\n\t")
      "ds_read_b32 %[gt], %[ag]\n\t"
      REP(4, "v_max3_f32 v[56+i_], v[96+4*i_], v[97+4*i_], v[98+4*i_]\n\t")
      REP(4, "v_max_f32 v[56+i_], v[56+i_], v[99+4*i_]\n\t")
      "v_permlane32_swap_b32 v56, v57\n\ts_nop 0\n\tv_permlane32_swap_b32 v58, v59\n\t"
      "v_max_f32 v56, v56, v57\n\tv_max_f32 v58, v58, v59\n\ts_nop 1\n\t"
      "v_permlane16_swap_b32 v56, v58\n\tv_max_f32 v56, v56, v58\n\t"
      "v_mul_f32 v57, 0xbeb8aa3b, v56\n\tv_mul_f32 v59, 0xbeb8aa3b, v56\n\ts_nop 1\n\t"
      "v_permlane16_swap_b32 v57, v59\n\tv_mov_b32 v56, v57\n\tv_mov_b32 v58, v59\n\ts_nop 0\n\t"
      "v_permlane32_swap_b32 v56, v57\n\tv_permlane32_swap_b32 v58, v59\n\t"
      REP(8, ".if (i_&2)==0\n\tv_pk_fma_f32 v[96+2*i_:97+2*i_], v[96+2*i_:97+2*i_], s[92:93], " BC("(i_>>1)") " op_sel:[0,0,0] op_sel_hi:[1,0,0]\n\t"
             ".else\n\tv_pk_fma_f32 v[96+2*i_:97+2*i_], v[96+2*i_:97+2*i_], s[92:93], " BC("(i_>>1)") " op_sel:[0,0,1] op_sel_hi:[1,0,1]\n\t.endif\n\t")
      REP(16, "v_exp_f32 v[96+i_], v[96+i_]\n\t")
      "s_mov_b64 exec, %[ev]\n\t" REP(8, "v_cvt_pk_bf16_f32 v[224+(i_>>1)*4+(i_&1)], v[96+2*i_], v[97+2*i_]\n\t")
      "s_not_b64 exec, exec\n\t" REP(8, "v_cvt_pk_bf16_f32 v[226+(i_>>1)*4+(i_&1)], v[96+2*i_], v[97+2*i_]\n\t")
      "s_mov_b64 exec, -1\n\t"
      REP(4, MM16("112", "%[one]", "224", "0"))
      "s_waitcnt lgkmcnt(0)\n\t"
      REP(4, MM16("64", "v[80+4*i_:80+4*i_+3]", "224", "0"))
      REP(4, "v_rcp_f32 v[56+i_], v[112+4*i_]\n\t")
      REP(4, "v_mul_f32 v[56+i_], v[56+i_], %[gt]\n\t")
      REP(8, ".if (i_&2)==0\n\tv_pk_mul_f32 v[64+2*i_:65+2*i_], v[64+2*i_:65+2*i_], " BC("(i_>>1)") " op_sel:[0,0] op_sel_hi:[1,0]\n\t"
             ".else\n\tv_pk_mul_f32 v[64+2*i_:65+2*i_], v[64+2*i_:65+2*i_], " BC("(i_>>1)") " op_sel:[0,1] op_sel_hi:[1,1]\n\t.endif\n\t")
      REP(8, "v_cvt_pk_bf16_f32 v[80+i_], v[64+2*i_], v[65+2*i_]\n\t")
      REP(4, "buffer_store_dwordx2 v[80+2*i_:80+2*i_+1], %[vo], %[rs], %[so] offen offset:i_*1024 sc1\n\t")
      GLDT("224", 8, "%[va]", "%[rs]", "%[sw]")
      : "=&{v[64:79]}"(c0), "=&{v[80:95]}"(c1), "=&{v[96:111]}"(c2), "=&{v[112:127]}"(c3), "=&{v[56:59]}"(c4), [gt] "=&v"(gt), [sw] "+s"(sw)
      : [so] "s"(so), [ev] "s"(0x5555555555555555ull), [aq] "v"(laq), [ak] "v"(lak), [av] "v"(lav), [ag] "v"(lag), [mk] "v"(mk), [one] "v"(cst4), [vo] "v"(lvo), [rs] "s"(rw4), [va] "v"(l16)
      : "memory", "scc");
  }
  PRIO(P_TA);
  TS(14);
  if (w == 0) POST(C_PL, vz, v1);
  wait_ge<C_PL, 2>(vz, 4);
  wait_min<C_LP, 4, 2>(vz, L_KV + 24 * KVS);
  TS(15);
  {
#define TCHK(b, r, o, bit) \
      "s_bitcmp1_b32 %[pend], " b "\n\ts_cbranch_scc0 1f\n\t" \
      FULLT(r, "%[tv]") "s_cbranch_vccnz 1f\n\t" \
      REP(4, "ds_write_b128 %[aw], v[" r "+4*i_:" r "+4*i_+3] offset:" o "+i_*1024\n\t") \
      "s_mov_b64 exec, 1\n\tds_or_b32 %[ar], " bit "\n\ts_mov_b64 exec, -1\n\t" \
      "s_bitset0_b32 %[pend], " b "\n\t1:\n\t"
    u32x16 c0, c1, c4; u32x8 c5;
    unsigned so, sb = WS_O + (ct * 32 + w * 8) * 1024, spin = 400000u, pend, done, todo, tv;
    asm volatile(
      "s_mov_b64 s[88:89], %[out]\n\ts_mov_b32 s90, 0\n\ts_mov_b32 s91, 0x20000\n\t"
      "10:\n\t"
      "s_mov_b32 %[pend], 7\n\ts_mov_b32 %[done], 0\n\t"
      "11:\n\t"
      "s_bitcmp1_b32 %[pend], 0\n\ts_cbranch_scc0 1f\n\t"
      "s_mov_b32 %[so], %[sb]\n\t" GLDT("64", 4, "%[va]", "%[rs]", "%[so]")
      "1:\n\t"
      "s_bitcmp1_b32 %[pend], 1\n\ts_cbranch_scc0 1f\n\t"
      "s_add_u32 %[so], %[sb], 0x1000\n\t" GLDT("80", 4, "%[va]", "%[rs]", "%[so]")
      "1:\n\t"
      "s_bitcmp1_b32 %[pend], 2\n\ts_cbranch_scc1 12f\n\t"
      "ds_read_b32 %[tv], %[ar]\n\ts_waitcnt lgkmcnt(0)\n\tv_readfirstlane_b32 %[so], %[tv]\n\t"
      "s_add_u32 %[todo], %[so], 0x101\n\ts_andn2_b32 %[so], %[so], %[todo]\n\t"
      "s_andn2_b32 %[todo], %[so], %[done]\n\ts_cbranch_scc0 12f\n\t"
      "s_mov_b32 %[done], %[so]\n\ts_mov_b32 vcc_lo, s90\n\ts_mov_b32 vcc_hi, 0\n\t"
      REPV("g_", 8,
        "s_bitcmp1_b32 %[todo], g_+(g_&4)\n\ts_cbranch_scc0 3f\n\t"
        REP(4, "ds_read_b128 v[96+4*i_:96+4*i_+3], %[rd] offset:g_*4096+i_*1024\n\t")
        "2:\n\t"
        REP(4, ".if g_<7\n\ts_waitcnt lgkmcnt(3)\n\t.else\n\ts_waitcnt lgkmcnt(3-i_)\n\t.endif\n\t"
               "v_mfma_f32_32x32x16_bf16 v[16+(g_&4)*4:31+(g_&4)*4], v[128+16*g_+4*i_:128+16*g_+4*i_+3], v[96+4*i_:96+4*i_+3], v[16+(g_&4)*4:31+(g_&4)*4]\n\t"
               ".if g_<7\n\tds_read_b128 v[96+4*i_:96+4*i_+3], %[rd] offset:g_*4096+4096+i_*1024\n\t.endif\n\t")
        "s_cbranch_vccnz 4f\n\ts_add_u32 %[so], %[wb], g_*4096\n\t"
        REP(4, "buffer_load_dwordx4 v[128+16*g_+4*i_:128+16*g_+4*i_+3], %[va], %[rs], %[so] offen offset:i_*1024 sc1\n\t")
        "4:\n\t"
        ".if g_<7\n\ts_bitcmp1_b32 %[todo], g_+1+((g_+1)&4)\n\ts_cbranch_scc1 2f\n\t.endif\n\t"
        "3:\n\t")
      "s_cmp_eq_u32 %[done], 0xf0f\n\ts_cbranch_scc1 13f\n\t"
      "12:\n\t"
      "s_and_b32 %[pend], %[pend], 3\n\ts_cbranch_scc0 14f\n\t"
      "s_cmp_eq_u32 %[pend], 3\n\ts_cbranch_scc1 1f\n\ts_waitcnt vmcnt(0)\n\t1:\n\ts_waitcnt vmcnt(4)\n\ts_setprio " STR(P_CK) "\n\t"
      TCHK("0", "64", "0", "%[bit]")
      "s_waitcnt vmcnt(0)\n\t"
      TCHK("1", "80", "4096", "%[bi2]")
      "s_setprio " STR(P_TA) "\n\t"
      "14:\n\t"
      "s_cmp_lg_u32 s90, 0\n\ts_cbranch_scc1 1f\n\ts_sleep " STR(ASLP) "\n\t1:\n\t"
      "s_sub_u32 %[spin], %[spin], 1\n\ts_cbranch_scc0 11b\n\t"
      "13:\n\t"
      "s_nop 11\n\t"
      REP(8, "v_pk_add_f32 v[96+2*i_:97+2*i_], v[16+2*i_:17+2*i_], v[32+2*i_:33+2*i_]\n\t")
      REP(8, "v_cvt_pk_bf16_f32 v[112+i_], v[96+2*i_], v[97+2*i_]\n\t")
      "buffer_store_dwordx4 v[112:115], %[vo], s[88:91], %[oo] offen\n\t"
      "buffer_store_dwordx4 v[116:119], %[vo], s[88:91], %[oo] offen offset:16\n\t"
      "s_cmp_lg_u32 s90, 0\n\ts_cbranch_scc1 15f\n\t"
      TSA(16)
      "v_add_u32 %[aw], 0x8000, %[aw]\n\tv_add_u32 %[rd], 0x8000, %[rd]\n\tv_add_u32 %[ar], 4, %[ar]\n\t"
      "s_add_u32 %[sb], %[sb], 0x200000\n\ts_mov_b32 s90, 0x200000\n\ts_mov_b32 %[spin], 4000000\n\t"
      "1:\n\tds_read_b128 v[96:99], %[ar] offset:" STR(C_LP) "-" STR(C_RDY) "-4\n\ts_waitcnt lgkmcnt(0)\n\t"
      "v_min3_u32 v96, v96, v97, v98\n\tv_min_u32 v96, v96, v99\n\ts_nop 0\n\tv_readfirstlane_b32 %[so], v96\n\t"
      "s_cmp_ge_u32 %[so], " STR(BTRIG) "\n\ts_cbranch_scc1 2f\n\ts_sleep 1\n\ts_sub_u32 %[spin], %[spin], 1\n\ts_cbranch_scc0 1b\n\t2:\n\t"
      "s_sleep " STR(BSLP) "\n\ts_mov_b32 %[spin], 400000\n\t"
      TSA(17)
      "s_branch 10b\n\t"
      "15:\n\t"
      : "+{v[16:31]}"(acc), "+{v[32:47]}"(acd), "=&{v[96:111]}"(c4), "=&{v[112:119]}"(c5), "=&{v[64:79]}"(c0), "=&{v[80:95]}"(c1),
        [so] "=&s"(so), [sb] "+s"(sb), [spin] "+s"(spin), [pend] "=&s"(pend), [done] "=&s"(done), [todo] "=&s"(todo), [tv] "=&v"(tv),
        [aw] "+v"(gaw), [rd] "+v"(gar), [ar] "+v"(rdy) TSOP
      : [va] "v"(l16), [rs] "s"(rw4), [out] "s"((unsigned long long)kb[2] | (unsigned long long)kb[3] << 32), [vo] "v"(vout), [oo] "s"(ct * 32768 + nt * 64), [wb] "s"(WS_WF + (16 + nt) * 32768), [bit] "v"(bit), [bi2] "v"(bi2)
      : "memory", "scc", "vcc");
  }
  TS(26);
  TSDUMP();
}

extern "C" void kernel_launch(void* const* d_in, const int* in_sizes, int n_in,
                              void* d_out, int out_size, void* d_ws, size_t ws_size,
                              hipStream_t stream) {
  (void)hipFuncSetAttribute((const void*)fused, hipFuncAttributeMaxDynamicSharedMemorySize, L_TOT);
  fused<<<256, 512, L_TOT, stream>>>(d_in[0], d_in[1], d_in[2], d_in[4], d_in[5], d_in[6], d_in[7], d_in[8], d_ws, d_out);
}
```

```cpp
#include <hip/hip_runtime.h>
#include <hip/hip_bf16.h>

typedef __bf16 bf16x2 __attribute__((ext_vector_type(2)));
typedef __bf16 bf16x4 __attribute__((ext_vector_type(4)));
typedef __bf16 bf16x8 __attribute__((ext_vector_type(8)));
typedef __bf16 bf16x16 __attribute__((ext_vector_type(16)));
typedef short s16x4 __attribute__((ext_vector_type(4)));
typedef float f32x4 __attribute__((ext_vector_type(4)));
typedef float f32x8 __attribute__((ext_vector_type(8)));
typedef float f32x16 __attribute__((ext_vector_type(16)));
typedef unsigned u32x2 __attribute__((ext_vector_type(2)));
typedef unsigned u32x4 __attribute__((ext_vector_type(4)));
typedef unsigned u32x8 __attribute__((ext_vector_type(8)));
typedef unsigned u32x16 __attribute__((ext_vector_type(16)));
typedef unsigned char u8;
typedef __attribute__((address_space(3))) u8 lds_u8;

#define STR2(x) #x
#define STR(x) STR2(x)
#define LOG2E 1.4426950408889634f
#define SC1 16
#define QD 2
#define NTM 16
#define NT0 ""
#define NT1 ""
#define NT2 ""
#define NTG ""
#define NTF "nt"
#define CPF 1
#define CLR 2
#define MJ 8
#define MPRE 1
#define GP 1
#define PFN 16
#define PFJ 7
#define HP 0
#define HX 0
#define LB 2
#define SWZ 2
#define KSL 3
#define P_PK 0
#define P_LA0 2
#define P_LA 2
#define P_L0 0
#define P_W 0
#define P_F 0
#define P_PL 0
#define P_CK 0
#define BTRIG 0xd0000000
#define P_TA 0
#define P_TB 0
#define ASLP 4
#define BSLP 0

#define WS_FLAG1 0u
#define WS_EPOCH 2048u
#define WS_G     4096u
#define WS_KV    (64u << 10)
#define WS_KT    (256u << 10)
#define WS_WF    (1u << 20)
#define WS_HL    (2u << 20)
#define WS_O     (4u << 20)
#define WS_END   (8u << 20)
#define TS(i) do {} while (0)
#define TSA(i) ""
#define TSG(o, r, sh) ""
#define TSDUMP() do {} while (0)
#define TS0() do {} while (0)
#define TSI()
#define TSOP
#define TSQ(r) do {} while (0)
#define TSQC() do {} while (0)

#define C_KV   0
#define C_PL   4
#define C_RDY  8
#define C_PG   16
#define C_LP   32
#define C_Q    48
#define L_G    128
#define L_GW   1024
#define L_KV   2048
#define KVS    2080
#define L_EW   2048
#define L_TW   18432
#define L_X(j) (49152u + ((j) << 13))
#define L_WQ(j) (32768u + ((81920u + ((j) << 13)) & 131071u))
#define L_RING 114688
#define L_PL   135168
#define L_GA   2048
#define L_TOT  163840

__device__ __forceinline__ unsigned pk_bf16(float lo, float hi) {
  bf16x2 v; v[0] = (__bf16)lo; v[1] = (__bf16)hi;
  return __builtin_bit_cast(unsigned, v);
}
__device__ __forceinline__ u32x4 mkrsrc(unsigned long long a, unsigned bytes) {
  u32x4 r;
  r[0] = (unsigned)a; r[1] = (unsigned)(a >> 32) & 0xffffu; r[2] = bytes; r[3] = 0x00020000u;
  return r;
}
__device__ __forceinline__ bf16x4 tr_read(unsigned a) {
  return __builtin_bit_cast(bf16x4, __builtin_amdgcn_ds_read_tr16_b64_v4i16((__attribute__((address_space(3))) s16x4*)(lds_u8*)(unsigned long long)a));
}
__device__ __forceinline__ bf16x8 cat8(bf16x4 a, bf16x4 b) { return __builtin_shufflevector(a, b, 0, 1, 2, 3, 4, 5, 6, 7); }
template <typename T> __device__ __forceinline__ T lds_ld(unsigned a) { return *(__attribute__((address_space(3))) T*)(lds_u8*)(unsigned long long)a; }
template <typename T> __device__ __forceinline__ void lds_st(unsigned a, T v) { *(__attribute__((address_space(3))) T*)(lds_u8*)(unsigned long long)a = v; }
#define RAW_BARRIER() do { __builtin_amdgcn_s_waitcnt(0xC07F); asm volatile("s_waitcnt lgkmcnt(0)\n\ts_barrier" ::: "memory"); } while (0)
#define VMW(n) asm volatile("s_waitcnt vmcnt(%0)" :: "n"(n) : "memory")
#define ST128(d, vo, so) __builtin_amdgcn_raw_buffer_store_b128((d), rws, (vo) + (so), 0, SC1)
#define SCHED_FENCE() __builtin_amdgcn_sched_barrier(0)
#define PRIO(n) asm volatile("s_setprio " STR(n) ::: "memory")
__device__ __forceinline__ float swap_lo(unsigned a) { return __builtin_bit_cast(float, a); }
#define POST(c, z, v) asm volatile("s_mov_b64 exec, 1\n\tds_add_u32 %0, %1 offset:" STR(c) "\n\ts_mov_b64 exec, -1" :: "v"(z), "v"(v) : "memory")
template <int C, int SLEEP> __device__ __forceinline__ void wait_ge(unsigned z, unsigned target) {
  unsigned t, v, spin = 4000000u;
  asm volatile("1:\n\tds_read_b32 %1, %3 offset:%6\n\ts_waitcnt lgkmcnt(0)\n\tv_readfirstlane_b32 %0, %1\n\ts_cmp_ge_u32 %0, %4\n\ts_cbranch_scc1 2f\n\t"
               "s_sleep %5\n\ts_sub_u32 %2, %2, 1\n\ts_cbranch_scc0 1b\n\t2:"
               : "=&s"(t), "=&v"(v), "+s"(spin) : "v"(z), "s"(target), "n"(SLEEP), "n"(C) : "memory", "scc");
}
template <int C, int N, int SLEEP> __device__ __forceinline__ void wait_min(unsigned z, unsigned target) {
  unsigned t, spin = 4000000u; u32x4 v;
  asm volatile("1:\n\tds_read_b128 v[124:127], %3 offset:%6\n\ts_waitcnt lgkmcnt(0)\n\tv_min3_u32 v124, v124, v125, v126\n\t.if %7==4\n\tv_min_u32 v124, v124, v127\n\t.endif\n\t"
               "s_nop 0\n\tv_readfirstlane_b32 %0, v124\n\t"
               "s_cmp_ge_u32 %0, %4\n\ts_cbranch_scc1 2f\n\ts_sleep %5\n\ts_sub_u32 %2, %2, 1\n\ts_cbranch_scc0 1b\n\t2:"
               : "=&s"(t), "=&{v[124:127]}"(v), "+s"(spin) : "v"(z), "s"(target), "n"(SLEEP), "n"(C), "n"(N) : "memory", "scc");
}
#define DMA2(m, v0, v1, rs, so) asm volatile("s_mov_b32 m0, %0\n\ts_nop 0\n\t" \
    "buffer_load_dwordx4 %1, %3, %4 offen " NT0 " lds\n\tbuffer_load_dwordx4 %2, %3, %4 offen offset:1024 " NT0 " lds" \
    :: "s"(m), "v"(v0), "v"(v1), "s"(rs), "s"(so) : "memory")
#define REPV(v, n, body) ".set " v ", 0\n\t.rept " #n "\n\t" body ".set " v ", " v "+1\n\t.endr\n\t"
#define REPX(v, n, body) REPV(v, n, body)
#define REP(n, body) REPV("i_", n, body)
#define GLDT(r, n, va, rs, so) REP(n, \
    "buffer_load_dwordx4 v[" r "+4*i_:" r "+4*i_+3], " va ", " rs ", " so " offen offset:(i_&3)*1024 sc1\n\t" \
    ".if (i_&3)==3\n\ts_add_u32 " so ", " so ", 0x1000\n\t.endif\n\t")
#define GLDN(r, n, so) do { unsigned so_ = (so); asm volatile(GLDT(#r, n, "%1", "%2", "%0") : "+s"(so_) : "v"(l16), "s"(rw4) : "memory", "scc"); } while (0)
#define TAGT(r, n, t) REP(n, \
    "v_bitop3_b32 v[" r "+8*i_+1], v[" r "+8*i_+1], v[" r "+8*i_+3], " t " bitop3:0x7e\n\t" \
    "v_bitop3_b32 v[" r "+8*i_+5], v[" r "+8*i_+5], v[" r "+8*i_+7], " t " bitop3:0x7e\n\t" \
    ".if i_==0\n\tv_or_b32 v[" r "+7], v[" r "+1], v[" r "+5]\n\t.else\n\t" \
    "v_or3_b32 v[" r "+7], v[" r "+7], v[" r "+8*i_+1], v[" r "+8*i_+5]\n\t.endif\n\t") \
    "v_cmp_ne_u32 vcc, 0, v[" r "+7]\n\t"
#define TAGP(r, n) REP(n, \
    "v_mov_b32 v[" r "+8*i_+1], v[" r "+8*i_+2]\n\t" \
    "v_pk_mov_b32 v[" r "+8*i_+2:" r "+8*i_+3], v[" r "+8*i_+4:" r "+8*i_+5], v[" r "+8*i_+6:" r "+8*i_+7] op_sel:[0,0]\n\t")
#define TAGGET(r, n, n2, so) do { unsigned so_, spin_ = 200000u; asm volatile("1:\n\t" TAGT(#r, n, "%[T]") TAGP(#r, n) "s_cbranch_vccz 2f\n\t" \
    "s_mov_b32 %[o], %[b]\n\t" GLDT(#r, n2, "%[va]", "%[rs]", "%[o]") "s_waitcnt vmcnt(0)\n\ts_sub_u32 %[sp], %[sp], 1\n\ts_cbranch_scc0 1b\n\t2:" \
    : [o] "=&s"(so_), [sp] "+s"(spin_) : [b] "s"(so), [va] "v"(l16), [rs] "s"(rw4), [T] "s"(T) : "memory", "scc", "vcc"); } while (0)
#define FULLT(r, t) "v_max3_u32 " t ", v[" r "], v[" r "+2], v[" r "+4]\n\tv_max3_u32 " t ", " t ", v[" r "+6], v[" r "+8]\n\t" \
    "v_max3_u32 " t ", " t ", v[" r "+10], v[" r "+12]\n\tv_max_u32 " t ", " t ", v[" r "+14]\n\tv_cmp_eq_u32 vcc, -1, " t "\n\t"
#define KVPUT(r, st, n, a, c) asm volatile(REP(n, "ds_write_b128 %0, v[" #r "+" #st "*i_:" #r "+" #st "*i_+3] offset:(i_>>1)*2080+(i_&1)*1024\n\t") \
    "s_mov_b64 exec, (1<<" #n ")-1\n\tds_write_b128 %1, %2\n\ts_mov_b64 exec, -1" :: "v"(a), "v"(c), "v"(cst4) : "memory")
#define KVPUTS(r, st, n, a, c) asm volatile(REP(n, "ds_write_b128 %0, v[" #r "+" #st "*i_:" #r "+" #st "*i_+3] offset:(i_>>1)*2080+(i_&1)*1024\n\t" \
    ".if (i_&1) && " STR(KSL) "\n\ts_sleep " STR(KSL) "\n\t.endif\n\t") \
    "s_mov_b64 exec, (1<<" #n ")-1\n\tds_write_b128 %1, %2\n\ts_mov_b64 exec, -1" :: "v"(a), "v"(c), "v"(cst4) : "memory")
#define GETT() asm volatile("s_waitcnt lgkmcnt(0)\n\ts_mul_i32 %0, s100, 1664525\n\ts_add_u32 %0, %0, 1013904223\n\ts_or_b32 %0, %0, 1" : "=s"(T) :: "memory", "scc")

#define E1(r) "v_exp_f32 v[128+" #r "], v[128+" #r "]\n\t"
#define E4(a, b, c, d) E1(a) E1(b) E1(c) E1(d)
#define PVM(d, a, b) "v_smfmac_f32_32x32x32_bf16 v[" d "], v[" a "], v[" b "], v232\n\t"
#define QKM(d, k, q) "v_mfma_f32_32x32x16_bf16 v[" d "], v[" k "], " q ", 0\n\t"
#define RDL(d, a, t, c) "ds_read_b128 v[" d "], " a " offset:(" t ")*2080+" c "\n\t"
#define DUAL(t, W, X, Ka, V0a, V1a, Kb, V1b) \
  E1(0) E1(1) W PVM("176:191", V1b, "152:159") RDL(V1b, "v235", #t "+1", "1280") E4(2, 3, 4, 5) E1(6) \
  QKM("144:159", Ka, "%[q1]") RDL(Ka, "v233", #t "+2", "0") E4(7, 8, 9, 10) E1(11) E1(12) \
  PVM("160:175", V0a, "128:135") E4(13, 14, 15, 16) E1(17) \
  PVM("160:175", V1a, "136:143") X E4(18, 19, 20, 21) E1(22) \
  QKM("128:143", Kb, "%[q0]") E4(23, 24, 25, 26) E1(27) E1(28) \
  PVM("176:191", V0a, "144:151") RDL(V0a, "v234", #t "+2", "1024") E1(29) E1(30) E1(31)
#define LW1 "s_waitcnt lgkmcnt(1)\n\t"
#define DUALX(t, W, X) DUAL(t, W, X, "192:195", "200:203", "204:207", "196:199", "212:215")
#define DUALY(t, W, X) DUAL(t, W, X, "196:199", "208:211", "212:215", "192:195", "204:207")
__device__ __forceinline__ int lane_id() {
  int l;
  asm volatile("v_mbcnt_lo_u32_b32 %0, -1, 0\n\tv_mbcnt_hi_u32_b32 %0, -1, %0" : "=v"(l));
  return l;
}
#define LANE_VARS \
  TSI() \
  const int lane = lane_id() & 63; \
  const int r32 = lane & 31, h = lane >> 5, r16 = lane & 15, g4 = lane >> 4, gs = g4 & 1; \
  const unsigned l16 = lane * 16; \
  const unsigned aK = L_KV + l16; \
  const unsigned ax = r32 * 128 + ((h ^ ((r32 >> 1) & 7)) * 16); \
  const unsigned awf = (h * 8 + (r16 >> 2)) * 128 + (r16 >> 3) * 64 + gs * 32 + (lane & 3) * 8; \
  const unsigned vsw = ((lane & 7) ^ (gs << 2)) * 16 + hg * 128;
#define PIN(x) asm volatile("" : "+v"(x))

__global__ void __launch_bounds__(512, 2) __attribute__((amdgpu_num_vgpr(64), amdgpu_num_sgpr(88)))
fused(const void* a0, const void* a1, const void* a2, const void* a3, const void* a4, const void* a5, const void* a6, const void*, void*, void*) {
  TS0();
  asm volatile(
               "s_getpc_b64 s[88:89]\n\t.Lfused_a:\n\tv_min_u32 v132, .Lfused_b-.Lfused_a, %2\n\tv_cmpx_lt_u32 vcc, 501, %3\n\t"
               "global_load_dword v255, v132, s[88:89]\n\ts_mov_b64 exec, -1\n\t"
               "v_mov_b64 v[128:129], 0\n\tv_mov_b64 v[130:131], 0\n\tds_write_b128 %0, v[128:131]\n\t"
               "s_load_dwordx2 s[88:89], %1, 0x38\n\ts_load_dwordx4 s[92:95], %1, 0x40\n\t"

               :: "v"((threadIdx.x & 7) * 16), "s"(__builtin_amdgcn_kernarg_segment_ptr()), "v"((threadIdx.x + ((blockIdx.x >> 3) & 15) * 10 - 502) * 128), "v"(threadIdx.x)
               : "memory", "vcc", "v255", "s100", "s101");
  const int wid = __builtin_amdgcn_readfirstlane(threadIdx.x >> 6);
  const int bid = blockIdx.x, xcd = bid & 7, bi = bid >> 3;
  const int tb = (xcd >> 1) * 8 + (bi >> 2);
  const int hg = ((xcd & 1) ^ HX) * 4 + ((bi + HP) & 3);
  const unsigned rot = (2 * ((bi >> 2) + bi)) & 6;
  const int t0 = tb * 64;
#define ARG(i) ((unsigned long long)a##i)
#define LBAR2 "\n\ts_barrier"
#define LBAR ""
#define ARGS(wt) \
  u8* ws; unsigned long long pout; \
  asm volatile(wt "s_mov_b64 %0, s[92:93]\n\ts_mov_b64 %1, s[94:95]" LBAR : "=s"(ws), "=s"(pout) :: "memory"); \
  const __amdgpu_buffer_rsrc_t rws = __builtin_amdgcn_make_buffer_rsrc(ws, 0, WS_END, 0x00020000); \
  const u32x4 rw4 = mkrsrc((unsigned long long)ws, WS_END);
  unsigned T;
  const int w = wid & 3;
  const unsigned ones = 0x3F803F80u;
  f32x16 zero16;
#pragma unroll
  for (int i = 0; i < 16; ++i) zero16[i] = 0.f;

  if (wid >= 4) {
    LANE_VARS
    const unsigned fn = (bid & 3) * 4 + w, fk = (bid >> 2) & 31;
    const bool hi = (lane & 16) != 0, on = (lane & 27) == 16;
    unsigned vf = (lane >> 2) * 1024 + (lane & 3) * 16;
    {
      const unsigned aV0 = hi ? (on ? L_KV + 1024u : L_KV + 1040u) : aK, aV1 = hi ? aV0 - 256u : aK;
      asm volatile(REP(20, "v_mov_b64 v[152+2*i_:153+2*i_], 0\n\t") REP(2, "v_mov_b64 v[212+2*i_:213+2*i_], 0\n\t") REP(2, "v_mov_b64 v[240+2*i_:241+2*i_], -1\n\t")
                   "v_mov_b32 v232, 0xdddddddd\n\tv_mov_b32 v233, %1\n\tv_mov_b32 v234, %2\n\tv_mov_b32 v235, %3\n\t"
                   "v_mov_b32 v236, 0\n\tv_mov_b32 v237, %4\n\tv_mov_b32 v238, %5\n\tv_mov_b32 v239, %6\n\t"
                   : "+v"(vf) : "v"(aK), "v"(aV0), "v"(aV1), "v"(L_G + 256 + (w >> 1) * 128 + r32 * 4), "s"(C_LP + w * 4), "v"(l16) : "memory");
    }
    const u32x4 rwo = mkrsrc(bid < 128 ? ARG(4) : ARG(5), 512u * 1024u);
    asm volatile("s_waitcnt lgkmcnt(0)\n\ts_barrier" ::: "memory");
    asm volatile("s_mov_b32 m0, %0\n\ts_nop 0\n\tbuffer_load_dwordx4 %1, %2, %3 offen " NTF " lds"
                 :: "s"(L_TW + w * 1024), "v"(vf), "s"(rwo), "s"(fk * 16384 + fn * 64) : "memory");
    ARGS("s_waitcnt lgkmcnt(0)\n\t")
#define CLEARS() asm volatile(REP(4, "buffer_store_dwordx4 v[240:243], v239, %0, %1 offen offset:i_*1024 sc1\n\t") "buffer_store_dwordx4 v[240:243], v239, %0, %2 offen sc1" \
                 :: "s"(rw4), "s"(WS_O + (w >> 1) * 0x200000 + ((tb * 2 + (w & 1)) * 32 + hg * 4) * 1024), "s"(WS_HL + (tb * 32 + hg * 4 + w) * 1024) : "memory")
    TS(1);
    VMW(CLR == 0 ? 5 : 0);
    {
      const unsigned ao = L_TW + w * 1024 + (h * 4 + (r16 >> 2)) * 64 + (lane & 1) * 32 + ((lane >> 1) & 1) * 8 + gs * 16;
      ST128(__builtin_bit_cast(u32x4, cat8(tr_read(ao), tr_read(ao + 512))), l16, WS_WF + (((bid >> 7) * 16 + fn) * 32 + fk) * 1024);
    }
    TS(2);
    u32x4 aQ, aQ1;
    {
      u32x16 c0, c1, c2, c3, c4;
      const unsigned xb = L_X(0) + ax + (w >> 1) * 4096;
      unsigned x0, x1, x2, x3, xs = rot << 13, vw, qt, bt = 2u, cn = 0u, wo = 81920u, spin = 4000000u;
#define QPOLL "s_bitcmp1_b32 %[cn], %[bt]\n\ts_cbranch_scc1 3f\n\t" \
      "2:\n\tds_read_b32 %[q0], v236 offset:" STR(C_Q) "\n\ts_waitcnt lgkmcnt(0)\n\tv_readfirstlane_b32 %[cn], %[q0]\n\t" \
      "s_bitcmp1_b32 %[cn], %[bt]\n\ts_cbranch_scc1 3f\n\ts_sub_u32 %[spin], %[spin], 1\n\ts_cbranch_scc0 2b\n\ts_mov_b32 %[spin], 0\n\t" \
      "3:\n\ts_add_u32 %[bt], %[bt], 4\n\t"
#define QRD1(f, i, o) "ds_read_b64_tr_b16 v[" f "+" #i "*4:" f "+" #i "*4+1], %[vw] offset:" o "+" #i "*2048\n\t" \
      "ds_read_b64_tr_b16 v[" f "+" #i "*4+2:" f "+" #i "*4+3], %[vw] offset:" o "+" #i "*2048+512\n\t" \
      "ds_read_b128 v[" f "+16+" #i "*4:" f "+16+" #i "*4+3], %[x" #i "] offset:" o "\n\t"
#define QRD(f, o) QRD1(f, 0, o) QRD1(f, 1, o) QRD1(f, 2, o) QRD1(f, 3, o)
#define QMM(f, n) REP(4, "s_waitcnt lgkmcnt(" n ")\n\tv_mfma_f32_32x32x16_bf16 v[48:63], v[" f "+4*i_:" f "+4*i_+3], v[" f "+16+4*i_:" f "+16+4*i_+3], v[48:63]\n\t")
      asm volatile(
        REP(8, "v_mov_b64 v[48+2*i_:49+2*i_], 0\n\t")
        "1:\n\t"
        "v_add_u32 %[vw], %[wo], %[aw]\n\t"
        "v_add_u32 %[x0], %[xs], %[b0]\n\tv_add_u32 %[x1], %[xs], %[b1]\n\tv_add_u32 %[x2], %[xs], %[b2]\n\tv_add_u32 %[x3], %[xs], %[b3]\n\t"
        QPOLL QRD("64", "0")
        "s_cmp_eq_u32 %[bt], 6\n\ts_cbranch_scc1 4f\n\t"
        QMM("96", "12")
        "4:\n\t"
        QPOLL QRD("96", "8192") QMM("64", "12")
        "s_add_u32 %[xs], %[xs], 0x4000\n\ts_and_b32 %[xs], %[xs], 0xffff\n\t"
        "s_add_u32 %[wo], %[wo], 0x4000\n\ts_and_b32 %[wo], %[wo], 0x1ffff\n\t"
        "s_cmp_lg_u32 %[bt], 34\n\ts_cbranch_scc1 1b\n\t"
        QMM("96", "9-3*i_")
        "s_nop 11\n\t"
        REP(8, "v_cvt_pk_bf16_f32 v[40+i_], v[48+2*i_], v[49+2*i_]\n\t")
        : "=&{v[40:43]}"(aQ), "=&{v[44:47]}"(aQ1), "=&{v[48:63]}"(c4), "=&{v[64:79]}"(c0), "=&{v[80:95]}"(c1), "=&{v[96:111]}"(c2), "=&{v[112:127]}"(c3),
          [x0] "=&v"(x0), [x1] "=&v"(x1), [x2] "=&v"(x2), [x3] "=&v"(x3), [xs] "+s"(xs), [vw] "=&v"(vw), [q0] "=&v"(qt), [bt] "+s"(bt), [cn] "+s"(cn), [wo] "+s"(wo), [spin] "+s"(spin)
        : [aw] "v"(32768u + (awf ^ ((w & 1) * 64))), [b0] "v"(xb), [b1] "v"(xb ^ 32u), [b2] "v"(xb ^ 64u), [b3] "v"(xb ^ 96u)
        : "memory", "scc");
    }
    TS(6);
    const unsigned so = WS_O + (2u << 20) + ((tb * 2 + (w >> 1)) * 32 + hg * 4 + (w & 1) * 2) * 1024;
    unsigned long long bad0, bad1;
    {
#define LFIN(d, o, bad, c) \
      "v_rcp_f32 v240, v[" d "+8]\n\t" \
      "v_maximum3_f32 v241, |v[" d "]|, |v[" d "+1]|, |v[" d "+2]|\n\t" \
      "v_maximum3_f32 v242, |v[" d "+3]|, |v[" d "+4]|, |v[" d "+5]|\n\t" \
      "v_maximum3_f32 v241, v241, |v[" d "+6]|, |v[" d "+7]|\n\t" \
      "v_mul_f32 v240, v240, v243\n\t" \
      "v_maximum3_f32 v241, v241, v242, v[" d "+8]\n\t" \
      "v_cmp_nle_f32 vcc, %[lo], v[" d "+8]\n\t" \
      "v_cmp_ngt_f32 " bad ", %[hi], v241\n\t" \
      REP(8, "v_mul_f32 v[" d "+i_], v[" d "+i_], v240\n\t") \
      "s_or_b64 " bad ", " bad ", vcc\n\t" \
      REP(4, "v_cvt_pk_bf16_f32 v[" o "+i_], v[" d "+2*i_], v[" d "+2*i_+1]\n\t") \
      "s_cbranch_scc1 5f\n\t" \
      "buffer_store_dwordx4 v[" o ":" o "+3], v239, %[rs], %[so] offen offset:" c " sc1\n\t" \
      "5:\n\t"
      unsigned tgt = 0u, spin = 4000000u, tmp;
      asm volatile(
        "s_waitcnt vmcnt(0) lgkmcnt(0)\n\ts_setprio 1\n\ts_barrier\n\t"
        TSA(7)
        RDL("192:195", "v233", "0", "0") RDL("196:199", "v233", "1", "0") RDL("200:203", "v234", "0", "1024") RDL("204:207", "v235", "0", "1280") RDL("208:211", "v234", "1", "1024")
        "s_waitcnt lgkmcnt(4)\n\t"
        QKM("128:143", "192:195", "%[q0]")
        "s_nop 11\n\t"
        "1:\n\t"
        DUALX(0, LW1, "ds_write_b32 v238, v233\n\t") DUALY(1, LW1, "ds_read_b32 v243, v237\n\t") DUALX(2, LW1, "s_add_u32 %[tgt], %[tgt], 4\n\t") DUALY(3, LW1, "ds_read_b32 v244, v236\n\t")
        TSG(6, "%[tgt]", 1)
        LW1
        "3:\n\t"
        "v_readfirstlane_b32 %[tmp], v244\n\t"
        "s_cmp_ge_u32 %[tmp], %[tgt]\n\t"
        "s_cbranch_scc0 6f\n\t"
        "2:\n\t"
        DUALX(4, "", "") DUALY(5, LW1, "") DUALX(6, LW1, "") DUALY(7, LW1, "") DUALX(8, LW1, "") DUALY(9, LW1, "") DUALX(10, LW1, "") DUALY(11, LW1, "")
        "v_add_u32 v233, 0x6180, v233\n\t"
        "v_add_u32 v234, 0x6180, v234\n\t"
        "v_add_u32 v235, 0x6180, v235\n\t"
        "s_branch 1b\n\t"
        "7:\n\t"
        "ds_read_b32 v244, v236\n\t"
        "s_waitcnt lgkmcnt(0)\n\t"
        TSG(7, "%[tgt]", 1)
        "s_sub_u32 %[spin], %[spin], 1\n\t"
        "s_cbranch_scc0 3b\n\t"
        "s_mov_b32 %[spin], 0\n\t"
        "s_branch 2b\n\t"
        "6:\n\t"
        "s_cmp_eq_u32 %[tgt], 24\n\t"
        "s_cbranch_scc0 7b\n\t"
        PVM("176:191", "212:215", "152:159")
        LFIN("160", "216", "%[b0]", "0")
        LFIN("176", "224", "%[b1]", "1024")
        ".Lfused_b:\n\t"
        "ds_write_b32 v238, v232\n\t"
        "s_waitcnt lgkmcnt(0)\n\t"
        : [tgt] "+s"(tgt), [spin] "+s"(spin), [tmp] "=&s"(tmp), [b0] "=&s"(bad0), [b1] "=&s"(bad1) TSOP
        : [q0] "v"(aQ), [q1] "v"(aQ1), [lo] "s"(5.4e-20f), [hi] "s"(1.0e36f), [rs] "s"(rw4), [so] "s"(so) : "memory", "scc", "vcc");
    }
    TS(20);
    if ((bad0 | bad1) != 0ull) {
      const int lane = lane_id() & 63;
      const unsigned l16 = lane * 16;
      const bool hi = (lane & 16) != 0, on = (lane & 27) == 16;
#pragma nounroll
      for (int qi = 0; qi < 2; ++qi) {
        if ((qi ? bad1 : bad0) == 0ull) continue;
        const bf16x8 qf = __builtin_bit_cast(bf16x8, qi ? aQ1 : aQ);
        float m = -3.0e38f;
#pragma nounroll
        for (int t = 0; t < 64; ++t) {
          const u32x4 kf = __builtin_amdgcn_raw_buffer_load_b128(rws, l16, WS_KV + t * 2048, SC1);
          f32x16 s = __builtin_amdgcn_mfma_f32_32x32x16_bf16(__builtin_bit_cast(bf16x8, kf), qf, zero16, 0, 0, 0);
#pragma unroll
          for (int i = 0; i < 16; ++i) m = fmaxf(m, s[i]);
        }
        { auto r = __builtin_amdgcn_permlane32_swap(__builtin_bit_cast(unsigned, m), __builtin_bit_cast(unsigned, m), false, false);
          m = fmaxf(swap_lo(r[0]), swap_lo(r[1])); }
        f32x16 F = zero16;
        const unsigned pad = on ? ones : 0u;
#pragma nounroll
        for (int t = 0; t < 64; ++t) {
          const u32x4 kf = __builtin_amdgcn_raw_buffer_load_b128(rws, l16, WS_KV + t * 2048, SC1);
          u32x4 v0 = __builtin_amdgcn_raw_buffer_load_b128(rws, (l16 & ~256u) + 1024, WS_KV + t * 2048, SC1);
          u32x4 v1 = __builtin_amdgcn_raw_buffer_load_b128(rws, (l16 & ~256u) + 1280, WS_KV + t * 2048, SC1);
#pragma unroll
          for (int i = 0; i < 4; ++i) { v0[i] = hi ? pad : v0[i]; v1[i] = hi ? pad : v1[i]; }
          f32x16 s = __builtin_amdgcn_mfma_f32_32x32x16_bf16(__builtin_bit_cast(bf16x8, kf), qf, zero16, 0, 0, 0);
          f32x8 p0, p1;
#pragma unroll
          for (int i = 0; i < 8; ++i) { p0[i] = __builtin_amdgcn_exp2f(s[i] - m); p1[i] = __builtin_amdgcn_exp2f(s[8 + i] - m); }
          F = __builtin_amdgcn_smfmac_f32_32x32x32_bf16(__builtin_bit_cast(bf16x8, v0), __builtin_bit_cast(bf16x16, p0), F, 0xDDDD, 0, 0);
          F = __builtin_amdgcn_smfmac_f32_32x32x32_bf16(__builtin_bit_cast(bf16x8, v1), __builtin_bit_cast(bf16x16, p1), F, 0xDDDD, 0, 0);
        }
        const float iv = __builtin_amdgcn_rcpf(F[8]) * lds_ld<float>(L_G + 256 + (w >> 1) * 128 + (lane & 31) * 4);
        ST128(((u32x4){pk_bf16(F[0] * iv, F[1] * iv), pk_bf16(F[2] * iv, F[3] * iv), pk_bf16(F[4] * iv, F[5] * iv), pk_bf16(F[6] * iv, F[7] * iv)}), l16, so + qi * 1024);
      }
    }
    TSDUMP();
    return;
  }

  LANE_VARS
  const int sp = (bi >> 1) & 1, isV = bi & 1;
  const int jg = (xcd & 1) * 2 + sp, xg = (jg + w) & 3;
  unsigned vx0 = (unsigned)(t0 + xg * 16 + (lane >> 3)) * 1024u + (unsigned)(((lane & 7) ^ g4) * 16), vx1 = (vx0 ^ 64u) + 7168u;
  const unsigned aj = L_X(0) + (xg * 16 + r16) * 128 + ((g4 ^ (r16 >> 1)) * 16);
  const unsigned n0 = (w * 5 - 4) >> 1;
  unsigned vw0 = (n0 * 8 + (lane >> 3)) * 1024u + vsw, vw1 = vw0 + 7168u, vw2 = vw0 + 14336u;
  PIN(vx0); PIN(vx1); PIN(vw0); PIN(vw1); PIN(vw2);
  const u32x4 rx = mkrsrc(ARG(0), 2048u * 1024u), rw = mkrsrc(ARG(1), 512u * 1024u);
  unsigned c = rot;
#define QREQ(nt) "s_and_b32 %[c], %[c], 7\n\ts_add_u32 m0, %[mw], (81920+j_*8192)&131071\n\ts_lshl_b32 %[so], %[c], 16\n\t" \
        "buffer_load_dwordx4 %[w0], %[rw], %[so] offen " nt " lds\n\tbuffer_load_dwordx4 %[w1], %[rw], %[so] offen offset:1024 " nt " lds\n\t" \
        "buffer_load_dwordx4 %[w2], %[rw], %[so] offen offset:2048 " nt " lds\n\t" \
        "s_lshr_b32 %[so], %[so], 3\n\ts_add_u32 m0, %[mx], %[so]\n\ts_lshl_b32 %[so], %[c], 7\n\ts_add_u32 %[c], %[c], 1\n\t" \
        "buffer_load_dwordx4 %[x0], %[rx], %[so] offen " nt " lds\n\tbuffer_load_dwordx4 %[x1], %[rx], %[so] offen offset:1024 " nt " lds\n\t"
#define QOPS [mw] "s"(32768u + n0 * 1024u), [mx] "s"(L_X(0) + xg * 2048), [w0] "v"(vw0), [w1] "v"(vw1), [w2] "v"(vw2), [rw] "s"(rw), [x0] "v"(vx0), [x1] "v"(vx1), [rx] "s"(rx)
  if (w == 0) {
    const u32x4 rkv = mkrsrc(isV ? ARG(3) : ARG(2), 16384u), rex = mkrsrc(ARG(6), 1024u);
#pragma nounroll
    for (unsigned j = 0; j < 8; ++j) {
      DMA2(L_EW + j * 2048, l16, l16, rkv, j << 11);
      DMA2(L_X(j) + xg * 2048, vx0, vx1, rx, j << 7);
    }
    asm volatile("s_mov_b32 m0, %0\n\ts_nop 0\n\tbuffer_load_dwordx4 %1, %2, 0 offen " NT0 " lds" :: "s"(L_GW), "v"(l16), "s"(rex) : "memory");
    TS(30);
    asm volatile("s_waitcnt lgkmcnt(0)\n\ts_load_dword s100, s[92:93], 0x800" LBAR2 ::: "memory", "s100");
  }
  unsigned vz = 0u, v1 = 1u;
  const unsigned cst = (lane & 1) ? 0u : ones;
  u32x4 cst4 = {cst, cst, cst, cst};
  const unsigned acs = L_KV + (lane >> 1) * KVS + 2048 + (lane & 1) * 16;
  unsigned am = aK + (w ? 2 + 4 * w : 0) * KVS, amc = acs + (w ? 2 + 4 * w : 0) * KVS;
  unsigned rb = (unsigned)w * 16384u;
  asm volatile("s_add_u32 %0, %0, %1" : "+s"(rb) : "n"(L_RING - 16384) : "scc");
  unsigned va0 = (unsigned)(lane >> 3) * 3072u + (unsigned)(w - 1) * 1024u + vsw, va1 = va0 + 23552u, va2 = va1 + 23552u, va3 = va2 + 23552u;
  unsigned aW0 = rb + awf, aW1 = rb + (awf ^ 64u), ax0 = L_X(0) + ax, ax1 = L_X(0) + (ax ^ 32u), ax2 = L_X(0) + (ax ^ 64u), ax3 = L_X(0) + (ax ^ 96u);
  unsigned ap = L_PL + (w == 1 ? 5120 : w * 2560 - 4608) + r32 * 32 + h * 16, apg = C_PG + (w - 1) * 4;
  unsigned ah = L_PL + h * 2560 + r32 * 16, l4 = lane * 4, lak = L_PL + w * 512 + r16 * 64 + g4 * 8, laq = L_PL + 5120 + w * 512 + r16 * 32 + g4 * 8;
  unsigned lav = L_PL + 2560 + w * 512 + g4 * 256 + (r16 >> 2) * 64 + (r16 & 3) * 8, lag = L_G + w * 64 + r16 * 4, lvo = gs * 8 + (h * 32 + r16) * 16;
  f32x4 mk;
#pragma unroll
  for (int r = 0; r < 4; ++r) mk[r] = ((unsigned)(4 * g4 + r - (r16 >> 1) - 1) < 8u) ? ((tb | w | h) ? 0.f : -4.0e9f) : -3.0e38f;
  unsigned gaw = L_GA + w * 8192 + l16, gar = L_GA + l16, vout = r32 * 1024 + h * 32, rdy = C_RDY, bit = 1u << (2 * w + (w & 2) * 2), bi2 = 2u << (2 * w + (w & 2) * 2), v4 = 4u;
  PIN(ah); PIN(lak); PIN(laq); PIN(lav); PIN(lag); PIN(lvo); PIN(mk); PIN(gaw); PIN(gar); PIN(vout); PIN(rdy); PIN(bit); PIN(bi2); PIN(v4); PIN(l4);
  f32x16 acc, acd;
  asm volatile(REP(8, "v_mov_b64 v[224+2*i_:225+2*i_], 0\n\t") REP(2, "v_mov_b64 v[240+2*i_:241+2*i_], -1\n\t") REP(16, "v_mov_b64 v[16+2*i_:17+2*i_], 0\n\t") : "={v[16:31]}"(acc), "={v[32:47]}"(acd) :: "memory");
  PIN(vz); PIN(v1); PIN(cst4); PIN(am); PIN(amc); PIN(va0); PIN(va1); PIN(va2); PIN(va3); PIN(aW0); PIN(aW1); PIN(ax0); PIN(ax1); PIN(ax2); PIN(ax3); PIN(ap); PIN(apg);
  const unsigned sm = WS_KT + 8192 + w * 16384;
  unsigned vT;
  unsigned sa = 0u, arw = rb + l16, apf = ((bi >> 2) * PFN + lane) * 3072u + (unsigned)(w - 1) * 1024u + hg * 128;
  PIN(arw); PIN(apf);
#define GREG() asm volatile(REP(4, \
      "buffer_load_dwordx4 v[192+16*i_:195+16*i_], %[a0], s[88:91], %[so] offen\n\tbuffer_load_dwordx4 v[196+16*i_:199+16*i_], %[a1], s[88:91], %[so] offen offset:1024\n\t" \
      "buffer_load_dwordx4 v[200+16*i_:203+16*i_], %[a2], s[88:91], %[so] offen offset:2048\n\tbuffer_load_dwordx4 v[204+16*i_:207+16*i_], %[a3], s[88:91], %[so] offen offset:3072\n\t" \
      "s_add_u32 %[so], %[so], 98304\n\t") \
      : [so] "+s"(sa) : [a0] "v"(va0), [a1] "v"(va1), [a2] "v"(va2), [a3] "v"(va3) : "memory", "scc")
  if (w != 0) {
    unsigned so;
    asm volatile("s_waitcnt lgkmcnt(0)\n\ts_barrier" ::: "memory");
    asm volatile(REPX("j_", QD, QREQ(NT1)) : [c] "+s"(c), [so] "=&s"(so) : QOPS : "memory", "scc");
    TS(30);
    asm volatile("s_waitcnt lgkmcnt(0)\n\ts_load_dword s100, s[92:93], 0x800\n\ts_mov_b32 s90, 0x180000\n\ts_mov_b32 s91, 0x20000" ::: "memory", "s100");
  }
  ARGS("")
  if (w == 0) {
    GETT();
    const unsigned ak = L_EW + (g4 * 8 + (r16 >> 2)) * 32 + (lane & 3) * 8;
    const int slot = (bi >> 2) * 8 + xcd;
    const int u = isV ? gs : h, fl = (isV ? h : gs) * 32 + sp * 16 + r16;
    unsigned at = slot < 18 ? u * 1024 + fl * 16 + WS_KT + slot * 4096 + isV * 2048 : WS_END, au = fl * 16 + u * 8 + WS_KV + slot * 2048 + isV * 1024;
    float sc = isV ? 1.0f : 0.25f * LOG2E;
    vT = T;
    unsigned qp = __builtin_rotateright32(0x1111u, 4u * rot);
    PIN(at); PIN(au); PIN(sc); PIN(vT); PIN(qp);
    f32x4 pa = {0.f, 0.f, 0.f, 0.f}, pb = pa;
#define KVM(a, b) REP(4, ".if i_>0\n\ts_waitcnt lgkmcnt(21-6*i_)\n\t.endif\n\tv_mfma_f32_16x16x32_bf16 %[pa], v[" a "+8*i_:" a "+8*i_+3], v[" b "+8*i_:" b "+8*i_+3], %[pa]\n\t" \
                         ".if i_>0\n\ts_waitcnt lgkmcnt(18-6*i_)\n\t.endif\n\tv_mfma_f32_16x16x32_bf16 %[pb], v[" a "+8*i_+4:" a "+8*i_+7], v[" b "+8*i_+4:" b "+8*i_+7], %[pb]\n\t")
    asm volatile(
      REPV("j_", 2,
        "s_waitcnt vmcnt(17-16*j_)\n\ts_mov_b64 exec, 1\n\tds_add_u32 %[z], %[qp] offset:" STR(C_Q) "\n\tv_xor_b32 %[qp], 0x11111111, %[qp]\n\ts_mov_b64 exec, -1\n\t"
        REP(4, "ds_read_b64_tr_b16 v[192+8*i_:192+8*i_+1], %[ak] offset:j_*8192+i_*2048\n\tds_read_b64_tr_b16 v[192+8*i_+2:192+8*i_+3], %[ak] offset:j_*8192+i_*2048+128\n\t"
               "ds_read_b128 v[128+32*j_+8*i_:128+32*j_+8*i_+3], %[a0] offset:j_*32768+i_*8192\n\t"
               "ds_read_b64_tr_b16 v[192+8*i_+4:192+8*i_+5], %[ak] offset:j_*8192+i_*2048+1024\n\tds_read_b64_tr_b16 v[192+8*i_+6:192+8*i_+7], %[ak] offset:j_*8192+i_*2048+1152\n\t"
               "ds_read_b128 v[128+32*j_+8*i_+4:128+32*j_+8*i_+7], %[a1] offset:j_*32768+i_*8192\n\t")
        "s_waitcnt lgkmcnt(15)\n\ts_cmp_lg_u32 %[v], 0\n\ts_cbranch_scc1 1f\n\t"
        KVM("192", "128+32*j_") "s_branch 2f\n\t1:\n\t" KVM("128+32*j_", "192") "2:\n\t")
      "s_waitcnt vmcnt(0)\n\t"
      REP(8, "ds_read_b128 v[192+4*i_:192+4*i_+3], %[ag] offset:" STR(L_GW) "+i_*64\n\t") "s_nop 1\n\t"
      : [pa] "+v"(pa), [pb] "+v"(pb), [qp] "+v"(qp) : [z] "v"(vz), [ak] "v"(ak), [a0] "v"(aj), [a1] "v"(aj ^ 64), [v] "s"(isV), [ag] "v"(g4 * 16) : "memory", "scc");
    const f32x4 p = (pa + pb) * sc;
    u32x2 o; o[0] = pk_bf16(p[0], p[1]); o[1] = pk_bf16(p[2], p[3]);
    ST128(((u32x4){o[0], vT, o[1], vT}), at, 0);
    __builtin_amdgcn_raw_buffer_store_b64(o, rws, au, 0, SC1);
    TS(2);
    f32x4 gz = {0.f, 0.f, 0.f, 0.f};
    asm volatile("s_waitcnt lgkmcnt(0)\n\ts_nop 1\n\t"
      REP(8, "v_mfma_f32_16x16x32_bf16 %[gz], v[192+4*i_:192+4*i_+3], v[128+4*i_:128+4*i_+3], %[gz]\n\t"
             "ds_read_b128 v[192+4*i_:192+4*i_+3], %[ag] offset:" STR(L_GW) "+512+i_*64\n\t")
      REP(8, "s_waitcnt lgkmcnt(7-i_)\n\tv_mfma_f32_16x16x32_bf16 %[gz], v[192+4*i_:192+4*i_+3], v[160+4*i_:160+4*i_+3], %[gz]\n\t")
      "s_nop 7\n\t"
      : [gz] "+v"(gz) : [ag] "v"(g4 * 16) : "memory");
    const float g = __builtin_amdgcn_rcpf(1.f + __builtin_amdgcn_exp2f(-gz[0] * LOG2E));
    TS(3);
    unsigned so = WS_KT;
    asm volatile("s_waitcnt vmcnt(0)\n\ts_mov_b64 exec, 1\n\tbuffer_store_dword %[vT], off, %[rs], %[fo] sc1\n\t"
                 "s_mov_b64 exec, %[gm]\n\tbuffer_store_dword %[g], %[l4], %[rs], %[go] offen sc1\n\ts_mov_b64 exec, -1\n\t"
                 TSA(27)
                 GLDT("128", 24, "%[va]", "%[rs]", "%[so]") "s_waitcnt vmcnt(0)\n\t"
                 : [so] "+s"(so) TSOP
                 : [vT] "v"(vT), [rs] "s"(rw4), [fo] "s"(bid * 4), [g] "v"(g), [l4] "v"(l4), [go] "s"(WS_G + (t0 + jg * 16) * 4), [gm] "s"(0xffull << (8 * isV)), [va] "v"(l16) : "memory", "scc");
    TS(4);
    TAGGET(128, 12, 24, WS_KT);
    asm volatile("buffer_load_dwordx4 v[244:247], %0, %1, 0 offen sc1" :: "v"(l16), "s"(rw4) : "memory");
    KVPUT(128, 8, 12, am, amc);
    POST(C_KV, vz, v1);
  } else {
    unsigned inc = 1u, so;
    asm volatile(
      ".set j_, " STR(QD) "\n\t.rept 9\n\t"
        ".if j_<8\n\t" QREQ(NT2) ".set n_, 5*" STR(QD) "\n\t"
        ".else\n\t.set n_, 5*(7+" STR(QD) "-j_)\n\t.endif\n\t"
        ".if j_==" STR(PFJ) " && " STR(PFN) "\n\ts_mov_b64 exec, %[pm]\n\tbuffer_load_dword v254, %[pf], s[88:91], 0 offen\n\ts_mov_b64 exec, -1\n\t.endif\n\t"
        ".if j_>=" STR(PFJ) " && j_<" STR(PFJ) "+" STR(QD) "+1 && " STR(PFN) "\n\t.set n_, n_+1\n\t.endif\n\t"
        ".if j_==7\n\t" TSA(29) ".endif\n\t"
        ".if j_==" STR(MJ) "+" STR(QD) "\n\ts_mov_b32 %[so], %[sm]\n\t" TSA(27) GLDT("128", 16, "%[va]", "%[rs]", "%[so]") ".endif\n\t"
        ".if j_>=" STR(MJ) "+" STR(QD) "\n\t.set n_, n_+16\n\t.endif\n\t"
        ".if j_<8+" STR(QD) "\n\t"
        "s_waitcnt vmcnt(n_)\n\ts_mov_b64 exec, 1\n\tds_add_u32 %[z], %[inc] offset:" STR(C_Q) "\n\ts_mov_b64 exec, -1\n\tv_lshlrev_b32 %[inc], 4, %[inc]\n\t"
        ".endif\n\t"
        ".if j_==" STR(QD) "\n\t" TSA(2) ".endif\n\t"
        ".if j_==" STR(QD) "+3\n\t" TSA(28) ".endif\n\t"
      ".set j_, j_+1\n\t.endr\n\t"
      : [c] "+s"(c), [so] "=&s"(so), [inc] "+v"(inc) TSOP
      : QOPS, [z] "v"(vz), [sm] "s"(sm), [va] "v"(l16), [rs] "s"(rw4), [pf] "v"(apf), [pm] "s"(~0ull >> ((64 - PFN) & 63))
      : "memory", "scc");
    TS(3);
    GETT();
    VMW(GP == 2 ? 16 : 0);
    TS(4);
    TAGGET(128, 8, 16, sm);
    GREG();
  }
  TS(5);
#define GDMA(q) "s_add_u32 m0, %[rb], " q "*4096\n\ts_nop 0\n\t" \
      "buffer_load_dwordx4 %[a0], s[88:91], %[so] offen " NTG " lds\n\tbuffer_load_dwordx4 %[a1], s[88:91], %[so] offen offset:1024 " NTG " lds\n\t" \
      "buffer_load_dwordx4 %[a2], s[88:91], %[so] offen offset:2048 " NTG " lds\n\tbuffer_load_dwordx4 %[a3], s[88:91], %[so] offen offset:3072 " NTG " lds\n\t" \
      "s_add_u32 %[so], %[so], 98304\n\t"
  RAW_BARRIER();
  TS(7);
  const int ct = tb * 2 + (hg >> 2);
  const int nt = (hg & 3) * 4 + w;
  const unsigned sh = WS_HL + (tb * 32 + hg * 4) * 1024;
  if (w == 0) {
#define FLAGS(fo, n) do { unsigned spin = 200000u; \
      asm volatile("s_waitcnt vmcnt(%[n_])\n\t1:\n\t" \
                   "v_bitop3_b32 v244, v244, v245, %[T] bitop3:0x7e\n\tv_bitop3_b32 v246, v246, v247, %[T] bitop3:0x7e\n\t" \
                   "v_or_b32 v244, v244, v246\n\tv_cmp_ne_u32 vcc, 0, v244\n\ts_cbranch_vccz 2f\n\t" \
                   "buffer_load_dwordx4 v[244:247], %[va], %[rs], 0 offen offset:" #fo " sc1\n\ts_waitcnt vmcnt(0)\n\t" \
                   "s_sub_u32 %[sp], %[sp], 1\n\ts_cbranch_scc0 1b\n\t2:\n\t" \
                   : [sp] "+s"(spin) : [va] "v"(l16), [rs] "s"(rw4), [T] "s"(T), [n_] "n"(n) : "memory", "scc", "vcc"); } while (0)
#define FLAG2() asm volatile("s_mov_b64 exec, 1\n\tbuffer_store_dword %0, off, %1, %2 offset:1024 sc1\n\ts_mov_b64 exec, -1" :: "v"(vT), "s"(rw4), "s"(bid * 4) : "memory")
    PRIO(2);
    FLAGS(0, (CLR < 2));
    am += 18 * KVS; amc += 18 * KVS;
    PIN(am); PIN(amc);
    PRIO(P_W);
    TS(8);
    GLDN(128, 24, WS_KV + 18 * 2048);
    {
      unsigned so = WS_O + (tb * 64 + hg * 4) * 1024;
      asm volatile(REPV("j_", 5, REP(4, "buffer_store_dwordx4 v[240:243], %[va], %[rs], %[so] offen offset:i_*1024 sc1\n\t")
                        ".if j_==3\n\ts_mov_b32 %[so], %[sh]\n\t.elseif j_==1\n\ts_add_u32 %[so], %[so], 0x1f8000\n\t.else\n\ts_add_u32 %[so], %[so], 0x8000\n\t.endif\n\t")
                   "s_waitcnt vmcnt(0)"
                   : [so] "+s"(so) : [va] "v"(l16), [rs] "s"(rw4), [sh] "s"(sh) : "memory", "scc");
    }
    FLAG2();
    TS(6);
    wait_min<C_PG, 3, 2>(vz, 65536u);
    asm volatile("buffer_load_dwordx4 v[244:247], %0, %1, 0 offen offset:1024 sc1" :: "v"(l16), "s"(rw4) : "memory");
    VMW(1);
    PRIO(P_F);
    TS(9);
    KVPUT(128, 4, 24, am, amc);
    POST(C_KV, vz, v4);
    TS(13);
    FLAGS(1024, 0);
    if (bid == 0) asm volatile("s_mov_b64 exec, 1\n\tbuffer_store_dword %0, off, %1, 0 offset:2048 sc1\n\ts_mov_b64 exec, -1" :: "v"(vT), "s"(rw4) : "memory");
    asm volatile("buffer_load_dword v244, %0, %1, %2 offen sc1\n\ts_waitcnt lgkmcnt(0)" :: "v"(l4), "s"(rw4), "s"(WS_G + t0 * 4) : "memory");
#pragma nounroll
    for (unsigned b = 1; b < 3; ++b) {
      GLDN(128, 24, WS_KV + (18 + 12 * b) * 2048);
      am += 12 * KVS; amc += 12 * KVS;
      PIN(am); PIN(amc);
      asm volatile("s_waitcnt vmcnt(24)\n\tv_sub_f32 v245, 1.0, v244\n\tds_write2_b32 %0, v244, v245 offset0:32 offset1:96" :: "v"(l4) : "memory");
      PRIO(P_W);
      wait_min<C_PG, 3, 2>(vz, 81920u + b * 16384u);
      VMW(0);
      PRIO(P_F);
      KVPUTS(128, 4, 24, am, amc);
      POST(C_KV, vz, v4);
      asm volatile("s_waitcnt lgkmcnt(0)" ::: "memory");
    }
    GLDN(128, 20, WS_KV + 54 * 2048);
    am += 12 * KVS; amc += 12 * KVS;
    PIN(am); PIN(amc);
    TS(10);
    PRIO(P_W);
    wait_min<C_PG, 3, 1>(vz, 131072u);
    PRIO(P_F);
    if (tb) asm volatile(REP(4, "buffer_load_dwordx4 v[208+4*i_:208+4*i_+3], %0, %1, %2 offen offset:i_*1024 sc1\n\t") "s_waitcnt vmcnt(4)" :: "v"(l16), "s"(rw4), "s"(sh - 32 * 1024) : "memory");
    else VMW(0);
    KVPUTS(128, 4, 20, am, amc);
    POST(C_KV, vz, v4);
    TS(12);
    PRIO(P_LA0);
    if (tb) {
      unsigned spin = 200000u;
      asm volatile("1:\n\ts_waitcnt vmcnt(0)\n\t" FULLT("208", "v244") "s_cbranch_vccz 2f\n\t"
                   REP(4, "buffer_load_dwordx4 v[208+4*i_:208+4*i_+3], %[va], %[rs], %[so] offen offset:i_*1024 sc1\n\t")
                   "s_sub_u32 %[sp], %[sp], 1\n\ts_cbranch_scc0 1b\n\t2:\n\t"
                   REP(4, "ds_write_b128 %[ah], v[208+4*i_:208+4*i_+3] offset:i_*7168\n\t")
                   : [sp] "+s"(spin) : [va] "v"(l16), [rs] "s"(rw4), [so] "s"(sh - 32 * 1024), [ah] "v"(ah) : "memory", "scc", "vcc");
    } else {
      asm volatile(REP(4, "ds_write_b128 %0, v[224:227] offset:i_*7168\n\t") :: "v"(ah) : "memory");
    }
    wait_ge<C_PL, 1>(vz, 3);
  } else {
#define GRD(f, wo, xr, xo) \
      "ds_read_b64_tr_b16 v[" f ":" f "+1], %[w0] offset:" wo "\n\tds_read_b64_tr_b16 v[" f "+2:" f "+3], %[w0] offset:" wo "+512\n\t" \
      "ds_read_b128 v[" f "+8:" f "+11], " xr " offset:" xo "\n\t" \
      "ds_read_b64_tr_b16 v[" f "+4:" f "+5], %[w1] offset:" wo "\n\tds_read_b64_tr_b16 v[" f "+6:" f "+7], %[w1] offset:" wo "+512\n\t" \
      "ds_read_b128 v[" f "+12:" f "+15], " xr " offset:" xo "+4096\n\t"
#define GM1(a, f, i, j, c) "v_mfma_f32_32x32x16_bf16 v[" a "], v[" f "+" i ":" f "+" i "+3], v[" f "+" j ":" f "+" j "+3], " c "\n\t"
#define GMM(f) GM1("128:143", f, "0", "8", "v[128:143]") GM1("160:175", f, "4", "8", "v[160:175]") GM1("144:159", f, "0", "12", "v[144:159]") GM1("176:191", f, "4", "12", "v[176:191]")
#define GST(q, xb, xa, xo, lab) GRD("208", q "*4096+2048", xb, "(" q ">>1)*8192") "s_waitcnt lgkmcnt(6)\n\t" GMM("192") lab \
      "s_waitcnt vmcnt(8)\n\t" GRD("192", "((" q "+1)&3)*4096", xa, xo) "s_waitcnt lgkmcnt(6)\n\t" GDMA(q) GMM("208")
    unsigned cnt = 4, tmp;
    KVPUT(128, 8, 8, am, amc);
    POST(C_KV, vz, v1);
    TS(6);
    asm volatile(REP(16, ".if (i_&3)==0\n\ts_waitcnt vmcnt(12-i_)\n\t.endif\n\tds_write_b128 %0, v[192+4*i_:195+4*i_] offset:i_*1024\n\t") :: "v"(arw) : "memory");
    TS(8);
    asm volatile(
      "s_waitcnt vmcnt(12)\n\t"
      TSA(9)
      GRD("192", "0", "%[x0]", "0") GRD("208", "2048", "%[x1]", "0")
      "s_waitcnt lgkmcnt(6)\n\t"
      GM1("128:143", "192", "0", "8", "0") GM1("160:175", "192", "4", "8", "0") GM1("144:159", "192", "0", "12", "0") GM1("176:191", "192", "4", "12", "0")
      "s_branch 2f\n\t"
      "1:\n\t"
      GST("0", "%[x1]", "%[x2]", "0", "2:\n\t") GST("1", "%[x3]", "%[x0]", "8192", "") GST("2", "%[x1]", "%[x2]", "8192", "") GST("3", "%[x3]", "%[x0]", "16384", "")
      "s_setprio 2\n\t"
      "v_add_u32 %[x0], 0x4000, %[x0]\n\tv_add_u32 %[x1], 0x4000, %[x1]\n\tv_add_u32 %[x2], 0x4000, %[x2]\n\tv_add_u32 %[x3], 0x4000, %[x3]\n\t"
      "s_setprio 0\n\t"
      "s_sub_u32 %[cnt], %[cnt], 1\n\t"
      "ds_write_b32 %[pg], %[x0]\n\t"
      "s_cmp_gt_u32 %[cnt], 1\n\t"
      "s_cselect_b32 s90, s90, 0\n\t"
      "s_cmp_lg_u32 %[cnt], 0\n\t"
      "s_cbranch_scc1 1b\n\t"
      "ds_read_b32 v192, %[z] offset:" STR(C_KV) "\n\t"
      "s_waitcnt vmcnt(0) lgkmcnt(0)\n\t"
      "ds_write_b32 %[pg], %[ap]\n\t"
      TSA(10)
      "s_nop 7\n\t"
      "s_setprio " STR(P_PK) "\n\t"
      REP(32, "v_cvt_pk_bf16_f32 v[128+(i_>>2)*8+(i_&3)], v[128+2*i_], v[128+2*i_+1]\n\t")
      REP(8, "v_mov_b64 v[224+2*i_:225+2*i_], 0\n\t")
      "4:\n\tv_readfirstlane_b32 %[tmp], v192\n\ts_cmp_ge_u32 %[tmp], 8\n\ts_cbranch_scc1 5f\n\t"
      "s_sleep 2\n\tds_read_b32 v192, %[z] offset:" STR(C_KV) "\n\ts_waitcnt lgkmcnt(0)\n\ts_add_u32 %[cnt], %[cnt], 1024\n\ts_cbranch_scc0 4b\n\t5:\n\t"
      "s_setprio " STR(P_PL) "\n\t"
      "s_cmp_eq_u32 %[rb], " STR(L_RING) "\n\ts_cbranch_scc1 3f\n\t"
      "s_mov_b64 exec, %[up]\n\t"
      REP(4, "buffer_store_dwordx4 v[144+(i_>>1)*32+(i_&1)*8:144+(i_>>1)*32+(i_&1)*8+3], %[ap], %[rs], %[sh] offen offset:i_*1024 sc1\n\t")
      "s_mov_b64 exec, -1\n\t3:\n\t"
      : [so] "+s"(sa), [cnt] "+s"(cnt), [tmp] "=&s"(tmp), [x0] "+v"(ax0), [x1] "+v"(ax1), [x2] "+v"(ax2), [x3] "+v"(ax3) TSOP
      : [rb] "s"(rb), [a0] "v"(va0), [a1] "v"(va1), [a2] "v"(va2), [a3] "v"(va3), [w0] "v"(aW0), [w1] "v"(aW1), [pg] "v"(apg), [ap] "v"(ap),
        [z] "v"(vz), [up] "s"(0xffff0000ffff0000ull), [rs] "s"(rw4), [sh] "s"(sh - L_PL - w * 2048 + 3072)
      : "memory", "scc");
    wait_min<C_PG, 3, 1>(vz, 131072u);
    asm volatile(REP(8, "ds_write_b128 %0, v[128+8*i_:128+8*i_+3] offset:((i_>>2)*2+(i_&1))*7168+((i_>>1)&1)*1024\n\t") :: "v"(ap) : "memory");
    POST(C_PL, vz, v1);
    if (tmp < 12) wait_ge<C_KV, 1>(vz, 12);
    wait_ge<C_PL, 1>(vz, 3);
    PRIO(P_LA);
  }
  TS(11);
  GLDN(128, 24, WS_WF + nt * 32768);
  {
    u32x16 c0, c1, c2, c3; u32x4 c4;
    unsigned so = __builtin_amdgcn_readfirstlane(WS_O + ((tb * 2 + (w >> 1)) * 32 + hg * 4) * 1024 + (w & 1) * 256), sw = WS_WF + nt * 32768 + 24576, gt;
#define MM16(d, a, b, c) "v_mfma_f32_16x16x32_bf16 v[" d "+4*i_:" d "+4*i_+3], " a ", v[" b "+4*i_:" b "+4*i_+3], " c "\n\t"
#define BC(i) "v[56+(" i "&2):57+(" i "&2)]"
    asm volatile(
      "s_mov_b32 s92, 0x3eb8aa3b\n\t"
      "s_mov_b64 exec, %[ev]\n\t" REP(4, "ds_read_b64 v[224+4*i_:224+4*i_+1], %[aq] offset:i_*7168\n\t")
      "s_not_b64 exec, exec\n\t" REP(4, "ds_read_b64 v[224+4*i_+2:224+4*i_+3], %[aq] offset:i_*7168\n\t")
      "s_mov_b64 exec, -1\n\t"
      REP(8, "ds_read_b64 v[64+2*i_:64+2*i_+1], %[ak] offset:(i_>>1)*7168+(i_&1)*32\n\t")
      REP(4, "s_waitcnt lgkmcnt(6-2*i_)\n\t" MM16("96", "v[64+4*i_:64+4*i_+3]", "224", "%[mk]"))
      REP(8, "ds_read_b64_tr_b16 v[80+2*i_:80+2*i_+1], %[av] offset:(i_>>1)*7168+(i_&1)*32\n\t")
      "ds_read_b32 %[gt], %[ag]\n\t"
      REP(4, "v_max3_f32 v[56+i_], v[96+4*i_], v[97+4*i_], v[98+4*i_]\n\t")
      REP(4, "v_max_f32 v[56+i_], v[56+i_], v[99+4*i_]\n\t")
      "v_permlane32_swap_b32 v56, v57\n\ts_nop 0\n\tv_permlane32_swap_b32 v58, v59\n\t"
      "v_max_f32 v56, v56, v57\n\tv_max_f32 v58, v58, v59\n\ts_nop 1\n\t"
      "v_permlane16_swap_b32 v56, v58\n\tv_max_f32 v56, v56, v58\n\t"
      "v_mul_f32 v57, 0xbeb8aa3b, v56\n\tv_mul_f32 v59, 0xbeb8aa3b, v56\n\ts_nop 1\n\t"
      "v_permlane16_swap_b32 v57, v59\n\tv_mov_b32 v56, v57\n\tv_mov_b32 v58, v59\n\ts_nop 0\n\t"
      "v_permlane32_swap_b32 v56, v57\n\tv_permlane32_swap_b32 v58, v59\n\t"
      REP(8, ".if (i_&2)==0\n\tv_pk_fma_f32 v[96+2*i_:97+2*i_], v[96+2*i_:97+2*i_], s[92:93], " BC("(i_>>1)") " op_sel:[0,0,0] op_sel_hi:[1,0,0]\n\t"
             ".else\n\tv_pk_fma_f32 v[96+2*i_:97+2*i_], v[96+2*i_:97+2*i_], s[92:93], " BC("(i_>>1)") " op_sel:[0,0,1] op_sel_hi:[1,0,1]\n\t.endif\n\t")
      REP(16, "v_exp_f32 v[96+i_], v[96+i_]\n\t")
      "s_mov_b64 exec, %[ev]\n\t" REP(8, "v_cvt_pk_bf16_f32 v[224+(i_>>1)*4+(i_&1)], v[96+2*i_], v[97+2*i_]\n\t")
      "s_not_b64 exec, exec\n\t" REP(8, "v_cvt_pk_bf16_f32 v[226+(i_>>1)*4+(i_&1)], v[96+2*i_], v[97+2*i_]\n\t")
      "s_mov_b64 exec, -1\n\t"
      REP(4, MM16("112", "%[one]", "224", "0"))
      "s_waitcnt lgkmcnt(0)\n\t"
      REP(4, MM16("64", "v[80+4*i_:80+4*i_+3]", "224", "0"))
      REP(4, "v_rcp_f32 v[56+i_], v[112+4*i_]\n\t")
      REP(4, "v_mul_f32 v[56+i_], v[56+i_], %[gt]\n\t")
      REP(8, ".if (i_&2)==0\n\tv_pk_mul_f32 v[64+2*i_:65+2*i_], v[64+2*i_:65+2*i_], " BC("(i_>>1)") " op_sel:[0,0] op_sel_hi:[1,0]\n\t"
             ".else\n\tv_pk_mul_f32 v[64+2*i_:65+2*i_], v[64+2*i_:65+2*i_], " BC("(i_>>1)") " op_sel:[0,1] op_sel_hi:[1,1]\n\t.endif\n\t")
      REP(8, "v_cvt_pk_bf16_f32 v[80+i_], v[64+2*i_], v[65+2*i_]\n\t")
      REP(4, "buffer_store_dwordx2 v[80+2*i_:80+2*i_+1], %[vo], %[rs], %[so] offen offset:i_*1024 sc1\n\t")
      GLDT("224", 8, "%[va]", "%[rs]", "%[sw]")
      : "=&{v[64:79]}"(c0), "=&{v[80:95]}"(c1), "=&{v[96:111]}"(c2), "=&{v[112:127]}"(c3), "=&{v[56:59]}"(c4), [gt] "=&v"(gt), [sw] "+s"(sw)
      : [so] "s"(so), [ev] "s"(0x5555555555555555ull), [aq] "v"(laq), [ak] "v"(lak), [av] "v"(lav), [ag] "v"(lag), [mk] "v"(mk), [one] "v"(cst4), [vo] "v"(lvo), [rs] "s"(rw4), [va] "v"(l16)
      : "memory", "scc");
  }
  PRIO(P_TA);
  TS(14);
  if (w == 0) POST(C_PL, vz, v1);
  wait_ge<C_PL, 2>(vz, 4);
  wait_min<C_LP, 4, 2>(vz, L_KV + 24 * KVS);
  TS(15);
  {
#define TCHK(b, r, o, bit) \
      "s_bitcmp1_b32 %[pend], " b "\n\ts_cbranch_scc0 1f\n\t" \
      FULLT(r, "%[tv]") "s_cbranch_vccnz 1f\n\t" \
      REP(4, "ds_write_b128 %[aw], v[" r "+4*i_:" r "+4*i_+3] offset:" o "+i_*1024\n\t") \
      "s_mov_b64 exec, 1\n\tds_or_b32 %[ar], " bit "\n\ts_mov_b64 exec, -1\n\t" \
      "s_bitset0_b32 %[pend], " b "\n\t1:\n\t"
    u32x16 c0, c1, c4; u32x8 c5;
    unsigned so, sb = WS_O + (ct * 32 + w * 8) * 1024, spin = 400000u, pend, done, todo, tv;
    asm volatile(
      "s_mov_b64 s[88:89], %[out]\n\ts_mov_b32 s90, 0\n\ts_mov_b32 s91, 0x20000\n\t"
      "10:\n\t"
      "s_mov_b32 %[pend], 7\n\ts_mov_b32 %[done], 0\n\t"
      "11:\n\t"
      "s_bitcmp1_b32 %[pend], 0\n\ts_cbranch_scc0 1f\n\t"
      "s_mov_b32 %[so], %[sb]\n\t" GLDT("64", 4, "%[va]", "%[rs]", "%[so]")
      "1:\n\t"
      "s_bitcmp1_b32 %[pend], 1\n\ts_cbranch_scc0 1f\n\t"
      "s_add_u32 %[so], %[sb], 0x1000\n\t" GLDT("80", 4, "%[va]", "%[rs]", "%[so]")
      "1:\n\t"
      "s_bitcmp1_b32 %[pend], 2\n\ts_cbranch_scc1 12f\n\t"
      "ds_read_b32 %[tv], %[ar]\n\ts_waitcnt lgkmcnt(0)\n\tv_readfirstlane_b32 %[so], %[tv]\n\t"
      "s_add_u32 %[todo], %[so], 0x101\n\ts_andn2_b32 %[so], %[so], %[todo]\n\t"
      "s_andn2_b32 %[todo], %[so], %[done]\n\ts_cbranch_scc0 12f\n\t"
      "s_mov_b32 %[done], %[so]\n\ts_mov_b32 vcc_lo, s90\n\ts_mov_b32 vcc_hi, 0\n\t"
      REPV("g_", 8,
        "s_bitcmp1_b32 %[todo], g_+(g_&4)\n\ts_cbranch_scc0 3f\n\t"
        REP(4, "ds_read_b128 v[96+4*i_:96+4*i_+3], %[rd] offset:g_*4096+i_*1024\n\t")
        "2:\n\t"
        REP(4, ".if g_<7\n\ts_waitcnt lgkmcnt(3)\n\t.else\n\ts_waitcnt lgkmcnt(3-i_)\n\t.endif\n\t"
               "v_mfma_f32_32x32x16_bf16 v[16+(g_&4)*4:31+(g_&4)*4], v[128+16*g_+4*i_:128+16*g_+4*i_+3], v[96+4*i_:96+4*i_+3], v[16+(g_&4)*4:31+(g_&4)*4]\n\t"
               ".if g_<7\n\tds_read_b128 v[96+4*i_:96+4*i_+3], %[rd] offset:g_*4096+4096+i_*1024\n\t.endif\n\t")
        "s_cbranch_vccnz 4f\n\ts_add_u32 %[so], %[wb], g_*4096\n\t"
        REP(4, "buffer_load_dwordx4 v[128+16*g_+4*i_:128+16*g_+4*i_+3], %[va], %[rs], %[so] offen offset:i_*1024 sc1\n\t")
        "4:\n\t"
        ".if g_<7\n\ts_bitcmp1_b32 %[todo], g_+1+((g_+1)&4)\n\ts_cbranch_scc1 2f\n\t.endif\n\t"
        "3:\n\t")
      "s_cmp_eq_u32 %[done], 0xf0f\n\ts_cbranch_scc1 13f\n\t"
      "12:\n\t"
      "s_and_b32 %[pend], %[pend], 3\n\ts_cbranch_scc0 14f\n\t"
      "s_cmp_eq_u32 %[pend], 3\n\ts_cbranch_scc1 1f\n\ts_waitcnt vmcnt(0)\n\t1:\n\ts_waitcnt vmcnt(4)\n\ts_setprio " STR(P_CK) "\n\t"
      TCHK("0", "64", "0", "%[bit]")
      "s_waitcnt vmcnt(0)\n\t"
      TCHK("1", "80", "4096", "%[bi2]")
      "s_setprio " STR(P_TA) "\n\t"
      "14:\n\t"
      "s_cmp_lg_u32 s90, 0\n\ts_cbranch_scc1 1f\n\ts_sleep " STR(ASLP) "\n\t1:\n\t"
      "s_sub_u32 %[spin], %[spin], 1\n\ts_cbranch_scc0 11b\n\t"
      "13:\n\t"
      "s_nop 11\n\t"
      REP(8, "v_pk_add_f32 v[96+2*i_:97+2*i_], v[16+2*i_:17+2*i_], v[32+2*i_:33+2*i_]\n\t")
      REP(8, "v_cvt_pk_bf16_f32 v[112+i_], v[96+2*i_], v[97+2*i_]\n\t")
      "buffer_store_dwordx4 v[112:115], %[vo], s[88:91], %[oo] offen\n\t"
      "buffer_store_dwordx4 v[116:119], %[vo], s[88:91], %[oo] offen offset:16\n\t"
      "s_cmp_lg_u32 s90, 0\n\ts_cbranch_scc1 15f\n\t"
      TSA(16)
      "v_add_u32 %[aw], 0x8000, %[aw]\n\tv_add_u32 %[rd], 0x8000, %[rd]\n\tv_add_u32 %[ar], 4, %[ar]\n\t"
      "s_add_u32 %[sb], %[sb], 0x200000\n\ts_mov_b32 s90, 0x200000\n\ts_mov_b32 %[spin], 4000000\n\t"
      "1:\n\tds_read_b128 v[96:99], %[ar] offset:" STR(C_LP) "-" STR(C_RDY) "-4\n\ts_waitcnt lgkmcnt(0)\n\t"
      "v_min3_u32 v96, v96, v97, v98\n\tv_min_u32 v96, v96, v99\n\ts_nop 0\n\tv_readfirstlane_b32 %[so], v96\n\t"
      "s_cmp_ge_u32 %[so], " STR(BTRIG) "\n\ts_cbranch_scc1 2f\n\ts_sleep 1\n\ts_sub_u32 %[spin], %[spin], 1\n\ts_cbranch_scc0 1b\n\t2:\n\t"
      "s_sleep " STR(BSLP) "\n\ts_mov_b32 %[spin], 400000\n\t"
      TSA(17)
      "s_branch 10b\n\t"
      "15:\n\t"
      : "+{v[16:31]}"(acc), "+{v[32:47]}"(acd), "=&{v[96:111]}"(c4), "=&{v[112:119]}"(c5), "=&{v[64:79]}"(c0), "=&{v[80:95]}"(c1),
        [so] "=&s"(so), [sb] "+s"(sb), [spin] "+s"(spin), [pend] "=&s"(pend), [done] "=&s"(done), [todo] "=&s"(todo), [tv] "=&v"(tv),
        [aw] "+v"(gaw), [rd] "+v"(gar), [ar] "+v"(rdy) TSOP
      : [va] "v"(l16), [rs] "s"(rw4), [out] "s"(pout), [vo] "v"(vout), [oo] "s"(ct * 32768 + nt * 64), [wb] "s"(WS_WF + (16 + nt) * 32768), [bit] "v"(bit), [bi2] "v"(bi2)
      : "memory", "scc", "vcc");
  }
  TS(26);
  TSDUMP();
}

extern "C" void kernel_launch(void* const* d_in, const int* in_sizes, int n_in,
                              void* d_out, int out_size, void* d_ws, size_t ws_size,
                              hipStream_t stream) {
  (void)hipFuncSetAttribute((const void*)fused, hipFuncAttributeMaxDynamicSharedMemorySize, L_TOT);
  fused<<<256, 512, L_TOT, stream>>>(d_in[0], d_in[4], d_in[5], d_in[6], d_in[2], d_in[7], d_in[8], d_in[1], d_ws, d_out);
}
```

```cpp
#include <hip/hip_runtime.h>
#include <hip/hip_bf16.h>

typedef __bf16 bf16x2 __attribute__((ext_vector_type(2)));
typedef __bf16 bf16x4 __attribute__((ext_vector_type(4)));
typedef __bf16 bf16x8 __attribute__((ext_vector_type(8)));
typedef __bf16 bf16x16 __attribute__((ext_vector_type(16)));
typedef short s16x4 __attribute__((ext_vector_type(4)));
typedef float f32x4 __attribute__((ext_vector_type(4)));
typedef float f32x8 __attribute__((ext_vector_type(8)));
typedef float f32x16 __attribute__((ext_vector_type(16)));
typedef unsigned u32x2 __attribute__((ext_vector_type(2)));
typedef unsigned u32x4 __attribute__((ext_vector_type(4)));
typedef unsigned u32x8 __attribute__((ext_vector_type(8)));
typedef unsigned u32x16 __attribute__((ext_vector_type(16)));
typedef unsigned char u8;
typedef __attribute__((address_space(3))) u8 lds_u8;

#define STR2(x) #x
#define STR(x) STR2(x)
#define LOG2E 1.4426950408889634f
#define SC1 16
#define QD 2
#define NTM 16
#define NT0 ""
#define NT1 ""
#define NT2 ""
#define NTG ""
#define NTF "nt"
#define CPF 1
#define CLR 2
#define MJ 8
#define MPRE 1
#define GP 1
#define PFN 16
#define PFJ 7
#define HP 0
#define HX 0
#define LB 2
#define SWZ 2
#define KSL 3
#define P_PK 0
#define P_LA0 2
#define P_LA 2
#define P_L0 0
#define P_W 0
#define P_F 0
#define P_PL 0
#define P_CK 0
#define BTRIG 0xd0000000
#define F1S 0
#define LAN 3
#define P_GA 2
#define P_FL 2
#define SL1 2
#define SL2 2
#define SL3 1
#define PERM 0
#define ENDP "s_endpgm\n\t"
#define LTRIG 0xd0000000
#define LSL 8
#define LSR 0
#define P_LF 1
#define CSL 0
#define EW 0
#define SWS 8
#define AG 3
#define HPG 1
#define PLT 0
#define MAP 0
#define E1P 3
#define E0P 3
#define LS0 0
#define T1 24
#define KS4 KSL
#define P_TA 0
#define P_TB 0
#define ASLP 4
#define BSLP 0

#define WS_FLAG1 0u
#define WS_EPOCH 2048u
#define WS_G     4096u
#define WS_KV    (64u << 10)
#define WS_KT    (256u << 10)
#define WS_WF    (1u << 20)
#define WS_HL    (2u << 20)
#define WS_O     (4u << 20)
#define WS_END   (8u << 20)
#define TS(i) do {} while (0)
#define TSA(i) ""
#define TSG(o, r, sh) ""
#define TSDUMP() do {} while (0)
#define TS0() do {} while (0)
#define TSI()
#define TSOP
#define TSQ(r) do {} while (0)
#define TSQC() do {} while (0)

#define C_KV   0
#define C_PL   4
#define C_RDY  8
#define C_PG   16
#define C_LP   32
#define C_Q    48
#define C_PH   64
#define L_G    128
#define L_GW   1024
#define L_KV   2048
#define KVS    2080
#define L_EW   2048
#define L_TW   18432
#define L_X(j) (49152u + ((j) << 13))
#define L_WQ(j) (32768u + ((81920u + ((j) << 13)) & 131071u))
#define L_RING 114688
#define L_PL   135168
#define L_GA   2048
#define L_WS   34816
#define L_GB   101888
#define L_TOT  163840

__device__ __forceinline__ unsigned pk_bf16(float lo, float hi) {
  bf16x2 v; v[0] = (__bf16)lo; v[1] = (__bf16)hi;
  return __builtin_bit_cast(unsigned, v);
}
__device__ __forceinline__ u32x4 mkrsrc(unsigned long long a, unsigned bytes) {
  u32x4 r;
  r[0] = (unsigned)a; r[1] = (unsigned)(a >> 32) & 0xffffu; r[2] = bytes; r[3] = 0x00020000u;
  return r;
}
__device__ __forceinline__ bf16x4 tr_read(unsigned a) {
  return __builtin_bit_cast(bf16x4, __builtin_amdgcn_ds_read_tr16_b64_v4i16((__attribute__((address_space(3))) s16x4*)(lds_u8*)(unsigned long long)a));
}
__device__ __forceinline__ bf16x8 cat8(bf16x4 a, bf16x4 b) { return __builtin_shufflevector(a, b, 0, 1, 2, 3, 4, 5, 6, 7); }
template <typename T> __device__ __forceinline__ T lds_ld(unsigned a) { return *(__attribute__((address_space(3))) T*)(lds_u8*)(unsigned long long)a; }
template <typename T> __device__ __forceinline__ void lds_st(unsigned a, T v) { *(__attribute__((address_space(3))) T*)(lds_u8*)(unsigned long long)a = v; }
#define RAW_BARRIER() do { __builtin_amdgcn_s_waitcnt(0xC07F); asm volatile("s_waitcnt lgkmcnt(0)\n\ts_barrier" ::: "memory"); } while (0)
#define VMW(n) asm volatile("s_waitcnt vmcnt(%0)" :: "n"(n) : "memory")
#define ST128(d, vo, so) __builtin_amdgcn_raw_buffer_store_b128((d), rws, (vo) + (so), 0, SC1)
#define SCHED_FENCE() __builtin_amdgcn_sched_barrier(0)
#define PRIO(n) asm volatile("s_setprio " STR(n) ::: "memory")
__device__ __forceinline__ float swap_lo(unsigned a) { return __builtin_bit_cast(float, a); }
#define POST(c, z, v) asm volatile("s_mov_b64 exec, 1\n\tds_add_u32 %0, %1 offset:" STR(c) "\n\ts_mov_b64 exec, -1" :: "v"(z), "v"(v) : "memory")
template <int C, int SLEEP> __device__ __forceinline__ void wait_ge(unsigned z, unsigned target) {
  unsigned t, v, spin = 4000000u;
  asm volatile("1:\n\tds_read_b32 %1, %3 offset:%6\n\ts_waitcnt lgkmcnt(0)\n\tv_readfirstlane_b32 %0, %1\n\ts_cmp_ge_u32 %0, %4\n\ts_cbranch_scc1 2f\n\t"
               "s_sleep %5\n\ts_sub_u32 %2, %2, 1\n\ts_cbranch_scc0 1b\n\t2:"
               : "=&s"(t), "=&v"(v), "+s"(spin) : "v"(z), "s"(target), "n"(SLEEP), "n"(C) : "memory", "scc");
}
template <int C, int N, int SLEEP> __device__ __forceinline__ void wait_min(unsigned z, unsigned target) {
  unsigned t, spin = 4000000u; u32x4 v;
  asm volatile("1:\n\tds_read_b128 v[124:127], %3 offset:%6\n\ts_waitcnt lgkmcnt(0)\n\tv_min3_u32 v124, v124, v125, v126\n\t.if %7==4\n\tv_min_u32 v124, v124, v127\n\t.endif\n\t"
               "s_nop 0\n\tv_readfirstlane_b32 %0, v124\n\t"
               "s_cmp_ge_u32 %0, %4\n\ts_cbranch_scc1 2f\n\ts_sleep %5\n\ts_sub_u32 %2, %2, 1\n\ts_cbranch_scc0 1b\n\t2:"
               : "=&s"(t), "=&{v[124:127]}"(v), "+s"(spin) : "v"(z), "s"(target), "n"(SLEEP), "n"(C), "n"(N) : "memory", "scc");
}
#define DMA2(m, v0, v1, rs, so) asm volatile("s_mov_b32 m0, %0\n\ts_nop 0\n\t" \
    "buffer_load_dwordx4 %1, %3, %4 offen " NT0 " lds\n\tbuffer_load_dwordx4 %2, %3, %4 offen offset:1024 " NT0 " lds" \
    :: "s"(m), "v"(v0), "v"(v1), "s"(rs), "s"(so) : "memory")
#define REPV(v, n, body) ".set " v ", 0\n\t.rept " #n "\n\t" body ".set " v ", " v "+1\n\t.endr\n\t"
#define REPX(v, n, body) REPV(v, n, body)
#define REP(n, body) REPV("i_", n, body)
#define GLDT(r, n, va, rs, so) REP(n, \
    "buffer_load_dwordx4 v[" r "+4*i_:" r "+4*i_+3], " va ", " rs ", " so " offen offset:(i_&3)*1024 sc1\n\t" \
    ".if (i_&3)==3\n\ts_add_u32 " so ", " so ", 0x1000\n\t.endif\n\t")
#define GLDN(r, n, so) do { unsigned so_ = (so); asm volatile(GLDT(#r, n, "%1", "%2", "%0") : "+s"(so_) : "v"(l16), "s"(rw4) : "memory", "scc"); } while (0)
#define TAGT(r, n, t) REP(n, \
    "v_bitop3_b32 v[" r "+8*i_+1], v[" r "+8*i_+1], v[" r "+8*i_+3], " t " bitop3:0x7e\n\t" \
    "v_bitop3_b32 v[" r "+8*i_+5], v[" r "+8*i_+5], v[" r "+8*i_+7], " t " bitop3:0x7e\n\t" \
    ".if i_==0\n\tv_or_b32 v[" r "+7], v[" r "+1], v[" r "+5]\n\t.else\n\t" \
    "v_or3_b32 v[" r "+7], v[" r "+7], v[" r "+8*i_+1], v[" r "+8*i_+5]\n\t.endif\n\t") \
    "v_cmp_ne_u32 vcc, 0, v[" r "+7]\n\t"
#define TAGP(r, n) REP(n, \
    "v_mov_b32 v[" r "+8*i_+1], v[" r "+8*i_+2]\n\t" \
    "v_pk_mov_b32 v[" r "+8*i_+2:" r "+8*i_+3], v[" r "+8*i_+4:" r "+8*i_+5], v[" r "+8*i_+6:" r "+8*i_+7] op_sel:[0,0]\n\t")
#define TAGGET(r, n, n2, so) do { unsigned so_, spin_ = 200000u; asm volatile("1:\n\t" TAGT(#r, n, "%[T]") TAGP(#r, n) "s_cbranch_vccz 2f\n\t" \
    "s_mov_b32 %[o], %[b]\n\t" GLDT(#r, n2, "%[va]", "%[rs]", "%[o]") "s_waitcnt vmcnt(0)\n\ts_sub_u32 %[sp], %[sp], 1\n\ts_cbranch_scc0 1b\n\t2:" \
    : [o] "=&s"(so_), [sp] "+s"(spin_) : [b] "s"(so), [va] "v"(l16), [rs] "s"(rw4), [T] "s"(T) : "memory", "scc", "vcc"); } while (0)
#define FULLT(r, t) "v_max3_u32 " t ", v[" r "], v[" r "+2], v[" r "+4]\n\tv_max3_u32 " t ", " t ", v[" r "+6], v[" r "+8]\n\t" \
    "v_max3_u32 " t ", " t ", v[" r "+10], v[" r "+12]\n\tv_max_u32 " t ", " t ", v[" r "+14]\n\tv_cmp_eq_u32 vcc, -1, " t "\n\t"
#define KVPUT(r, st, n, a, c) asm volatile(REP(n, "ds_write_b128 %0, v[" #r "+" #st "*i_:" #r "+" #st "*i_+3] offset:(i_>>1)*2080+(i_&1)*1024\n\t") \
    "s_mov_b64 exec, (1<<" #n ")-1\n\tds_write_b128 %1, %2\n\ts_mov_b64 exec, -1" :: "v"(a), "v"(c), "v"(cst4) : "memory")
#define KVPUTS(r, st, n, a, c) asm volatile(REP(n, "ds_write_b128 %0, v[" #r "+" #st "*i_:" #r "+" #st "*i_+3] offset:(i_>>1)*2080+(i_&1)*1024\n\t" \
    ".if (i_&1) && " STR(KSL) "\n\ts_sleep " STR(KSL) "\n\t.endif\n\t") \
    "s_mov_b64 exec, (1<<" #n ")-1\n\tds_write_b128 %1, %2\n\ts_mov_b64 exec, -1" :: "v"(a), "v"(c), "v"(cst4) : "memory")
#define KVPART(r, f, n, a) asm volatile(REP(n, "ds_write_b128 %0, v[" #r "+4*(i_+" #f "):" #r "+4*(i_+" #f ")+3] offset:((i_+" #f ")>>1)*2080+(i_&1)*1024\n\t") :: "v"(a) : "memory")
#define KVPAD(n, c) asm volatile("s_mov_b64 exec, (1<<" #n ")-1\n\tds_write_b128 %0, %1\n\ts_mov_b64 exec, -1" :: "v"(c), "v"(cst4) : "memory")
#define GETT() asm volatile("s_waitcnt lgkmcnt(0)\n\ts_mul_i32 %0, s100, 1664525\n\ts_add_u32 %0, %0, 1013904223\n\ts_or_b32 %0, %0, 1" : "=s"(T) :: "memory", "scc")

#define E1(r) "v_exp_f32 v[128+" #r "], v[128+" #r "]\n\t"
#define E4(a, b, c, d) E1(a) E1(b) E1(c) E1(d)
#define PVM(d, a, b) "v_smfmac_f32_32x32x32_bf16 v[" d "], v[" a "], v[" b "], v232\n\t"
#define QKM(d, k, q) "v_mfma_f32_32x32x16_bf16 v[" d "], v[" k "], " q ", 0\n\t"
#define RDL(d, a, t, c) "ds_read_b128 v[" d "], " a " offset:(" t ")*2080+" c "\n\t"
#define M1 2
#define M2 7
#define M3 13
#define RP 0
#define ER(a, b) ".set e_, " a "\n\t.rept (" b ")-(" a ")\n\tv_exp_f32 v[128+e_], v[128+e_]\n\t.set e_, e_+1\n\t.endr\n\t"
#define DUAL(t, W, X, Ka, V0a, V1a, Kb, V1b) \
  ER("0", STR(M1)) W PVM("176:191", V1b, "152:159") RDL(V1b, "v235", #t "+1", "1280") ER(STR(M1), STR(M2)) \
  QKM("144:159", Ka, "%[q1]") RDL(Ka, "v233", #t "+2", "0") ER(STR(M2), STR(M3)) \
  PVM("160:175", V0a, "128:135") ER(STR(M3), "16+" STR(M1)) \
  PVM("160:175", V1a, "136:143") X ER("16+" STR(M1), "16+" STR(M2)) \
  QKM("128:143", Kb, "%[q0]") ER("16+" STR(M2), "16+" STR(M3)) \
  PVM("176:191", V0a, "144:151") RDL(V0a, "v234", #t "+2", "1024") ER("16+" STR(M3), "32")
#define LW1 "s_waitcnt lgkmcnt(1)\n\t"
#define DUALX(t, W, X) DUAL(t, W, X, "192:195", "200:203", "204:207", "196:199", "212:215")
#define DUALY(t, W, X) DUAL(t, W, X, "196:199", "208:211", "212:215", "192:195", "204:207")
__device__ __forceinline__ int lane_id() {
  int l;
  asm volatile("v_mbcnt_lo_u32_b32 %0, -1, 0\n\tv_mbcnt_hi_u32_b32 %0, -1, %0" : "=v"(l));
  return l;
}
#define LANE_VARS \
  TSI() \
  const int lane = lane_id() & 63; \
  const int r32 = lane & 31, h = lane >> 5, r16 = lane & 15, g4 = lane >> 4, gs = g4 & 1; \
  const unsigned l16 = lane * 16; \
  const unsigned aK = L_KV + l16; \
  const unsigned ax = r32 * 128 + ((h ^ ((r32 >> 1) & 7)) * 16); \
  const unsigned awf = (h * 8 + (r16 >> 2)) * 128 + (r16 >> 3) * 64 + gs * 32 + (lane & 3) * 8; \
  const unsigned vsw = ((lane & 7) ^ ((gs << SWZ) & 7)) * 16 + hg * 128;
#define PIN(x) asm volatile("" : "+v"(x))

__global__ void __launch_bounds__(512, 2) __attribute__((amdgpu_num_vgpr(64), amdgpu_num_sgpr(88)))
fused(const void* a0, const void* a1, const void* a2, const void* a3, const void* a4, const void* a5, const void* a6, const void*, void*, void*) {
  TS0();
  asm volatile(
               "s_getpc_b64 s[88:89]\n\t.Lfused_a:\n\tv_min_u32 v132, .Lfused_b-.Lfused_a, %2\n\tv_cmpx_lt_u32 vcc, 501, %3\n\t"
               "global_load_dword v255, v132, s[88:89]\n\ts_mov_b64 exec, -1\n\t"
               "v_mov_b64 v[128:129], 0\n\tv_mov_b64 v[130:131], 0\n\tds_write_b128 %0, v[128:131]\n\t"
               "s_load_dwordx2 s[88:89], %1, 0x38\n\ts_load_dwordx4 s[92:95], %1, 0x40\n\t"

               :: "v"((threadIdx.x & 7) * 16), "s"(__builtin_amdgcn_kernarg_segment_ptr()), "v"((threadIdx.x + ((blockIdx.x >> 3) & 15) * 10 - 502) * 128), "v"(threadIdx.x)
               : "memory", "vcc", "v255", "s100", "s101");
  const int wid = __builtin_amdgcn_readfirstlane(threadIdx.x >> 6);
  const int bid = blockIdx.x, xcd = bid & 7, bi = bid >> 3;
  const int tb = (xcd >> 1) * 8 + (bi >> 2);
  const int hg = ((xcd & 1) ^ HX) * 4 + ((bi + HP) & 3);
  const unsigned rot = (2 * ((bi >> 2) + bi)) & 6;
  const int t0 = tb * 64;
#define ARG(i) ((unsigned long long)a##i)
#define LBAR2 "\n\ts_barrier"
#define LBAR ""
#define ARGS(wt) \
  u8* ws; unsigned long long pout; \
  asm volatile(wt "s_mov_b64 %0, s[92:93]\n\ts_mov_b64 %1, s[94:95]" LBAR : "=s"(ws), "=s"(pout) :: "memory"); \
  const __amdgpu_buffer_rsrc_t rws = __builtin_amdgcn_make_buffer_rsrc(ws, 0, WS_END, 0x00020000); \
  const u32x4 rw4 = mkrsrc((unsigned long long)ws, WS_END);
  unsigned T;
  const int w = wid & 3;
  const unsigned ones = 0x3F803F80u;
  f32x16 zero16;
#pragma unroll
  for (int i = 0; i < 16; ++i) zero16[i] = 0.f;

  if (wid >= 4) {
    LANE_VARS
    const unsigned fn = (bid & 3) * 4 + w, fk = (bid >> 2) & 31;
    const bool hi = (lane & 16) != 0, on = (lane & 27) == 16;
    unsigned vf = (lane >> 2) * 1024 + (lane & 3) * 16;
    {
      const unsigned aV0 = hi ? (on ? L_KV + 1024u : L_KV + 1040u) : aK, aV1 = hi ? aV0 - 256u : aK;
      asm volatile(REP(20, "v_mov_b64 v[152+2*i_:153+2*i_], 0\n\t") REP(2, "v_mov_b64 v[212+2*i_:213+2*i_], 0\n\t") REP(2, "v_mov_b64 v[240+2*i_:241+2*i_], -1\n\t")
                   "v_mov_b32 v232, 0xdddddddd\n\tv_mov_b32 v233, %1\n\tv_mov_b32 v234, %2\n\tv_mov_b32 v235, %3\n\t"
                   "v_mov_b32 v236, 0\n\tv_mov_b32 v237, %4\n\tv_mov_b32 v238, %5\n\tv_mov_b32 v239, %6\n\t"
                   "v_add_u32 v245, %7, %6\n\tv_mov_b32 v246, %8\n\tv_lshlrev_b32 v247, 2, v246\n\tv_mov_b32 v248, %9\n\t"
                   : "+v"(vf) : "v"(aK), "v"(aV0), "v"(aV1), "v"(L_G + 256 + (w >> 1) * 128 + r32 * 4), "s"(C_LP + w * 4), "v"(l16),
                     "s"(L_GB + w * 4096), "s"(1u << ((w >> 1) + (w & 1) * 8)), "s"(65536u << w) : "memory");
    }
    const u32x4 rwo = mkrsrc(bid < 128 ? ARG(4) : ARG(5), 512u * 1024u);
    asm volatile("s_waitcnt lgkmcnt(0)\n\ts_barrier" ::: "memory");
    asm volatile("s_mov_b32 m0, %0\n\ts_nop 0\n\tbuffer_load_dwordx4 %1, %2, %3 offen " NTF " lds"
                 :: "s"(L_TW + w * 1024), "v"(vf), "s"(rwo), "s"(fk * 16384 + fn * 64) : "memory");
    ARGS("s_waitcnt lgkmcnt(0)\n\t")
#define CLEARS() asm volatile(REP(4, "buffer_store_dwordx4 v[240:243], v239, %0, %1 offen offset:i_*1024 sc1\n\t") "buffer_store_dwordx4 v[240:243], v239, %0, %2 offen sc1" \
                 :: "s"(rw4), "s"(WS_O + (w >> 1) * 0x200000 + ((tb * 2 + (w & 1)) * 32 + hg * 4) * 1024), "s"(WS_HL + (tb * 32 + hg * 4 + w) * 1024) : "memory")
    TS(1);
    VMW(CLR == 0 ? 5 : 0);
    {
      const unsigned ao = L_TW + w * 1024 + (h * 4 + (r16 >> 2)) * 64 + (lane & 1) * 32 + ((lane >> 1) & 1) * 8 + gs * 16;
      ST128(__builtin_bit_cast(u32x4, cat8(tr_read(ao), tr_read(ao + 512))), l16, WS_WF + (((bid >> 7) * 16 + fn) * 32 + fk) * 1024);
    }
    TS(2);
    u32x4 aQ, aQ1;
    {
      u32x16 c0, c1, c2, c3, c4;
      const unsigned xb = L_X(0) + ax + (w >> 1) * 4096;
      unsigned x0, x1, x2, x3, xs = rot << 13, vw, qt, bt = 2u, cn = 0u, wo = 81920u, spin = 4000000u;
#define QPOLL "s_bitcmp1_b32 %[cn], %[bt]\n\ts_cbranch_scc1 3f\n\t" \
      "2:\n\tds_read_b32 %[q0], v236 offset:" STR(C_Q) "\n\ts_waitcnt lgkmcnt(0)\n\tv_readfirstlane_b32 %[cn], %[q0]\n\t" \
      "s_bitcmp1_b32 %[cn], %[bt]\n\ts_cbranch_scc1 3f\n\ts_sub_u32 %[spin], %[spin], 1\n\ts_cbranch_scc0 2b\n\ts_mov_b32 %[spin], 0\n\t" \
      "3:\n\ts_add_u32 %[bt], %[bt], 4\n\t"
#define QRD1(f, i, o) "ds_read_b64_tr_b16 v[" f "+" #i "*4:" f "+" #i "*4+1], %[vw] offset:" o "+" #i "*2048\n\t" \
      "ds_read_b64_tr_b16 v[" f "+" #i "*4+2:" f "+" #i "*4+3], %[vw] offset:" o "+" #i "*2048+512\n\t" \
      "ds_read_b128 v[" f "+16+" #i "*4:" f "+16+" #i "*4+3], %[x" #i "] offset:" o "\n\t"
#define QRD(f, o) QRD1(f, 0, o) QRD1(f, 1, o) QRD1(f, 2, o) QRD1(f, 3, o)
#define QMM(f, n) REP(4, "s_waitcnt lgkmcnt(" n ")\n\tv_mfma_f32_32x32x16_bf16 v[48:63], v[" f "+4*i_:" f "+4*i_+3], v[" f "+16+4*i_:" f "+16+4*i_+3], v[48:63]\n\t")
      asm volatile(
        REP(8, "v_mov_b64 v[48+2*i_:49+2*i_], 0\n\t")
        "1:\n\t"
        "v_add_u32 %[vw], %[wo], %[aw]\n\t"
        "v_add_u32 %[x0], %[xs], %[b0]\n\tv_add_u32 %[x1], %[xs], %[b1]\n\tv_add_u32 %[x2], %[xs], %[b2]\n\tv_add_u32 %[x3], %[xs], %[b3]\n\t"
        QPOLL QRD("64", "0")
        "s_cmp_eq_u32 %[bt], 6\n\ts_cbranch_scc1 4f\n\t"
        QMM("96", "12")
        "4:\n\t"
        QPOLL QRD("96", "8192") QMM("64", "12")
        "s_add_u32 %[xs], %[xs], 0x4000\n\ts_and_b32 %[xs], %[xs], 0xffff\n\t"
        "s_add_u32 %[wo], %[wo], 0x4000\n\ts_and_b32 %[wo], %[wo], 0x1ffff\n\t"
        "s_cmp_lg_u32 %[bt], 34\n\ts_cbranch_scc1 1b\n\t"
        QMM("96", "9-3*i_")
        "s_nop 11\n\t"
        REP(8, "v_cvt_pk_bf16_f32 v[40+i_], v[48+2*i_], v[49+2*i_]\n\t")
        : "=&{v[40:43]}"(aQ), "=&{v[44:47]}"(aQ1), "=&{v[48:63]}"(c4), "=&{v[64:79]}"(c0), "=&{v[80:95]}"(c1), "=&{v[96:111]}"(c2), "=&{v[112:127]}"(c3),
          [x0] "=&v"(x0), [x1] "=&v"(x1), [x2] "=&v"(x2), [x3] "=&v"(x3), [xs] "+s"(xs), [vw] "=&v"(vw), [q0] "=&v"(qt), [bt] "+s"(bt), [cn] "+s"(cn), [wo] "+s"(wo), [spin] "+s"(spin)
        : [aw] "v"(32768u + (awf ^ ((w & 1) * 64))), [b0] "v"(xb), [b1] "v"(xb ^ 32u), [b2] "v"(xb ^ 64u), [b3] "v"(xb ^ 96u)
        : "memory", "scc");
    }
    TS(6);
    const unsigned so = WS_O + (2u << 20) + ((tb * 2 + (w >> 1)) * 32 + hg * 4 + (w & 1) * 2) * 1024;
    const unsigned sb = WS_O + 0x200000 + ((tb * 2 + (hg >> 2)) * 32 + w * 4) * 1024;
    unsigned long long bad0, bad1;
    {
#define LFIN(d, o, bad, c) \
      "v_rcp_f32 v240, v[" d "+8]\n\t" \
      "v_maximum3_f32 v241, |v[" d "]|, |v[" d "+1]|, |v[" d "+2]|\n\t" \
      "v_maximum3_f32 v242, |v[" d "+3]|, |v[" d "+4]|, |v[" d "+5]|\n\t" \
      "v_maximum3_f32 v241, v241, |v[" d "+6]|, |v[" d "+7]|\n\t" \
      "v_mul_f32 v240, v240, v243\n\t" \
      "v_maximum3_f32 v241, v241, v242, v[" d "+8]\n\t" \
      "v_cmp_nle_f32 vcc, %[lo], v[" d "+8]\n\t" \
      "v_cmp_ngt_f32 " bad ", %[hi], v241\n\t" \
      REP(8, "v_mul_f32 v[" d "+i_], v[" d "+i_], v240\n\t") \
      "s_or_b64 " bad ", " bad ", vcc\n\t" \
      REP(4, "v_cvt_pk_bf16_f32 v[" o "+i_], v[" d "+2*i_], v[" d "+2*i_+1]\n\t") \
      "s_cbranch_scc1 5f\n\t" \
      "buffer_store_dwordx4 v[" o ":" o "+3], v239, %[rs], %[so] offen offset:" c " sc1\n\t" \
      "5:\n\t"
      unsigned tgt = 0u, spin = 4000000u, tmp;
      asm volatile(
        "s_waitcnt vmcnt(0) lgkmcnt(0)\n\ts_setprio 1\n\ts_barrier\n\t"
        TSA(7)
        RDL("192:195", "v233", "0", "0") RDL("196:199", "v233", "1", "0") RDL("200:203", "v234", "0", "1024") RDL("204:207", "v235", "0", "1280") RDL("208:211", "v234", "1", "1024")
        "s_waitcnt lgkmcnt(4)\n\t"
        QKM("128:143", "192:195", "%[q0]")
        "s_nop 11\n\t"
        "1:\n\t"
        DUALX(0, LW1, "ds_write_b32 v238, v233\n\t") DUALY(1, LW1, "ds_read_b32 v243, v237\n\t") DUALX(2, LW1, "s_add_u32 %[tgt], %[tgt], 4\n\t") DUALY(3, LW1, "ds_read_b32 v244, v236\n\t")
        TSG(6, "%[tgt]", 1)
        LW1
        "3:\n\t"
        "v_readfirstlane_b32 %[tmp], v244\n\t"
        "s_cmp_ge_u32 %[tmp], %[tgt]\n\t"
        "s_cbranch_scc0 6f\n\t"
        "2:\n\t"
        DUALX(4, "", "") DUALY(5, LW1, "") DUALX(6, LW1, "") DUALY(7, LW1, "") DUALX(8, LW1, "") DUALY(9, LW1, "") DUALX(10, LW1, "") DUALY(11, LW1, "")
        "v_add_u32 v233, 0x6180, v233\n\t"
        "v_add_u32 v234, 0x6180, v234\n\t"
        "v_add_u32 v235, 0x6180, v235\n\t"
        "s_branch 1b\n\t"
        "7:\n\t"
        "ds_read_b32 v244, v236\n\t"
        "s_waitcnt lgkmcnt(0)\n\t"
        TSG(7, "%[tgt]", 1)
        "s_sub_u32 %[spin], %[spin], 1\n\t"
        "s_cbranch_scc0 3b\n\t"
        "s_mov_b32 %[spin], 0\n\t"
        "s_branch 2b\n\t"
        "6:\n\t"
        "s_cmp_eq_u32 %[tgt], 24\n\t"
        "s_cbranch_scc0 7b\n\t"
        PVM("176:191", "212:215", "152:159")
#define G0REQ(p) ".if " STR(E1P) "==" p " || " STR(E0P) "==" p "\n\t.if " STR(E1P) "!=" STR(E0P) "\n\ts_bitcmp1_b32 %[sb], 15\n\t.if " STR(E1P) "==" p "\n\ts_cbranch_scc0 1f\n\t.else\n\ts_cbranch_scc1 1f\n\t.endif\n\t.endif\n\t" \
        REP(4, "buffer_load_dwordx4 v[128+4*i_:128+4*i_+3], v239, %[rs], %[sb] offen offset:i_*1024 sc1\n\t") "1:\n\t.endif\n\t"
        G0REQ("0")
#define AGO "s_mov_b64 exec, 1\n\tds_or_b32 v236, v248 offset:" STR(C_RDY) "\n\ts_mov_b64 exec, -1\n\t"
        LFIN("160", "216", "%[b0]", "0")
        LFIN("176", "224", "%[b1]", "1024")
        G0REQ("1")
        TSA(20)
        "s_barrier\n\t"
        : [tgt] "+s"(tgt), [spin] "+s"(spin), [tmp] "=&s"(tmp), [b0] "=&s"(bad0), [b1] "=&s"(bad1) TSOP
        : [q0] "v"(aQ), [q1] "v"(aQ1), [lo] "s"(5.4e-20f), [hi] "s"(1.0e36f), [rs] "s"(rw4), [so] "s"(so), [sb] "s"(sb) : "memory", "scc", "vcc");
    }
    if (__builtin_expect((bad0 | bad1) != 0ull, 0)) {
      const int lane = lane_id() & 63;
      const unsigned l16 = lane * 16;
      const bool hi = (lane & 16) != 0, on = (lane & 27) == 16;
#pragma nounroll
      for (int qi = 0; qi < 2; ++qi) {
        if ((qi ? bad1 : bad0) == 0ull) continue;
        const bf16x8 qf = __builtin_bit_cast(bf16x8, qi ? aQ1 : aQ);
        float m = -3.0e38f;
#pragma nounroll
        for (int t = 0; t < 64; ++t) {
          const u32x4 kf = __builtin_amdgcn_raw_buffer_load_b128(rws, l16, WS_KV + t * 2048, SC1);
          f32x16 s = __builtin_amdgcn_mfma_f32_32x32x16_bf16(__builtin_bit_cast(bf16x8, kf), qf, zero16, 0, 0, 0);
#pragma unroll
          for (int i = 0; i < 16; ++i) m = fmaxf(m, s[i]);
        }
        { auto r = __builtin_amdgcn_permlane32_swap(__builtin_bit_cast(unsigned, m), __builtin_bit_cast(unsigned, m), false, false);
          m = fmaxf(swap_lo(r[0]), swap_lo(r[1])); }
        f32x16 F = zero16;
        const unsigned pad = on ? ones : 0u;
#pragma nounroll
        for (int t = 0; t < 64; ++t) {
          const u32x4 kf = __builtin_amdgcn_raw_buffer_load_b128(rws, l16, WS_KV + t * 2048, SC1);
          u32x4 v0 = __builtin_amdgcn_raw_buffer_load_b128(rws, (l16 & ~256u) + 1024, WS_KV + t * 2048, SC1);
          u32x4 v1 = __builtin_amdgcn_raw_buffer_load_b128(rws, (l16 & ~256u) + 1280, WS_KV + t * 2048, SC1);
#pragma unroll
          for (int i = 0; i < 4; ++i) { v0[i] = hi ? pad : v0[i]; v1[i] = hi ? pad : v1[i]; }
          f32x16 s = __builtin_amdgcn_mfma_f32_32x32x16_bf16(__builtin_bit_cast(bf16x8, kf), qf, zero16, 0, 0, 0);
          f32x8 p0, p1;
#pragma unroll
          for (int i = 0; i < 8; ++i) { p0[i] = __builtin_amdgcn_exp2f(s[i] - m); p1[i] = __builtin_amdgcn_exp2f(s[8 + i] - m); }
          F = __builtin_amdgcn_smfmac_f32_32x32x32_bf16(__builtin_bit_cast(bf16x8, v0), __builtin_bit_cast(bf16x16, p0), F, 0xDDDD, 0, 0);
          F = __builtin_amdgcn_smfmac_f32_32x32x32_bf16(__builtin_bit_cast(bf16x8, v1), __builtin_bit_cast(bf16x16, p1), F, 0xDDDD, 0, 0);
        }
        const float iv = __builtin_amdgcn_rcpf(F[8]) * lds_ld<float>(L_G + 256 + (w >> 1) * 128 + (lane & 31) * 4);
        ST128(((u32x4){pk_bf16(F[0] * iv, F[1] * iv), pk_bf16(F[2] * iv, F[3] * iv), pk_bf16(F[4] * iv, F[5] * iv), pk_bf16(F[6] * iv, F[7] * iv)}), l16, so + qi * 1024);
      }
    }
    {
#define TCHK(b, r, o, aw, ar, bit, tv) \
      "s_bitcmp1_b32 %[pend], " b "\n\ts_cbranch_scc0 1f\n\t" \
      FULLT(r, tv) "s_cbranch_vccnz 1f\n\t" \
      REP(4, "ds_write_b128 " aw ", v[" r "+4*i_:" r "+4*i_+3] offset:" o "+i_*1024\n\t") \
      "s_mov_b64 exec, 1\n\tds_or_b32 " ar ", " bit "\n\ts_mov_b64 exec, -1\n\t" \
      "s_bitset0_b32 %[pend], " b "\n\t1:\n\t"
      unsigned sq, spin = 4000000u, pend = 3u;
      asm volatile(
        "s_setprio " STR(P_LF) "\n\t"
        G0REQ("2")
        "s_bitcmp1_b32 %[sb], 15\n\ts_cbranch_scc1 1f\n\ts_sleep " STR(LS0) "\n\t1:\n\t"
        "s_sleep " STR(LSL) "\n\ts_mov_b32 %[spin], 400000\n\ts_add_u32 %[so], %[sb], 0x4000\n\t"
        TSA(21)
        G0REQ("3")
        "s_branch 12f\n\t"
        "11:\n\t"
        "s_bitcmp1_b32 %[pend], 0\n\ts_cbranch_scc0 13f\n\t"
        "s_cmp_eq_u32 %[pend], 3\n\ts_cbranch_scc1 1f\n\ts_waitcnt vmcnt(0)\n\t1:\n\ts_waitcnt vmcnt(4)\n\t"
        TCHK("0", "128", "0", "v245", "v236", "v246 offset:" STR(C_RDY) "+4", "v160")
        TSA(19)
        "s_bitcmp1_b32 %[pend], 0\n\ts_cbranch_scc0 13f\n\t"
        REP(4, "buffer_load_dwordx4 v[128+4*i_:128+4*i_+3], v239, %[rs], %[sb] offen offset:i_*1024 sc1\n\t")
        "13:\n\t"
        "s_bitcmp1_b32 %[pend], 1\n\ts_cbranch_scc0 14f\n\t"
        "s_cmp_eq_u32 %[pend], 3\n\ts_cbranch_scc1 1f\n\ts_waitcnt vmcnt(0)\n\t1:\n\ts_waitcnt vmcnt(4)\n\t"
        TCHK("1", "144", "16384", "v245", "v236", "v247 offset:" STR(C_RDY) "+4", "v160")
        "s_bitcmp1_b32 %[pend], 1\n\ts_cbranch_scc0 14f\n\t"
        "12:\n\t"
        REP(4, "buffer_load_dwordx4 v[144+4*i_:144+4*i_+3], v239, %[rs], %[so] offen offset:i_*1024 sc1\n\t")
        "14:\n\t"
        "s_cmp_eq_u32 %[pend], 0\n\ts_cbranch_scc1 15f\n\t"
        "s_sub_u32 %[spin], %[spin], 1\n\ts_cbranch_scc0 11b\n\t"
        "15:\n\ts_waitcnt lgkmcnt(0)\n\t"
        ".Lfused_b:\n\t" ENDP
        : [so] "=&s"(sq), [spin] "+s"(spin), [pend] "+s"(pend) TSOP
        : [rs] "s"(rw4), [sb] "s"(sb)
        : "memory", "scc", "vcc");
    }
    TS(25);
    TSDUMP();
    return;
  }

  LANE_VARS
  const int sp = (bi >> 1) & 1, isV = bi & 1;
  const int jg = ((MAP ? bi >> 2 : xcd) & 1) * 2 + sp, xg = (jg + w) & 3;
  unsigned vx0 = (unsigned)(t0 + xg * 16 + (lane >> 3)) * 1024u + (unsigned)(((lane & 7) ^ g4) * 16), vx1 = (vx0 ^ 64u) + 7168u;
  const unsigned aj = L_X(0) + (xg * 16 + r16) * 128 + ((g4 ^ (r16 >> 1)) * 16);
  const unsigned n0 = (w * 5 - 4) >> 1;
  unsigned vw0 = (n0 * 8 + (lane >> 3)) * 1024u + vsw, vw1 = vw0 + 7168u, vw2 = vw0 + 14336u;
  PIN(vx0); PIN(vx1); PIN(vw0); PIN(vw1); PIN(vw2);
  const u32x4 rx = mkrsrc(ARG(0), 2048u * 1024u), rw = mkrsrc(ARG(1), 512u * 1024u);
  unsigned c = rot;
#define QREQ(nt) "s_and_b32 %[c], %[c], 7\n\ts_add_u32 m0, %[mw], (81920+j_*8192)&131071\n\ts_lshl_b32 %[so], %[c], 16\n\t" \
        "buffer_load_dwordx4 %[w0], %[rw], %[so] offen " nt " lds\n\tbuffer_load_dwordx4 %[w1], %[rw], %[so] offen offset:1024 " nt " lds\n\t" \
        "buffer_load_dwordx4 %[w2], %[rw], %[so] offen offset:2048 " nt " lds\n\t" \
        "s_lshr_b32 %[so], %[so], 3\n\ts_add_u32 m0, %[mx], %[so]\n\ts_lshl_b32 %[so], %[c], 7\n\ts_add_u32 %[c], %[c], 1\n\t" \
        "buffer_load_dwordx4 %[x0], %[rx], %[so] offen " nt " lds\n\tbuffer_load_dwordx4 %[x1], %[rx], %[so] offen offset:1024 " nt " lds\n\t"
#define QOPS [mw] "s"(32768u + n0 * 1024u), [mx] "s"(L_X(0) + xg * 2048), [w0] "v"(vw0), [w1] "v"(vw1), [w2] "v"(vw2), [rw] "s"(rw), [x0] "v"(vx0), [x1] "v"(vx1), [rx] "s"(rx)
  if (w == 0) {
    const u32x4 rkv = mkrsrc(isV ? ARG(3) : ARG(2), 16384u), rex = mkrsrc(ARG(6), 1024u);
#pragma nounroll
    for (unsigned j = 0; j < 8; ++j) {
      DMA2(L_EW + j * 2048, l16, l16, rkv, j << 11);
      DMA2(L_X(j) + xg * 2048, vx0, vx1, rx, j << 7);
    }
    asm volatile("s_mov_b32 m0, %0\n\ts_nop 0\n\tbuffer_load_dwordx4 %1, %2, 0 offen " NT0 " lds" :: "s"(L_GW), "v"(l16), "s"(rex) : "memory");
    TS(30);
    asm volatile("s_waitcnt lgkmcnt(0)\n\ts_load_dword s100, s[92:93], 0x800" LBAR2 ::: "memory", "s100");
  }
  unsigned vz = 0u, v1 = 1u;
  const unsigned cst = (lane & 1) ? 0u : ones;
  u32x4 cst4 = {cst, cst, cst, cst};
  const unsigned acs = L_KV + (lane >> 1) * KVS + 2048 + (lane & 1) * 16;
  unsigned am = aK + (w ? 2 + 4 * w : 0) * KVS, amc = acs + (w ? 2 + 4 * w : 0) * KVS;
  unsigned rb = (unsigned)w * 16384u;
  asm volatile("s_add_u32 %0, %0, %1" : "+s"(rb) : "n"(L_RING - 16384) : "scc");
  unsigned va0 = (unsigned)(lane >> 3) * 3072u + (unsigned)(w - 1) * 1024u + vsw, va1 = va0 + 23552u, va2 = va1 + 23552u, va3 = va2 + 23552u;
  unsigned aW0 = rb + awf, aW1 = rb + (awf ^ 64u), ax0 = L_X(0) + ax, ax1 = L_X(0) + (ax ^ 32u), ax2 = L_X(0) + (ax ^ 64u), ax3 = L_X(0) + (ax ^ 96u);
  unsigned ap = L_PL + (w == 1 ? 5120 : w * 2560 - 4608) + r32 * 32 + h * 16, apg = C_PG + (w - 1) * 4;
  unsigned ah = L_PL + h * 2560 + r32 * 16, l4 = lane * 4, lak = L_PL + w * 512 + r16 * 64 + g4 * 8, laq = L_PL + 5120 + w * 512 + r16 * 32 + g4 * 8;
  unsigned lav = L_PL + 2560 + w * 512 + g4 * 256 + (r16 >> 2) * 64 + (r16 & 3) * 8, lag = L_G + w * 64 + r16 * 4, lvo = gs * 8 + (h * 32 + r16) * 16;
  f32x4 mk;
#pragma unroll
  for (int r = 0; r < 4; ++r) mk[r] = ((unsigned)(4 * g4 + r - (r16 >> 1) - 1) < 8u) ? ((tb | w | h) ? 0.f : -4.0e9f) : -3.0e38f;
  unsigned v4 = 4u;
  PIN(ah); PIN(lak); PIN(laq); PIN(lav); PIN(lag); PIN(lvo); PIN(mk); PIN(v4); PIN(l4);
  asm volatile(REP(8, "v_mov_b64 v[224+2*i_:225+2*i_], 0\n\t") REP(2, "v_mov_b64 v[240+2*i_:241+2*i_], -1\n\t") ::: "memory");
  PIN(vz); PIN(v1); PIN(cst4); PIN(am); PIN(amc); PIN(va0); PIN(va1); PIN(va2); PIN(va3); PIN(aW0); PIN(aW1); PIN(ax0); PIN(ax1); PIN(ax2); PIN(ax3); PIN(ap); PIN(apg);
  const unsigned sm = WS_KT + 8192 + w * 16384;
  unsigned vT;
  unsigned sa = 0u, arw = rb + l16, apf = ((bi >> (MAP ? 3 : 2)) * PFN + lane) * 3072u + (unsigned)(w - 1) * 1024u + hg * 128;
  PIN(arw); PIN(apf);
#define GREG() asm volatile(REP(4, \
      "buffer_load_dwordx4 v[192+16*i_:195+16*i_], %[a0], s[88:91], %[so] offen\n\tbuffer_load_dwordx4 v[196+16*i_:199+16*i_], %[a1], s[88:91], %[so] offen offset:1024\n\t" \
      "buffer_load_dwordx4 v[200+16*i_:203+16*i_], %[a2], s[88:91], %[so] offen offset:2048\n\tbuffer_load_dwordx4 v[204+16*i_:207+16*i_], %[a3], s[88:91], %[so] offen offset:3072\n\t" \
      "s_add_u32 %[so], %[so], 98304\n\t") \
      : [so] "+s"(sa) : [a0] "v"(va0), [a1] "v"(va1), [a2] "v"(va2), [a3] "v"(va3) : "memory", "scc")
  if (w != 0) {
    unsigned so;
    asm volatile("s_waitcnt lgkmcnt(0)\n\ts_barrier" ::: "memory");
    asm volatile(REPX("j_", QD, QREQ(NT1)) : [c] "+s"(c), [so] "=&s"(so) : QOPS : "memory", "scc");
    TS(30);
    asm volatile("s_waitcnt lgkmcnt(0)\n\ts_load_dword s100, s[92:93], 0x800\n\ts_mov_b32 s90, 0x180000\n\ts_mov_b32 s91, 0x20000" ::: "memory", "s100");
  }
  ARGS("")
  if (w == 0) {
    GETT();
    const unsigned ak = L_EW + (g4 * 8 + (r16 >> 2)) * 32 + (lane & 3) * 8;
    const int slot = (bi >> 2) * 8 + xcd;
    const int u = isV ? gs : h, fl = (isV ? h : gs) * 32 + sp * 16 + r16;
    unsigned at = slot < 18 ? u * 1024 + fl * 16 + WS_KT + slot * 4096 + isV * 2048 : WS_END, au = fl * 16 + u * 8 + WS_KV + slot * 2048 + isV * 1024;
    float sc = isV ? 1.0f : 0.25f * LOG2E;
    vT = T;
    unsigned qp = __builtin_rotateright32(0x1111u, 4u * rot);
    PIN(at); PIN(au); PIN(sc); PIN(vT); PIN(qp);
    f32x4 pa = {0.f, 0.f, 0.f, 0.f}, pb = pa;
#define KVM(a, b) REP(4, ".if i_>0\n\ts_waitcnt lgkmcnt(21-6*i_)\n\t.endif\n\tv_mfma_f32_16x16x32_bf16 %[pa], v[" a "+8*i_:" a "+8*i_+3], v[" b "+8*i_:" b "+8*i_+3], %[pa]\n\t" \
                         ".if i_>0\n\ts_waitcnt lgkmcnt(18-6*i_)\n\t.endif\n\tv_mfma_f32_16x16x32_bf16 %[pb], v[" a "+8*i_+4:" a "+8*i_+7], v[" b "+8*i_+4:" b "+8*i_+7], %[pb]\n\t")
    asm volatile(
      REPV("j_", 2,
        "s_waitcnt vmcnt(17-16*j_)\n\ts_mov_b64 exec, 1\n\tds_add_u32 %[z], %[qp] offset:" STR(C_Q) "\n\tv_xor_b32 %[qp], 0x11111111, %[qp]\n\ts_mov_b64 exec, -1\n\t"
        REP(4, "ds_read_b64_tr_b16 v[192+8*i_:192+8*i_+1], %[ak] offset:j_*8192+i_*2048\n\tds_read_b64_tr_b16 v[192+8*i_+2:192+8*i_+3], %[ak] offset:j_*8192+i_*2048+128\n\t"
               "ds_read_b128 v[128+32*j_+8*i_:128+32*j_+8*i_+3], %[a0] offset:j_*32768+i_*8192\n\t"
               "ds_read_b64_tr_b16 v[192+8*i_+4:192+8*i_+5], %[ak] offset:j_*8192+i_*2048+1024\n\tds_read_b64_tr_b16 v[192+8*i_+6:192+8*i_+7], %[ak] offset:j_*8192+i_*2048+1152\n\t"
               "ds_read_b128 v[128+32*j_+8*i_+4:128+32*j_+8*i_+7], %[a1] offset:j_*32768+i_*8192\n\t")
        "s_waitcnt lgkmcnt(15)\n\ts_cmp_lg_u32 %[v], 0\n\ts_cbranch_scc1 1f\n\t"
        KVM("192", "128+32*j_") "s_branch 2f\n\t1:\n\t" KVM("128+32*j_", "192") "2:\n\t")
      "s_waitcnt vmcnt(0)\n\t"
      REP(8, "ds_read_b128 v[192+4*i_:192+4*i_+3], %[ag] offset:" STR(L_GW) "+i_*64\n\t") "s_nop 1\n\t"
      : [pa] "+v"(pa), [pb] "+v"(pb), [qp] "+v"(qp) : [z] "v"(vz), [ak] "v"(ak), [a0] "v"(aj), [a1] "v"(aj ^ 64), [v] "s"(isV), [ag] "v"(g4 * 16) : "memory", "scc");
    const f32x4 p = (pa + pb) * sc;
    u32x2 o; o[0] = pk_bf16(p[0], p[1]); o[1] = pk_bf16(p[2], p[3]);
    ST128(((u32x4){o[0], vT, o[1], vT}), at, 0);
    __builtin_amdgcn_raw_buffer_store_b64(o, rws, au, 0, SC1);
    TS(2);
    f32x4 gz = {0.f, 0.f, 0.f, 0.f};
    asm volatile("s_waitcnt lgkmcnt(0)\n\ts_nop 1\n\t"
      REP(8, "v_mfma_f32_16x16x32_bf16 %[gz], v[192+4*i_:192+4*i_+3], v[128+4*i_:128+4*i_+3], %[gz]\n\t"
             "ds_read_b128 v[192+4*i_:192+4*i_+3], %[ag] offset:" STR(L_GW) "+512+i_*64\n\t")
      REP(8, "s_waitcnt lgkmcnt(7-i_)\n\tv_mfma_f32_16x16x32_bf16 %[gz], v[192+4*i_:192+4*i_+3], v[160+4*i_:160+4*i_+3], %[gz]\n\t")
      "s_nop 7\n\t"
      : [gz] "+v"(gz) : [ag] "v"(g4 * 16) : "memory");
    const float g = __builtin_amdgcn_rcpf(1.f + __builtin_amdgcn_exp2f(-gz[0] * LOG2E));
    TS(3);
    unsigned so = WS_KT;
    asm volatile("s_waitcnt vmcnt(0)\n\ts_mov_b64 exec, 1\n\tbuffer_store_dword %[vT], off, %[rs], %[fo] sc1\n\t"
                 "s_mov_b64 exec, %[gm]\n\tbuffer_store_dword %[g], %[l4], %[rs], %[go] offen sc1\n\ts_mov_b64 exec, -1\n\t"
                 TSA(27)
                 GLDT("128", 24, "%[va]", "%[rs]", "%[so]") "s_waitcnt vmcnt(0)\n\t"
                 : [so] "+s"(so) TSOP
                 : [vT] "v"(vT), [rs] "s"(rw4), [fo] "s"(bid * 4), [g] "v"(g), [l4] "v"(l4), [go] "s"(WS_G + (t0 + jg * 16) * 4), [gm] "s"(0xffull << (8 * isV)), [va] "v"(l16) : "memory", "scc");
    TS(4);
    TAGGET(128, 12, 24, WS_KT);
    asm volatile("buffer_load_dwordx4 v[244:247], %0, %1, 0 offen sc1" :: "v"(l16), "s"(rw4) : "memory");
    KVPUT(128, 8, 12, am, amc);
    POST(C_KV, vz, v1);
  } else {
    unsigned inc = 1u, so;
    asm volatile(
      ".set j_, " STR(QD) "\n\t.rept 9\n\t"
        ".if j_<8\n\t" QREQ(NT2) ".set n_, 5*" STR(QD) "\n\t"
        ".else\n\t.set n_, 5*(7+" STR(QD) "-j_)\n\t.endif\n\t"
        ".if j_==" STR(PFJ) " && " STR(PFN) "\n\ts_mov_b64 exec, %[pm]\n\tbuffer_load_dword v254, %[pf], s[88:91], 0 offen\n\ts_mov_b64 exec, -1\n\t.endif\n\t"
        ".if j_>=" STR(PFJ) " && j_<" STR(PFJ) "+" STR(QD) "+1 && " STR(PFN) "\n\t.set n_, n_+1\n\t.endif\n\t"
        ".if j_==7\n\t" TSA(29) ".endif\n\t"
        ".if j_==" STR(MJ) "+" STR(QD) "\n\ts_mov_b32 %[so], %[sm]\n\t" TSA(27) GLDT("128", 16, "%[va]", "%[rs]", "%[so]") ".endif\n\t"
        ".if j_>=" STR(MJ) "+" STR(QD) "\n\t.set n_, n_+16\n\t.endif\n\t"
        ".if j_<8+" STR(QD) "\n\t"
        "s_waitcnt vmcnt(n_)\n\ts_mov_b64 exec, 1\n\tds_add_u32 %[z], %[inc] offset:" STR(C_Q) "\n\ts_mov_b64 exec, -1\n\tv_lshlrev_b32 %[inc], 4, %[inc]\n\t"
        ".endif\n\t"
        ".if j_==" STR(QD) "\n\t" TSA(2) ".endif\n\t"
        ".if j_==" STR(QD) "+3\n\t" TSA(28) ".endif\n\t"
      ".set j_, j_+1\n\t.endr\n\t"
      : [c] "+s"(c), [so] "=&s"(so), [inc] "+v"(inc) TSOP
      : QOPS, [z] "v"(vz), [sm] "s"(sm), [va] "v"(l16), [rs] "s"(rw4), [pf] "v"(apf), [pm] "s"(~0ull >> ((64 - PFN) & 63))
      : "memory", "scc");
    TS(3);
    GETT();
    VMW(GP == 2 ? 16 : 0);
    TS(4);
    TAGGET(128, 8, 16, sm);
    GREG();
  }
  TS(5);
#define GDMA(q) "s_add_u32 m0, %[rb], " q "*4096\n\ts_nop 0\n\t" \
      "buffer_load_dwordx4 %[a0], s[88:91], %[so] offen " NTG " lds\n\tbuffer_load_dwordx4 %[a1], s[88:91], %[so] offen offset:1024 " NTG " lds\n\t" \
      "buffer_load_dwordx4 %[a2], s[88:91], %[so] offen offset:2048 " NTG " lds\n\tbuffer_load_dwordx4 %[a3], s[88:91], %[so] offen offset:3072 " NTG " lds\n\t" \
      "s_add_u32 %[so], %[so], 98304\n\t"
  RAW_BARRIER();
  TS(7);
  const int ct = tb * 2 + (hg >> 2);
  const int nt = (hg & 3) * 4 + w;
  const unsigned sh = WS_HL + (tb * 32 + hg * 4) * 1024;
  if (w == 0) {
#define FLAGS(fo, n) do { unsigned spin = 200000u; \
      asm volatile("s_waitcnt vmcnt(%[n_])\n\t1:\n\t" \
                   "v_bitop3_b32 v244, v244, v245, %[T] bitop3:0x7e\n\tv_bitop3_b32 v246, v246, v247, %[T] bitop3:0x7e\n\t" \
                   "v_or_b32 v244, v244, v246\n\tv_cmp_ne_u32 vcc, 0, v244\n\ts_cbranch_vccz 2f\n\t" \
                   "buffer_load_dwordx4 v[244:247], %[va], %[rs], 0 offen offset:" #fo " sc1\n\ts_waitcnt vmcnt(0)\n\t" \
                   "s_sub_u32 %[sp], %[sp], 1\n\ts_cbranch_scc0 1b\n\t2:\n\t" \
                   : [sp] "+s"(spin) : [va] "v"(l16), [rs] "s"(rw4), [T] "s"(T), [n_] "n"(n) : "memory", "scc", "vcc"); } while (0)
#define FLAG2() asm volatile("s_mov_b64 exec, 1\n\tbuffer_store_dword %0, off, %1, %2 offset:1024 sc1\n\ts_mov_b64 exec, -1" :: "v"(vT), "s"(rw4), "s"(bid * 4) : "memory")
    PRIO(P_FL);
    FLAGS(0, (CLR < 2));
    am += 18 * KVS; amc += 18 * KVS;
    PIN(am); PIN(amc);
    PRIO(P_W);
    TS(8);
    GLDN(128, 24, WS_KV + 18 * 2048);
    {
      unsigned so = WS_O + (tb * 64 + hg * 4) * 1024;
      asm volatile(REPV("j_", 5, REP(4, "buffer_store_dwordx4 v[240:243], %[va], %[rs], %[so] offen offset:i_*1024 sc1\n\t")
                        ".if j_==3\n\ts_mov_b32 %[so], %[sh]\n\t.elseif j_==1\n\ts_add_u32 %[so], %[so], 0x1f8000\n\t.else\n\ts_add_u32 %[so], %[so], 0x8000\n\t.endif\n\t")
                   "s_waitcnt vmcnt(0)"
                   : [so] "+s"(so) : [va] "v"(l16), [rs] "s"(rw4), [sh] "s"(sh) : "memory", "scc");
    }
    FLAG2();
    TS(6);
    wait_min<C_PG, 3, SL1>(vz, 65536u);
    asm volatile("buffer_load_dwordx4 v[244:247], %0, %1, 0 offen offset:1024 sc1" :: "v"(l16), "s"(rw4) : "memory");
    VMW(1);
    PRIO(P_F);
    TS(9);
    KVPUT(128, 4, 24, am, amc);
    POST(C_KV, vz, v4);
    TS(13);
    FLAGS(1024, 0);
    if (bid == 0) asm volatile("s_mov_b64 exec, 1\n\tbuffer_store_dword %0, off, %1, 0 offset:2048 sc1\n\ts_mov_b64 exec, -1" :: "v"(vT), "s"(rw4) : "memory");
    asm volatile("buffer_load_dword v244, %0, %1, %2 offen sc1\n\ts_waitcnt lgkmcnt(0)" :: "v"(l4), "s"(rw4), "s"(WS_G + t0 * 4) : "memory");
#pragma nounroll
    for (unsigned b = 1; b < 3; ++b) {
      GLDN(128, 24, WS_KV + (18 + 12 * b) * 2048);
      am += 12 * KVS; amc += 12 * KVS;
      PIN(am); PIN(amc);
      asm volatile("s_waitcnt vmcnt(24)\n\tv_sub_f32 v245, 1.0, v244\n\tds_write2_b32 %0, v244, v245 offset0:32 offset1:96" :: "v"(l4) : "memory");
      PRIO(P_W);
      if (b == 1) wait_min<C_PH, 3, SL1>(vz, 81920u);
      else
      wait_min<C_PG, 3, SL1>(vz, 81920u + b * 16384u);
      VMW(0);
      PRIO(P_F);
      KVPUTS(128, 4, 24, am, amc);
      POST(C_KV, vz, v4);
      asm volatile("s_waitcnt lgkmcnt(0)" ::: "memory");
    }
    POST(C_KV, vz, v1);
    TS(10);
    if (tb) asm volatile(REP(4, "buffer_load_dwordx4 v[208+4*i_:208+4*i_+3], %0, %1, %2 offen offset:i_*1024 sc1\n\t") :: "v"(l16), "s"(rw4), "s"(sh - 32 * 1024) : "memory");
    PRIO(P_W);
    wait_ge<C_PL, 1>(vz, 3);
    TS(12);
#if !(LAN & 2)
    PRIO(P_LA0);
#endif
    if (tb) {
      unsigned spin = 200000u;
      asm volatile("1:\n\ts_waitcnt vmcnt(0)\n\t" FULLT("208", "v244") "s_cbranch_vccz 2f\n\t"
                   REP(4, "buffer_load_dwordx4 v[208+4*i_:208+4*i_+3], %[va], %[rs], %[so] offen offset:i_*1024 sc1\n\t")
                   "s_sub_u32 %[sp], %[sp], 1\n\ts_cbranch_scc0 1b\n\t2:\n\t"
                   REP(4, "ds_write_b128 %[ah], v[208+4*i_:208+4*i_+3] offset:i_*7168\n\t")
                   : [sp] "+s"(spin) : [va] "v"(l16), [rs] "s"(rw4), [so] "s"(sh - 32 * 1024), [ah] "v"(ah) : "memory", "scc", "vcc");
    } else {
      asm volatile(REP(4, "ds_write_b128 %0, v[224:227] offset:i_*7168\n\t") :: "v"(ah) : "memory");
    }
    asm volatile("s_waitcnt lgkmcnt(0)" ::: "memory");
  } else {
#define GRD(f, wo, xr, xo) \
      "ds_read_b64_tr_b16 v[" f ":" f "+1], %[w0] offset:" wo "\n\tds_read_b64_tr_b16 v[" f "+2:" f "+3], %[w0] offset:" wo "+512\n\t" \
      "ds_read_b128 v[" f "+8:" f "+11], " xr " offset:" xo "\n\t" \
      "ds_read_b64_tr_b16 v[" f "+4:" f "+5], %[w1] offset:" wo "\n\tds_read_b64_tr_b16 v[" f "+6:" f "+7], %[w1] offset:" wo "+512\n\t" \
      "ds_read_b128 v[" f "+12:" f "+15], " xr " offset:" xo "+4096\n\t"
#define GM1(a, f, i, j, c) "v_mfma_f32_32x32x16_bf16 v[" a "], v[" f "+" i ":" f "+" i "+3], v[" f "+" j ":" f "+" j "+3], " c "\n\t"
#define GMM(f) GM1("128:143", f, "0", "8", "v[128:143]") GM1("160:175", f, "4", "8", "v[160:175]") GM1("144:159", f, "0", "12", "v[144:159]") GM1("176:191", f, "4", "12", "v[176:191]")
#define GST(q, xb, xa, xo, lab) GRD("208", q "*4096+2048", xb, "(" q ">>1)*8192") "s_waitcnt lgkmcnt(6)\n\t" GMM("192") lab \
      "s_waitcnt vmcnt(8)\n\t" GRD("192", "((" q "+1)&3)*4096", xa, xo) "s_waitcnt lgkmcnt(6)\n\t" GDMA(q) GMM("208") \
      ".if " q "==1 && " STR(HPG) "\n\tds_write_b32 %[pg], %[x0] offset:" STR(C_PH) "-" STR(C_PG) "\n\t.endif\n\t"
    unsigned cnt = 4, tmp;
    KVPUT(128, 8, 8, am, amc);
    POST(C_KV, vz, v1);
    TS(6);
    asm volatile(REP(16, ".if (i_&3)==0\n\ts_waitcnt vmcnt(12-i_)\n\t.endif\n\tds_write_b128 %0, v[192+4*i_:195+4*i_] offset:i_*1024\n\t") :: "v"(arw) : "memory");
    TS(8);
    asm volatile(
      "s_waitcnt vmcnt(12)\n\t"
      TSA(9)
      GRD("192", "0", "%[x0]", "0") GRD("208", "2048", "%[x1]", "0")
      "s_waitcnt lgkmcnt(6)\n\t"
      GM1("128:143", "192", "0", "8", "0") GM1("160:175", "192", "4", "8", "0") GM1("144:159", "192", "0", "12", "0") GM1("176:191", "192", "4", "12", "0")
      "s_branch 2f\n\t"
      "1:\n\t"
      GST("0", "%[x1]", "%[x2]", "0", "2:\n\t") GST("1", "%[x3]", "%[x0]", "8192", "") GST("2", "%[x1]", "%[x2]", "8192", "") GST("3", "%[x3]", "%[x0]", "16384", "")
      "s_setprio " STR(P_GA) "\n\t"
      "v_add_u32 %[x0], 0x4000, %[x0]\n\tv_add_u32 %[x1], 0x4000, %[x1]\n\tv_add_u32 %[x2], 0x4000, %[x2]\n\tv_add_u32 %[x3], 0x4000, %[x3]\n\t"
      "s_setprio 0\n\t"
      "s_sub_u32 %[cnt], %[cnt], 1\n\t"
      "ds_write_b32 %[pg], %[x0]\n\t"
      "s_cmp_gt_u32 %[cnt], 1\n\t"
      "s_cselect_b32 s90, s90, 0\n\t"
      "s_cmp_lg_u32 %[cnt], 0\n\t"
      "s_cbranch_scc1 1b\n\t"
      "ds_read_b32 v192, %[z] offset:" STR(C_KV) "\n\t"
      "s_waitcnt vmcnt(0) lgkmcnt(0)\n\t"
      "ds_write_b32 %[pg], %[ap]\n\t"
      TSA(10)
      "s_nop 7\n\t"
      "s_setprio " STR(P_PK) "\n\t"
      REP(32, "v_cvt_pk_bf16_f32 v[128+(i_>>2)*8+(i_&3)], v[128+2*i_], v[128+2*i_+1]\n\t")
      REP(8, "v_mov_b64 v[224+2*i_:225+2*i_], 0\n\t")
      "4:\n\tv_readfirstlane_b32 %[tmp], v192\n\ts_cmp_ge_u32 %[tmp], 8\n\ts_cbranch_scc1 5f\n\t"
      "s_sleep 2\n\tds_read_b32 v192, %[z] offset:" STR(C_KV) "\n\ts_waitcnt lgkmcnt(0)\n\ts_add_u32 %[cnt], %[cnt], 1024\n\ts_cbranch_scc0 4b\n\t5:\n\t"
      "s_setprio " STR(P_PL) "\n\t"
      "s_cmp_eq_u32 %[rb], " STR(L_RING) "\n\ts_cbranch_scc1 3f\n\t"
      "s_mov_b64 exec, %[up]\n\t"
      REP(4, "buffer_store_dwordx4 v[144+(i_>>1)*32+(i_&1)*8:144+(i_>>1)*32+(i_&1)*8+3], %[ap], %[rs], %[sh] offen offset:i_*1024 sc1\n\t")
      "s_mov_b64 exec, -1\n\t3:\n\t"
      : [so] "+s"(sa), [cnt] "+s"(cnt), [tmp] "=&s"(tmp), [x0] "+v"(ax0), [x1] "+v"(ax1), [x2] "+v"(ax2), [x3] "+v"(ax3) TSOP
      : [rb] "s"(rb), [a0] "v"(va0), [a1] "v"(va1), [a2] "v"(va2), [a3] "v"(va3), [w0] "v"(aW0), [w1] "v"(aW1), [pg] "v"(apg), [ap] "v"(ap),
        [z] "v"(vz), [up] "s"(0xffff0000ffff0000ull), [rs] "s"(rw4), [sh] "s"(sh - L_PL - w * 2048 + 3072)
      : "memory", "scc");
    wait_min<C_PG, 3, SL3>(vz, 131072u);
    asm volatile(REP(8, "ds_write_b128 %0, v[128+8*i_:128+8*i_+3] offset:((i_>>2)*2+(i_&1))*7168+((i_>>1)&1)*1024\n\t") :: "v"(ap) : "memory");
    POST(C_PL, vz, v1);
    if (tmp < 12) wait_ge<C_KV, 1>(vz, 12);
    wait_ge<C_PL, SL3>(vz, 3);
#if !(LAN & 1)
    PRIO(P_LA);
#endif
    am += (49 - w) * KVS; amc += (49 - w) * KVS;
    PIN(am); PIN(amc);
  }
  TS(11);
  GLDN(128, 24, WS_WF + nt * 32768);
  {
    u32x16 c0, c1, c2, c3; u32x4 c4;
    unsigned so = __builtin_amdgcn_readfirstlane(WS_O + ((tb * 2 + (w >> 1)) * 32 + hg * 4) * 1024 + (w & 1) * 256), sw = WS_WF + nt * 32768 + 24576, gt;
#define MM16(d, a, b, c) "v_mfma_f32_16x16x32_bf16 v[" d "+4*i_:" d "+4*i_+3], " a ", v[" b "+4*i_:" b "+4*i_+3], " c "\n\t"
#define BC(i) "v[56+(" i "&2):57+(" i "&2)]"
    asm volatile(
      "s_mov_b32 s92, 0x3eb8aa3b\n\t"
      "s_mov_b64 exec, %[ev]\n\t" REP(4, "ds_read_b64 v[224+4*i_:224+4*i_+1], %[aq] offset:i_*7168\n\t")
      "s_not_b64 exec, exec\n\t" REP(4, "ds_read_b64 v[224+4*i_+2:224+4*i_+3], %[aq] offset:i_*7168\n\t")
      "s_mov_b64 exec, -1\n\t"
      REP(8, "ds_read_b64 v[64+2*i_:64+2*i_+1], %[ak] offset:(i_>>1)*7168+(i_&1)*32\n\t")
      REP(4, "s_waitcnt lgkmcnt(6-2*i_)\n\t" MM16("96", "v[64+4*i_:64+4*i_+3]", "224", "%[mk]"))
      REP(8, "ds_read_b64_tr_b16 v[80+2*i_:80+2*i_+1], %[av] offset:(i_>>1)*7168+(i_&1)*32\n\t")
      "ds_read_b32 %[gt], %[ag]\n\t"
      "s_setprio " STR(P_LA) "\n\t"
      REP(4, "v_max3_f32 v[56+i_], v[96+4*i_], v[97+4*i_], v[98+4*i_]\n\t")
      REP(4, "v_max_f32 v[56+i_], v[56+i_], v[99+4*i_]\n\t")
      "v_permlane32_swap_b32 v56, v57\n\ts_nop 0\n\tv_permlane32_swap_b32 v58, v59\n\t"
      "v_max_f32 v56, v56, v57\n\tv_max_f32 v58, v58, v59\n\ts_nop 1\n\t"
      "v_permlane16_swap_b32 v56, v58\n\tv_max_f32 v56, v56, v58\n\t"
      "v_mul_f32 v57, 0xbeb8aa3b, v56\n\tv_mul_f32 v59, 0xbeb8aa3b, v56\n\ts_nop 1\n\t"
      "v_permlane16_swap_b32 v57, v59\n\tv_mov_b32 v56, v57\n\tv_mov_b32 v58, v59\n\ts_nop 0\n\t"
      "v_permlane32_swap_b32 v56, v57\n\tv_permlane32_swap_b32 v58, v59\n\t"
      REP(8, ".if (i_&2)==0\n\tv_pk_fma_f32 v[96+2*i_:97+2*i_], v[96+2*i_:97+2*i_], s[92:93], " BC("(i_>>1)") " op_sel:[0,0,0] op_sel_hi:[1,0,0]\n\t"
             ".else\n\tv_pk_fma_f32 v[96+2*i_:97+2*i_], v[96+2*i_:97+2*i_], s[92:93], " BC("(i_>>1)") " op_sel:[0,0,1] op_sel_hi:[1,0,1]\n\t.endif\n\t")
      REP(16, "v_exp_f32 v[96+i_], v[96+i_]\n\t")
      "s_mov_b64 exec, %[ev]\n\t" REP(8, "v_cvt_pk_bf16_f32 v[224+(i_>>1)*4+(i_&1)], v[96+2*i_], v[97+2*i_]\n\t")
      "s_not_b64 exec, exec\n\t" REP(8, "v_cvt_pk_bf16_f32 v[226+(i_>>1)*4+(i_&1)], v[96+2*i_], v[97+2*i_]\n\t")
      "s_mov_b64 exec, -1\n\t"
      REP(4, MM16("112", "%[one]", "224", "0"))
      "s_waitcnt lgkmcnt(0)\n\t"
      REP(4, MM16("64", "v[80+4*i_:80+4*i_+3]", "224", "0"))
      REP(4, "v_rcp_f32 v[56+i_], v[112+4*i_]\n\t")
      REP(4, "v_mul_f32 v[56+i_], v[56+i_], %[gt]\n\t")
      REP(8, ".if (i_&2)==0\n\tv_pk_mul_f32 v[64+2*i_:65+2*i_], v[64+2*i_:65+2*i_], " BC("(i_>>1)") " op_sel:[0,0] op_sel_hi:[1,0]\n\t"
             ".else\n\tv_pk_mul_f32 v[64+2*i_:65+2*i_], v[64+2*i_:65+2*i_], " BC("(i_>>1)") " op_sel:[0,1] op_sel_hi:[1,1]\n\t.endif\n\t")
      REP(8, "v_cvt_pk_bf16_f32 v[80+i_], v[64+2*i_], v[65+2*i_]\n\t")
      REP(4, "buffer_store_dwordx2 v[80+2*i_:80+2*i_+1], %[vo], %[rs], %[so] offen offset:i_*1024 sc1\n\t")
      GLDT("224", 8, "%[va]", "%[rs]", "%[sw]")
      : "=&{v[64:79]}"(c0), "=&{v[80:95]}"(c1), "=&{v[96:111]}"(c2), "=&{v[112:127]}"(c3), "=&{v[56:59]}"(c4), [gt] "=&v"(gt), [sw] "+s"(sw)
      : [so] "s"(so), [ev] "s"(0x5555555555555555ull), [aq] "v"(laq), [ak] "v"(lak), [av] "v"(lav), [ag] "v"(lag), [mk] "v"(mk), [one] "v"(cst4), [vo] "v"(lvo), [rs] "s"(rw4), [va] "v"(l16)
      : "memory", "scc");
  }
  unsigned gaw, gar, rdy, bit, bi2, wsa, vout;
  asm volatile("v_lshrrev_b32 %0, 4, %7\n\tv_lshl_or_b32 %6, %7, 6, %0\n\tv_and_b32 %6, 0x7c20, %6\n\t"
               "v_add_u32 %0, %8, %7\n\tv_add_u32 %1, " STR(L_GA) ", %7\n\tv_mov_b32 %2, " STR(C_RDY) "\n\tv_mov_b32 %3, %9\n\tv_lshlrev_b32 %4, 2, %3\n\tv_add_u32 %5, %10, %7"
               : "=&v"(gaw), "=&v"(gar), "=&v"(rdy), "=&v"(bit), "=&v"(bi2), "=&v"(wsa), "=&v"(vout)
               : "v"(l16), "s"(L_GA + w * 4096), "s"(1u << ((w >> 1) + (w & 1) * 8)), "s"(L_WS + w * 16384) : "memory");
  PRIO(P_TA);
  TS(14);
  if (w == 0) POST(C_PL, vz, v1);
  else {
    u32x16 c0, c1;
    unsigned so = WS_KV + (51 + 3 * w) * 2048;
    asm volatile(GLDT("64", 8, "%[va]", "%[rs]", "%[so]") "s_waitcnt vmcnt(0)\n\t"
      REP(8, "ds_write_b128 %[a], v[64+4*i_:64+4*i_+3] offset:(i_>>1)*2080+(i_&1)*1024\n\t.if (i_&1) && " STR(KS4) "\n\ts_sleep " STR(KS4) "\n\t.endif\n\t")
      "s_mov_b64 exec, 255\n\tds_write_b128 %[c], %[k]\n\ts_mov_b64 exec, -1"
      : "=&{v[64:79]}"(c0), "=&{v[80:95]}"(c1), [so] "+s"(so) : [va] "v"(l16), [rs] "s"(rw4), [a] "v"(am), [c] "v"(amc), [k] "v"(cst4) : "memory", "scc");
    POST(C_KV, vz, v1);
  }
  TS(12);
  {
#define LPWAIT(t, sl) "1:\n\tds_read_b128 v[56:59], %[ar] offset:" STR(C_LP) "-" STR(C_RDY) "\n\ts_waitcnt lgkmcnt(0)\n\tv_min3_u32 v56, v56, v57, v58\n\tv_min_u32 v56, v56, v59\n\t" \
      "s_nop 0\n\tv_readfirstlane_b32 %[so], v56\n\ts_cmp_ge_u32 %[so], " STR(L_KV) "+" t "*" STR(KVS) "\n\ts_cbranch_scc1 2f\n\t" \
      "s_sleep " sl "\n\ts_sub_u32 %[spin], %[spin], 1\n\ts_cbranch_scc0 1b\n\t2:\n\t"
    u32x16 c0, c1, c2, c3, c4, acc, acd;
    unsigned so, spin = 4000000u, pend = 3u;
#define GB(g) "((" g ")>>1)+((" g ")&1)*8"
    asm volatile(
      "s_mov_b64 s[88:89], %[out]\n\ts_mov_b32 s90, 0x200000\n\ts_mov_b32 s91, 0x20000\n\t"
      "s_add_u32 %[so], %[wb], 0x4000\n\ts_waitcnt lgkmcnt(0)\n\t" GLDT("64", 16, "%[va]", "%[rs]", "%[so]")
      LPWAIT("48", STR(SWS))
      TSA(15)
      REP(16, ".if (i_&3)==0\n\ts_waitcnt vmcnt(12-i_)\n\t.endif\n\tds_write_b128 %[ws], v[64+4*i_:64+4*i_+3] offset:i_*1024\n\t")
      "s_waitcnt lgkmcnt(0)\n\t"
      TSA(13)
      "s_mov_b32 %[so], %[wb]\n\t" GLDT("48", 12, "%[va]", "%[rs]", "%[so]") GLDT("112", 4, "%[va]", "%[rs]", "%[so]")
      "1:\n\tds_read_b32 v96, %[z] offset:" STR(C_PL) "\n\ts_waitcnt lgkmcnt(0)\n\tv_readfirstlane_b32 %[so], v96\n\ts_cmp_ge_u32 %[so], %[pl]\n\ts_cbranch_scc1 2f\n\t"
      "s_sleep " STR(SL2) "\n\ts_sub_u32 %[spin], %[spin], 1\n\ts_cbranch_scc0 1b\n\t2:\n\t"
      "s_mov_b32 %[spin], 400000\n\t"
      TSA(9)
      "11:\n\t"
      "s_bitcmp1_b32 %[pend], 0\n\ts_cbranch_scc0 1f\n\t"
      "s_mov_b32 %[so], %[sb]\n\t" GLDT("16", 4, "%[va]", "%[rs]", "%[so]")
      "1:\n\t"
      "s_bitcmp1_b32 %[pend], 1\n\ts_cbranch_scc0 1f\n\t"
      "s_add_u32 %[so], %[sb], 0x4000\n\t" GLDT("32", 4, "%[va]", "%[rs]", "%[so]")
      "1:\n\t"
      "s_waitcnt vmcnt(0)\n\ts_setprio " STR(P_CK) "\n\t"
      TCHK("0", "16", "0", "%[aw]", "%[ar]", "%[bit]", "v96")
      TCHK("1", "32", "16384", "%[aw]", "%[ar]", "%[bi2]", "v96")
      "s_setprio " STR(P_TA) "\n\t"
      "s_waitcnt lgkmcnt(0)\n\t"
      "s_cmp_eq_u32 %[pend], 0\n\ts_cbranch_scc1 12f\n\t"
      "s_sleep " STR(ASLP) "\n\ts_sub_u32 %[spin], %[spin], 1\n\ts_cbranch_scc0 11b\n\t"
      "12:\n\t"
      TSA(21)
      "s_mov_b32 %[spin], 4000000\n\t"
      "1:\n\tds_read_b32 v96, %[ar]\n\ts_waitcnt lgkmcnt(0)\n\tv_readfirstlane_b32 %[so], v96\n\ts_cmp_eq_u32 %[so], 0xf0f\n\ts_cbranch_scc1 2f\n\t"
      "s_sleep 1\n\ts_sub_u32 %[spin], %[spin], 1\n\ts_cbranch_scc0 1b\n\t2:\n\t"
      REP(4, "ds_read_b128 v[96+4*i_:96+4*i_+3], %[rd] offset:i_*1024\n\t")
      "s_mov_b32 %[spin], 4000000\n\t"
      "s_barrier\n\t"
      REPV("g_", 8,
        REP(4, "s_waitcnt lgkmcnt((3-i_)*(1+(g_+3)/8)+i_*(1+g_/4-g_/7))\n\t"
               ".if g_+i_==0\n\tv_mfma_f32_32x32x16_bf16 v[16:31], v[128:131], v[96:99], 0\n\t.else\n\t"
               "v_mfma_f32_32x32x16_bf16 v[16:31], v[128+16*g_+4*i_:128+16*g_+4*i_+3], v[96+4*i_:96+4*i_+3], v[16:31]\n\t.endif\n\t"
               ".if g_>=4\n\tds_read_b128 v[128+16*g_+4*i_:128+16*g_+4*i_+3], %[ws] offset:(g_-4)*4096+i_*1024\n\t.endif\n\t"
               ".if g_<7\n\tds_read_b128 v[96+4*i_:96+4*i_+3], %[rd] offset:g_*4096+4096+i_*1024\n\t.endif\n\t"))
      TSA(16)
      "v_add_u32 %[rd], " STR(L_GB) "-" STR(L_GA) ", %[rd]\n\t"
      "s_branch 40f\n\t"
#define BGRP(g, w) "3" #g ":\n\ts_waitcnt lgkmcnt(4)\n\tv_readfirstlane_b32 %[so], v32\n\ts_bitcmp1_b32 %[so], " GB(#g) "\n\ts_cbranch_scc0 4" #g "f\n\t5" #g ":\n\t" \
      REP(4, ".if " #g "<7\n\ts_waitcnt lgkmcnt(3+(i_+3)/4)\n\t.else\n\ts_waitcnt lgkmcnt(3-i_)\n\t.endif\n\t" \
             "v_mfma_f32_32x32x16_bf16 v[16:31], v[" #w "+4*i_:" #w "+4*i_+3], v[96+4*i_:96+4*i_+3], v[16:31]\n\t" \
             ".if " #g "<7\n\t.if i_==0\n\tds_read_b32 v32, %[ar] offset:4\n\t.endif\n\t" \
             "ds_read_b128 v[96+4*i_:96+4*i_+3], %[rd] offset:" #g "*4096+4096+i_*1024\n\t.endif\n\t")
#define BRET(g) "4" #g ":\n\t.if " STR(CSL) "\n\ts_sleep " STR(CSL) "\n\t.endif\n\tds_read_b32 v32, %[ar] offset:4\n\t" \
      REP(4, "ds_read_b128 v[96+4*i_:96+4*i_+3], %[rd] offset:" #g "*4096+i_*1024\n\t") \
      "s_sub_u32 %[spin], %[spin], 1\n\ts_cbranch_scc0 3" #g "b\n\ts_mov_b32 %[spin], 0\n\ts_branch 5" #g "b\n\t"
      BGRP(0, 48) BGRP(1, 64) BGRP(2, 80) BGRP(3, 112) BGRP(4, 192) BGRP(5, 208) BGRP(6, 224) BGRP(7, 240)
      "s_nop 11\n\t"
      REP(4, "v_cvt_pk_bf16_f32 v[48+i_], v[16+2*i_], v[17+2*i_]\n\t")
      "buffer_store_dwordx4 v[48:51], %[vo], s[88:91], %[oo] offen\n\t"
      REP(4, "v_cvt_pk_bf16_f32 v[52+i_], v[24+2*i_], v[25+2*i_]\n\t")
      "buffer_store_dwordx4 v[52:55], %[vo], s[88:91], %[oo] offen offset:16\n\t"
      ENDP
      "s_branch 15f\n\t"
      BRET(0) BRET(1) BRET(2) BRET(3) BRET(4) BRET(5) BRET(6) BRET(7)
      "15:\n\t"
      : "=&{v[16:31]}"(acc), "=&{v[32:47]}"(acd), "=&{v[48:63]}"(c4), "=&{v[64:79]}"(c0), "=&{v[80:95]}"(c1), "=&{v[96:111]}"(c2), "=&{v[112:127]}"(c3),
        [so] "=&s"(so), [spin] "+s"(spin), [pend] "+s"(pend),
        [rd] "+v"(gar) TSOP
      : [ar] "v"(rdy), [aw] "v"(gaw), [sb] "s"(WS_O + (ct * 32 + w * 4) * 1024), [va] "v"(l16), [rs] "s"(rw4), [out] "s"(pout), [vo] "v"(vout), [oo] "s"(ct * 32768 + nt * 64),
        [wb] "s"(WS_WF + (16 + nt) * 32768), [bit] "v"(bit), [bi2] "v"(bi2), [ws] "v"(wsa), [z] "v"(vz), [pl] "s"(PLT ? 4 - (hg >> 2) : 4)
      : "memory", "scc", "vcc");
  }
  TS(26);
  TSDUMP();
}

extern "C" void kernel_launch(void* const* d_in, const int* in_sizes, int n_in,
                              void* d_out, int out_size, void* d_ws, size_t ws_size,
                              hipStream_t stream) {
  (void)hipFuncSetAttribute((const void*)fused, hipFuncAttributeMaxDynamicSharedMemorySize, L_TOT);
  fused<<<256, 512, L_TOT, stream>>>(d_in[0], d_in[4], d_in[5], d_in[6], d_in[2], d_in[7], d_in[8], d_in[1], d_ws, d_out);
}
```
